```python
import torch

def _softmax(x, axis=-1):
    return torch.softmax(x, dim=axis)
def _gelu(x):
    return torch.nn.functional.gelu(x, approximate="tanh")
def _silu(x):
    return torch.nn.functional.silu(x)
def _sigmoid(x):
    return torch.sigmoid(x)
def _layernorm(x, axis=-1, eps=1e-5):
    m = x.mean(dim=axis, keepdim=True)
    v = x.var(dim=axis, keepdim=True, unbiased=False)
    return (x - m) / torch.sqrt(v + eps)
def _scatter_add(tgt, idx, src, axis=0):
    return tgt.clone().index_add_(axis, idx, src)
def _segment_sum(src, seg, n):
    out = torch.zeros((n,) + src.shape[1:], dtype=src.dtype)
    return out.index_add_(0, seg, src)
def _pad(x, pw):
    flat = tuple(v for pair in reversed(pw) for v in pair)
    return torch.nn.functional.pad(x, flat)

kernel_name = 'parallel_sum__linear_kernel_hedgehog__glu_gated_geglu__prompt'

def setup_inputs():

    _w = (lambda a: (lambda t: t.to(torch.bfloat16) if t.is_floating_point() else t)(torch.as_tensor(a)))
    def _A_setup():
        B, S, D, H = 1, 2048, 640, 32
        Dh = D // H
        return {
            "x": _w((lambda *s: torch.randn(*s, dtype=torch.bfloat16) * 0.05)(B, S, D)),
            "wqkv": _w((lambda *s: torch.randn(*s, dtype=torch.bfloat16) * 0.2)(D, 3 * D)),
            "w_phi": _w((lambda *s: torch.randn(*s, dtype=torch.bfloat16) * 0.2)(H, Dh, Dh) * (Dh ** -0.5)),
            "wo": _w((lambda *s: torch.randn(*s, dtype=torch.bfloat16) * 0.05)(D, D)),
        }
    _A_in = _A_setup()

    def _B_setup():
        B, S, Dm, Df = 1, 2048, 640, 1536
        return {
            "x": _w((lambda *s: torch.randn(*s, dtype=torch.bfloat16) * 0.05)(B, S, Dm)),
            "w_gate": _w((lambda *s: torch.randn(*s, dtype=torch.bfloat16) * 0.4)(Dm, Df) * (Dm ** -0.5)),
            "w_up": _w((lambda *s: torch.randn(*s, dtype=torch.bfloat16) * 0.4)(Dm, Df) * (Dm ** -0.5)),
            "w_down": _w((lambda *s: torch.randn(*s, dtype=torch.bfloat16) * 0.4)(Df, Dm) * (Df ** -0.5)),
        }
    _B_in = _B_setup()

    return {
        "A_x": _A_in["x"],
        "A_wqkv": _A_in["wqkv"],
        "A_w_phi": _A_in["w_phi"],
        "A_wo": _A_in["wo"],
        "B_x": _B_in["x"],
        "B_w_gate": _B_in["w_gate"],
        "B_w_up": _B_in["w_up"],
        "B_w_down": _B_in["w_down"],
    }

def reference(A_x, A_wqkv, A_w_phi, A_wo, B_x, B_w_gate, B_w_up, B_w_down):
    def _A_ref(x, wqkv, w_phi, wo):
        B, S, D = x.shape
        H = 32; Dh = D // H
        qkv = (lambda x, *d: x.permute(*d))((x @ wqkv).reshape(B, S, 3, H, Dh), 2, 0, 3, 1, 4)
        q, k, v = qkv[0], qkv[1], qkv[2]
        pq = torch.einsum("bhsd,hdf->bhsf", q, w_phi)
        pk = torch.einsum("bhsd,hdf->bhsf", k, w_phi)
        phi_q = torch.concatenate([_softmax(pq, axis=-1), _softmax(-pq, axis=-1)], axis=-1)
        phi_k = torch.concatenate([_softmax(pk, axis=-1), _softmax(-pk, axis=-1)], axis=-1)
        kv = torch.einsum("bhsf,bhsd->bhfd", phi_k, v)
        z  = torch.einsum("bhsf->bhf", phi_k)
        num = torch.einsum("bhqf,bhfd->bhqd", phi_q, kv)
        den = torch.einsum("bhqf,bhf->bhq", phi_q, z)[..., None] + 1e-6
        o = num / den
        return (lambda x, *d: x.permute(*d))(o, 0, 2, 1, 3).reshape(B, S, D) @ wo

    def _B_ref(x, w_gate, w_up, w_down):
        g = torch.einsum("bsd,df->bsf", x, w_gate)
        u = torch.einsum("bsd,df->bsf", x, w_up)
        h = _gelu(g) * u
        return torch.einsum("bsf,fd->bsd", h, w_down)

    x = A_x
    xn = _layernorm(x, axis=-1)
    return x + _A_ref(xn, A_wqkv, A_w_phi, A_wo) + _B_ref(xn, B_w_gate, B_w_up, B_w_down)
```

```cpp
#include <hip/hip_runtime.h>
#include <hip/hip_bf16.h>
typedef __hip_bfloat16 bf16;
#define S_ 2048
#define D_ 640
#define H_ 32
#define DH 20
#define DF 1536
__device__ __forceinline__ float b2f(bf16 v){return __bfloat162float(v);}
__device__ __forceinline__ float rbf(float v){return __bfloat162float(__float2bfloat16(v));}

__global__ void k_ln(const bf16* x, float* xn){
  int row=blockIdx.x; int t=threadIdx.x; __shared__ float red[64];
  float s=0; for(int i=t;i<D_;i+=64) s+=b2f(x[row*D_+i]);
  for(int o=32;o>0;o>>=1) s+=__shfl_xor(s,o);
  float m=s/D_; float v=0; for(int i=t;i<D_;i+=64){float d=b2f(x[row*D_+i])-m; v+=d*d;}
  for(int o=32;o>0;o>>=1) v+=__shfl_xor(v,o);
  v/=D_; float r=1.0f/sqrtf(v+1e-5f);
  for(int i=t;i<D_;i+=64) xn[row*D_+i]=(b2f(x[row*D_+i])-m)*r;
}
__global__ void k_gemm(const float* A,const bf16* B,float* C,int M,int N,int K){
  int n=blockIdx.x*256+threadIdx.x; int m=blockIdx.y; if(n>=N) return;
  float s=0; for(int k=0;k<K;++k) s+=A[m*K+k]*b2f(B[(long)k*N+n]);
  C[(long)m*N+n]=s;
}
__global__ void k_phi(const float* qkv,const bf16* wphi,float* phi  ){
  int idx=blockIdx.x*256+threadIdx.x; if(idx>=2*S_*H_) return;
  int w=idx/(S_*H_); int r=idx%(S_*H_); int s=r/H_, h=r%H_;
  float p[DH]; const float* q=qkv+(long)s*3*D_+w*D_+h*DH;
  for(int f=0;f<DH;++f){float a=0; for(int d=0;d<DH;++d) a+=q[d]*b2f(wphi[h*DH*DH+d*DH+f]); p[f]=a;}
  float mx=-1e30f,mn=1e30f; for(int f=0;f<DH;++f){mx=fmaxf(mx,p[f]);mn=fminf(mn,p[f]);}
  float s1=0,s2=0; for(int f=0;f<DH;++f){s1+=expf(p[f]-mx); s2+=expf(mn-p[f]);}
  float* o=phi+((long)w*S_*H_+r)*40;
  for(int f=0;f<DH;++f){o[f]=expf(p[f]-mx)/s1; o[20+f]=expf(mn-p[f])/s2;}
}
__global__ void k_kv(const float* qkv,const float* phi,float* kv ){
  int h=blockIdx.x; int t=threadIdx.x; if(t>=840) return; int f=t/21,d=t%21;
  float a=0; for(int s=0;s<S_;++s){float pk=phi[((long)S_*H_+(long)s*H_+h)*40+f]; float v=d<20?qkv[(long)s*3*D_+2*D_+h*DH+d]:1.f; a+=pk*v;}
  kv[h*840+t]=a;
}
__global__ void k_o(const float* phi,const float* kv,float* o){
  int idx=blockIdx.x*256+threadIdx.x; if(idx>=S_*D_) return; int s=idx/D_,c=idx%D_,h=c/DH,d=c%DH;
  const float* pq=phi+((long)s*H_+h)*40; float num=0,den=0;
  for(int f=0;f<40;++f){num+=pq[f]*kv[h*840+f*21+d]; den+=pq[f]*kv[h*840+f*21+20];}
  o[idx]=num/(den+1e-6f);
}
__global__ void k_h(const float* g,const float* u,float* hh){
  int idx=blockIdx.x*256+threadIdx.x; if(idx>=S_*DF) return; float x=g[idx];
  float t=tanhf(0.7978845608028654f*(x+0.044715f*x*x*x)); hh[idx]=0.5f*x*(1+t)*u[idx];
}
__global__ void k_fin(const bf16* x,const float* a,const float* b,bf16* out){
  int idx=blockIdx.x*256+threadIdx.x; if(idx>=S_*D_) return; out[idx]=__float2bfloat16(b2f(x[idx])+a[idx]+b[idx]);
}
extern "C" void kernel_launch(void* const* d_in, const int* in_sizes, int n_in, void* d_out, int out_size, void* d_ws, size_t ws_size, hipStream_t stream) {
  const bf16* x=(const bf16*)d_in[0]; const bf16* wqkv=(const bf16*)d_in[1]; const bf16* wphi=(const bf16*)d_in[2]; const bf16* wo=(const bf16*)d_in[3];
  const bf16* wg=(const bf16*)d_in[5]; const bf16* wu=(const bf16*)d_in[6]; const bf16* wd=(const bf16*)d_in[7];
  float* ws=(float*)d_ws; float* xn=ws; float* qkv=xn+S_*D_; float* phi=qkv+S_*3*D_; float* kv=phi+2*S_*H_*40; float* o=kv+32*840+64;
  float* g=o+S_*D_; float* u=g+S_*DF; float* hh=u+S_*DF; float* a=hh+S_*DF; float* b=a+S_*D_;
  k_ln<<<S_,64,0,stream>>>(x,xn);
  k_gemm<<<dim3((3*D_+255)/256,S_),256,0,stream>>>(xn,wqkv,qkv,S_,3*D_,D_);
  k_gemm<<<dim3((DF+255)/256,S_),256,0,stream>>>(xn,wg,g,S_,DF,D_);
  k_gemm<<<dim3((DF+255)/256,S_),256,0,stream>>>(xn,wu,u,S_,DF,D_);
  k_phi<<<(2*S_*H_+255)/256,256,0,stream>>>(qkv,wphi,phi);
  k_kv<<<H_,1024,0,stream>>>(qkv,phi,kv);
  k_o<<<(S_*D_+255)/256,256,0,stream>>>(phi,kv,o);
  k_h<<<(S_*DF+255)/256,256,0,stream>>>(g,u,hh);
  k_gemm<<<dim3((D_+255)/256,S_),256,0,stream>>>(o,wo,a,S_,D_,D_);
  k_gemm<<<dim3((D_+255)/256,S_),256,0,stream>>>(hh,wd,b,S_,D_,DF);
  k_fin<<<(S_*D_+255)/256,256,0,stream>>>(x,a,b,(bf16*)d_out);
}
```

```cpp
#include <hip/hip_runtime.h>
#include <hip/hip_bf16.h>
#include <stdint.h>

typedef __attribute__((ext_vector_type(8))) __bf16 bf16x8;
typedef __attribute__((ext_vector_type(4))) __bf16 bf16x4;
typedef __attribute__((ext_vector_type(2))) __bf16 bf16x2;
typedef __attribute__((ext_vector_type(4))) short s16x4;
typedef __attribute__((ext_vector_type(8))) short s16x8;
typedef __attribute__((ext_vector_type(4))) float f32x4;
typedef __attribute__((ext_vector_type(2))) float f32x2;
typedef __attribute__((ext_vector_type(4))) unsigned u32x4;
typedef __attribute__((ext_vector_type(2))) unsigned u32x2;
#define LAS __attribute__((address_space(3)))
#define GAS __attribute__((address_space(1)))
#define DI __device__ __forceinline__
typedef LAS char* ldsp;

constexpr int S_ = 2048, D_ = 640, DF_ = 1536, NH = 32;
constexpr int NRG = 12;
constexpr size_t WS_H = 0;
constexpr size_t WS_PQ = WS_H + (size_t)S_ * DF_ * 2;
constexpr size_t WS_PQ2 = WS_PQ + (size_t)NRG * NH * 11 * 1024;
constexpr size_t WS_KV = WS_PQ2 + (size_t)NRG * NH * 11 * 256;
constexpr size_t WS_OM = WS_KV + (size_t)NRG * NH * 3360;
constexpr size_t WS_OR = WS_OM + (size_t)NH * S_ * 32;
constexpr size_t WS_FLAG = WS_OR + (size_t)4 * S_ * 64;

__device__ const unsigned g_ones[320] = {
#define O4 0x3F803F80u, 0x3F803F80u, 0x3F803F80u, 0x3F803F80u
#define O16 O4, O4, O4, O4
#define O64 O16, O16, O16, O16
    O64, O64, O64, O64, O64};

DI u32x4 make_srd(const void* p, unsigned bytes) {
  uint64_t a = (uint64_t)p;
  u32x4 r;
  r[0] = (unsigned)a;
  r[1] = (unsigned)(a >> 32) & 0xffffu;
  r[2] = bytes;
  r[3] = 0x00020000u;
  return r;
}
DI void dma_g(const void* vaddr, unsigned m0v) {
  asm volatile("s_mov_b32 m0, %1\n\ts_nop 0\n\tglobal_load_lds_dwordx4 %0, off" ::"v"(vaddr), "s"(m0v) : "memory");
}
DI void dma_b(u32x4 srd, unsigned voff, unsigned soff, unsigned m0v) {
  asm volatile("s_mov_b32 m0, %3\n\ts_nop 0\n\tbuffer_load_dwordx4 %0, %1, %2 offen lds" ::"v"(voff), "s"(srd), "s"(soff), "s"(m0v) : "memory");
}
DI void dma_b_sc1(u32x4 srd, unsigned voff, unsigned soff, unsigned m0v) {
  asm volatile("s_mov_b32 m0, %3\n\ts_nop 0\n\tbuffer_load_dwordx4 %0, %1, %2 offen sc1 lds" ::"v"(voff), "s"(srd), "s"(soff), "s"(m0v) : "memory");
}
#define WAIT_BAR(N) asm volatile("s_waitcnt vmcnt(" #N ") lgkmcnt(0)\n\ts_barrier" ::: "memory")
#define WAIT_VM(N) asm volatile("s_waitcnt vmcnt(" #N ")" ::: "memory")
#define LGKM_BAR() asm volatile("s_waitcnt lgkmcnt(0)\n\ts_barrier" ::: "memory")
#define LGKM0() asm volatile("s_waitcnt lgkmcnt(0)" ::: "memory")
#define SCHED0() __builtin_amdgcn_sched_barrier(0)

DI bf16x8 ld128(ldsp p) { return *(const LAS bf16x8*)p; }
DI bf16x8 tr2(ldsp p0, ldsp p1) {
  s16x4 a = __builtin_amdgcn_ds_read_tr16_b64_v4i16((LAS s16x4*)p0);
  s16x4 b = __builtin_amdgcn_ds_read_tr16_b64_v4i16((LAS s16x4*)p1);
  return __builtin_bit_cast(bf16x8, __builtin_shufflevector(a, b, 0, 1, 2, 3, 4, 5, 6, 7));
}
DI f32x4 mfma32(bf16x8 a, bf16x8 b, f32x4 c) { return __builtin_amdgcn_mfma_f32_16x16x32_bf16(a, b, c, 0, 0, 0); }
DI unsigned pk2(float a, float b) {
  bf16x2 v;
  v[0] = (__bf16)a;
  v[1] = (__bf16)b;
  return __builtin_bit_cast(unsigned, v);
}
DI float dot2(unsigned a, unsigned b, float c) {
  return __builtin_amdgcn_fdot2_f32_bf16(__builtin_bit_cast(bf16x2, a), __builtin_bit_cast(bf16x2, b), c, false);
}
DI float xg_max(float x) {
  auto r = __builtin_amdgcn_permlane16_swap(__float_as_uint(x), __float_as_uint(x), false, false);
  x = fmaxf(__uint_as_float(r[0]), __uint_as_float(r[1]));
  auto s = __builtin_amdgcn_permlane32_swap(__float_as_uint(x), __float_as_uint(x), false, false);
  return fmaxf(__uint_as_float(s[0]), __uint_as_float(s[1]));
}
DI float xg_min(float x) {
  auto r = __builtin_amdgcn_permlane16_swap(__float_as_uint(x), __float_as_uint(x), false, false);
  x = fminf(__uint_as_float(r[0]), __uint_as_float(r[1]));
  auto s = __builtin_amdgcn_permlane32_swap(__float_as_uint(x), __float_as_uint(x), false, false);
  return fminf(__uint_as_float(s[0]), __uint_as_float(s[1]));
}
DI float xg_sum(float x) {
  auto r = __builtin_amdgcn_permlane16_swap(__float_as_uint(x), __float_as_uint(x), false, false);
  x = __uint_as_float(r[0]) + __uint_as_float(r[1]);
  auto s = __builtin_amdgcn_permlane32_swap(__float_as_uint(x), __float_as_uint(x), false, false);
  return __uint_as_float(s[0]) + __uint_as_float(s[1]);
}
#define STAMPAT(i)
constexpr size_t WS_DBG = 64u<<20;
DI int rs_of(int rg) { return (512 * rg) / 3; }

constexpr int A_SLOT = 22528, B_SLOT = 16384;
constexpr int L_A = 0;
constexpr int L_B = 3 * A_SLOT;
constexpr int L_MISC = L_B + 5 * B_SLOT;
constexpr int L_DUMMY = L_MISC;
constexpr int L_WPHI = L_MISC + 1024;
constexpr int L_STAT = L_WPHI + 4096;
constexpr int L_CS = L_STAT + 1536;
constexpr int K1_LDS = L_CS + 1024;

DI void featmap(const float (&v)[5], float (&pos)[5], float (&neg)[5]) {
  float mx = fmaxf(fmaxf(fmaxf(v[0], v[1]), fmaxf(v[2], v[3])), v[4]);
  float mn = fminf(fminf(fminf(v[0], v[1]), fminf(v[2], v[3])), v[4]);
  mx = xg_max(mx);
  mn = xg_min(mn);
  const float L2E = 1.4426950408889634f;
  float sp = 0.f, sn = 0.f;
#pragma unroll
  for (int i = 0; i < 5; ++i) {
    pos[i] = __builtin_amdgcn_exp2f((v[i] - mx) * L2E);
    neg[i] = __builtin_amdgcn_exp2f((mn - v[i]) * L2E);
    sp += pos[i];
    sn += neg[i];
  }
  sp = __builtin_amdgcn_rcpf(xg_sum(sp));
  sn = __builtin_amdgcn_rcpf(xg_sum(sn));
#pragma unroll
  for (int i = 0; i < 5; ++i) {
    pos[i] *= sp;
    neg[i] *= sn;
  }
}

template <int MT, bool GU>
DI void k1_body(ldsp sm, const char* x, const char* wqkv, const char* wphi, const char* wg, const char* wu, char* ws,
                int rg, int ct, int w, int lane) {
  const int mh = (MT == 6) ? 0 : 1;
  const int wn = w & 3;
  const int r0 = rs_of(rg), nrow = rs_of(rg + 1) - r0;
  const unsigned smb = (unsigned)(size_t)sm;
  const int g = lane >> 4, fr = lane & 15;

  constexpr int KID=0;
  STAMPAT(0);
  const char* aptr[3];
  unsigned adst[3];
#pragma unroll
  for (int c = 0; c < 3; ++c) {
    int i = w + 8 * c;
    int slot = 8 * i + (lane >> 3);
    int ch = (lane & 7) ^ ((slot >> 1) & 7);
    int row = r0 + (slot < nrow ? slot : nrow - 1);
    const char* p = x + (size_t)row * 1280 + ch * 16;
    if (slot == 175) p = (const char*)g_ones + ch * 16;
    if (i >= 22) p = x + (lane & 7) * 16;
    aptr[c] = p;
    adst[c] = (i >= 22) ? (unsigned)(L_DUMMY) : (unsigned)(L_A + i * 1024);
  }
  const bool adummy = (w >= 6);
  u32x4 srd0, srd1;
  unsigned voff0, voff1, sstep;
  if (GU) {
    srd0 = make_srd(wg, D_ * DF_ * 2);
    srd1 = make_srd(wu, D_ * DF_ * 2);
    int krow = 4 * w + (lane >> 4);
    int chp = lane & 15;
    int f = (krow & 3) | (((krow >> 3) & 1) << 2);
    int ch = (((chp >> 1) ^ f) << 1) | (chp & 1);
    voff0 = voff1 = krow * 3072 + ct * 256 + ch * 16;
    sstep = 32 * 3072;
  } else {
    srd0 = srd1 = make_srd(wqkv, D_ * 3 * D_ * 2);
    unsigned vo[2];
#pragma unroll
    for (int c = 0; c < 2; ++c) {
      int cidx = (w + 8 * c) * 64 + lane;
      int rho = cidx / 30, cc = cidx % 30;
      int seg = cc / 10, ch = cc % 10;
      int k = (rho & 16) | ((rho & 4) << 1) | ((rho & 8) >> 1) | (rho & 3);
      vo[c] = (cidx < 960) ? (unsigned)(k * 3840 + seg * 1280 + ct * 160 + ch * 16) : (unsigned)((lane & 7) * 16);
    }
    voff0 = vo[0];
    voff1 = vo[1];
    sstep = 32 * 3840;
  }
  const unsigned bdst0 = smb + L_B + w * 1024, bdst1 = bdst0 + 8192;

  auto issueA = [&](int slotoff) {
#pragma unroll
    for (int c = 0; c < 3; ++c) {
      unsigned d = smb + adst[c] + ((c == 2 && adummy) ? 0 : slotoff);
      dma_g(aptr[c], d);
      aptr[c] += 128;
    }
  };
  unsigned bsoff = 0;
  auto issueB = [&](int slotoff) {
    dma_b(srd0, voff0, bsoff, bdst0 + slotoff);
    dma_b(srd1, voff1, bsoff, bdst1 + slotoff);
    bsoff += sstep;
  };

  if (!GU) {
    int head = ct * 4 + wn;
    const char* p = wphi + head * 800 + (lane < 50 ? lane : 49) * 16;
    dma_g(p, smb + L_WPHI + wn * 1024);
  }
  issueA(0);
  issueB(0);
  issueB(B_SLOT);
  issueA(A_SLOT);
  issueB(2 * B_SLOT);
  issueB(3 * B_SLOT);
  issueA(2 * A_SLOT);
  issueB(4 * B_SLOT);

  const int fa = (fr >> 1) & 7;
  const int aoff0 = L_A + mh * 6 * 2048 + fr * 128 + ((g ^ fa) * 16);
  const int aoff1 = aoff0 ^ 64;
  int boff[4];
  constexpr int TSTEP = GU ? 1024 : 3840;
  {
    const int q = fr >> 2, p = lane & 3;
    if (GU) {
      int f = q | ((g & 1) << 2);
#pragma unroll
      for (int j = 0; j < 4; ++j)
        boff[j] = L_B + (j >> 1) * 8192 + (8 * g + q) * 256 + (((2 * wn + (j & 1)) ^ f) * 32) + p * 8;
    } else {
      int rho = 16 * (g >> 1) + 4 * (g & 1) + q;
#pragma unroll
      for (int j = 0; j < 3; ++j) boff[j] = L_B + rho * 480 + (20 * j + 5 * wn + p) * 8;
      boff[3] = L_B + rho * 480 + (20 * (p == 3 ? 0 : p) + 5 * wn + 4) * 8;
    }
  }
  const int soff = L_A + w * 1024 + lane * 16;

  f32x4 acc[MT][4];
#pragma unroll
  for (int m = 0; m < MT; ++m)
#pragma unroll
    for (int j = 0; j < 4; ++j) acc[m][j] = f32x4{0.f, 0.f, 0.f, 0.f};
  float s1[3] = {0.f, 0.f, 0.f}, s2[3] = {0.f, 0.f, 0.f};
  bf16x8 fa0[MT], fb0[4], fa1[MT], fb1[4];

  STAMPAT(1);
  WAIT_BAR(14);
  STAMPAT(2);
#pragma unroll
  for (int m = 0; m < MT; ++m) fa0[m] = ld128(sm + aoff0 + m * 2048);
#pragma unroll
  for (int j = 0; j < 4; ++j) fb0[j] = tr2(sm + boff[j], sm + boff[j] + TSTEP);

  int a_rd = 0;
  int b_rd = 0;
#pragma unroll 1
  for (int t = 0; t < 10; ++t) {
    SCHED0();
    if (t < 8) { WAIT_BAR(12); } else if (t == 8) { WAIT_BAR(7); } else { WAIT_BAR(0); }
    if (t < 8) issueB(b_rd);
    int b_nx = b_rd + B_SLOT; if (b_nx == 5 * B_SLOT) b_nx = 0;
    {
      ldsp pa = sm + a_rd + aoff1;
#pragma unroll
      for (int m = 0; m < MT; ++m) fa1[m] = ld128(pa + m * 2048);
#pragma unroll
      for (int j = 0; j < 4; ++j) fb1[j] = tr2(sm + b_nx + boff[j], sm + b_nx + boff[j] + TSTEP);
      u32x4 st[3];
#pragma unroll
      for (int c = 0; c < 3; ++c) st[c] = *(const LAS u32x4*)(sm + a_rd + soff + c * 8192);
#pragma unroll
      for (int m = 0; m < MT; ++m)
#pragma unroll
        for (int j = 0; j < 4; ++j) acc[m][j] = mfma32(fb0[j], fa0[m], acc[m][j]);
#pragma unroll
      for (int c = 0; c < 3; ++c)
#pragma unroll
        for (int e = 0; e < 4; ++e) {
          s1[c] = dot2(st[c][e], 0x3F803F80u, s1[c]);
          s2[c] = dot2(st[c][e], st[c][e], s2[c]);
        }
    }
    b_rd = b_nx;
    SCHED0();
    if (t < 8) { WAIT_BAR(9); } else if (t == 8) { WAIT_BAR(2); } else { WAIT_BAR(0); }
    if (t < 7) { issueB(b_rd); issueA(a_rd); }
    b_nx = b_rd + B_SLOT; if (b_nx == 5 * B_SLOT) b_nx = 0;
    int a_nx = a_rd + A_SLOT; if (a_nx == 3 * A_SLOT) a_nx = 0;
    {
      ldsp pa = sm + a_nx + aoff0;
#pragma unroll
      for (int m = 0; m < MT; ++m) fa0[m] = ld128(pa + m * 2048);
#pragma unroll
      for (int j = 0; j < 4; ++j) fb0[j] = tr2(sm + b_nx + boff[j], sm + b_nx + boff[j] + TSTEP);
#pragma unroll
      for (int m = 0; m < MT; ++m)
#pragma unroll
        for (int j = 0; j < 4; ++j) acc[m][j] = mfma32(fb1[j], fa1[m], acc[m][j]);
    }
    b_rd = b_nx;
    a_rd = a_nx;
  }
  SCHED0();
  STAMPAT(3);

#pragma unroll
  for (int c = 0; c < 3; ++c) {
    float a = s1[c], b = s2[c];
    a += __shfl_xor(a, 1); b += __shfl_xor(b, 1);
    a += __shfl_xor(a, 2); b += __shfl_xor(b, 2);
    a += __shfl_xor(a, 4); b += __shfl_xor(b, 4);
    float mean = a * (1.0f / 640.0f);
    float var = fmaxf(b * (1.0f / 640.0f) - mean * mean, 0.f);
    float rstd = 1.0f / sqrtf(var + 1e-5f);
    int slot = 8 * (w + 8 * c) + (lane >> 3);
    if ((lane & 7) == 0) *(LAS f32x2*)(sm + L_STAT + slot * 8) = f32x2{mean * rstd, rstd};
  }
  if (mh == 1 && fr == 15) {
#pragma unroll
    for (int j = 0; j < 4; ++j) *(LAS f32x4*)(sm + L_CS + wn * 256 + j * 64 + g * 16) = acc[MT - 1][j];
  }
  LGKM_BAR();
  f32x4 cs[4];
#pragma unroll
  for (int j = 0; j < 4; ++j) cs[j] = *(const LAS f32x4*)(sm + L_CS + wn * 256 + j * 64 + g * 16);
#pragma unroll
  for (int m = 0; m < MT; ++m) {
    f32x2 st = *(const LAS f32x2*)(sm + L_STAT + ((mh * 6 + m) * 16 + fr) * 8);
#pragma unroll
    for (int j = 0; j < 4; ++j)
#pragma unroll
      for (int r = 0; r < 4; ++r) acc[m][j][r] = acc[m][j][r] * st[1] - st[0] * cs[j][r];
  }

  STAMPAT(4);
  if (GU) {
    const float K0 = -2.0f * 0.7978845608028654f * 1.4426950408889634f;
    const float K1c = K0 * 0.044715f;
#pragma unroll
    for (int m = 0; m < MT; ++m) {
      int slot = (mh * 6 + m) * 16 + fr;
#pragma unroll
      for (int j = 0; j < 2; ++j) {
        float hv[4];
#pragma unroll
        for (int r = 0; r < 4; ++r) {
          float gv = acc[m][j][r], uv = acc[m][j + 2][r];
          float e = __builtin_amdgcn_exp2f(gv * (K0 + K1c * gv * gv));
          hv[r] = gv * uv * __builtin_amdgcn_rcpf(1.0f + e);
        }
        int chunk = 4 * wn + 2 * j + (g >> 1);
        *(LAS u32x2*)(sm + slot * 256 + ((chunk ^ fr) * 16) + (g & 1) * 8) = u32x2{pk2(hv[0], hv[1]), pk2(hv[2], hv[3])};
      }
    }
    LGKM_BAR();
    STAMPAT(5);
    char* hp = ws + WS_H;
#pragma unroll
    for (int i = 0; i < 6; ++i) {
      int r = 4 * (w + 8 * i) + g;
      if (r < nrow) {
        u32x4 v = *(const LAS u32x4*)(sm + r * 256 + ((fr ^ (r & 15)) * 16));
        *(u32x4*)(hp + (size_t)(r0 + r) * 3072 + ct * 256 + fr * 16) = v;
      }
    }
  } else {
    const int head = ct * 4 + wn;
    bf16x8 wq[2], wk[2];
    {
      const LAS unsigned short* wp = (const LAS unsigned short*)(sm + L_WPHI + wn * 1024);
#pragma unroll
      for (int ft = 0; ft < 2; ++ft) {
        int f = (ft == 0) ? (5 * (fr >> 2) + (fr & 3)) : (5 * (fr >> 2) + 4);
        bool valid = (ft == 0) || ((fr & 3) == 0);
        s16x8 a, b;
#pragma unroll
        for (int j = 0; j < 4; ++j) {
          short lo = valid ? (short)wp[(4 * g + j) * 20 + f] : (short)0;
          short hi = valid ? (short)wp[(16 + j) * 20 + f] : (short)0;
          a[j] = lo; b[j] = lo;
          a[4 + j] = (g == 0) ? hi : (short)0;
          b[4 + j] = (g == 1) ? hi : (short)0;
        }
        wq[ft] = __builtin_bit_cast(bf16x8, a);
        wk[ft] = __builtin_bit_cast(bf16x8, b);
      }
    }
    ldsp pk_img = sm + w * 17408;
    ldsp v_img = pk_img + 96 * 104;
    char* pq = ws + WS_PQ + ((size_t)(rg * NH + head) * 11 + mh * 6) * 1024 + lane * 16;
    char* pq2 = ws + WS_PQ2 + ((size_t)(rg * NH + head) * 11 + mh * 6) * 256 + lane * 4;
#pragma unroll
    for (int m = 0; m < 6; ++m) {
      int tok = m * 16 + fr;
      if (m < MT) {
        u32x2 rem = u32x2{pk2(acc[m][3][0], acc[m][3][1]), pk2(acc[m][3][2], acc[m][3][3])};
        u32x4 qb = u32x4{pk2(acc[m][0][0], acc[m][0][1]), pk2(acc[m][0][2], acc[m][0][3]), rem[0], rem[1]};
        u32x4 kb = u32x4{pk2(acc[m][1][0], acc[m][1][1]), pk2(acc[m][1][2], acc[m][1][3]), rem[0], rem[1]};
        f32x4 z4 = f32x4{0.f, 0.f, 0.f, 0.f};
        f32x4 q0 = mfma32(wq[0], __builtin_bit_cast(bf16x8, qb), z4);
        f32x4 q1 = mfma32(wq[1], __builtin_bit_cast(bf16x8, qb), z4);
        f32x4 k0 = mfma32(wk[0], __builtin_bit_cast(bf16x8, kb), z4);
        f32x4 k1 = mfma32(wk[1], __builtin_bit_cast(bf16x8, kb), z4);
        float pos[5], neg[5];
        {
          float v[5] = {q0[0], q0[1], q0[2], q0[3], q1[0]};
          featmap(v, pos, neg);
          *(u32x4*)(pq + m * 1024) = u32x4{pk2(pos[0], pos[1]), pk2(pos[2], pos[3]), pk2(neg[0], neg[1]), pk2(neg[2], neg[3])};
          *(unsigned*)(pq2 + m * 256) = pk2(pos[4], neg[4]);
        }
        {
          float v[5] = {k0[0], k0[1], k0[2], k0[3], k1[0]};
          featmap(v, pos, neg);
          bool live = (mh * 96 + tok) < nrow;
          u32x2 a = u32x2{pk2(pos[0], pos[1]), pk2(pos[2], pos[3])};
          u32x2 b = u32x2{pk2(neg[0], neg[1]), pk2(neg[2], neg[3])};
          u32x2 c = u32x2{pk2(pos[4], neg[4]), 0u};
          if (!live) { a = u32x2{0u, 0u}; b = a; c = a; }
          *(LAS u32x2*)(pk_img + tok * 104 + g * 8) = a;
          *(LAS u32x2*)(pk_img + tok * 104 + 32 + g * 8) = b;
          *(LAS u32x2*)(pk_img + tok * 104 + 64 + g * 8) = c;
        }
        *(LAS u32x2*)(v_img + tok * 72 + g * 8) = u32x2{pk2(acc[m][2][0], acc[m][2][1]), pk2(acc[m][2][2], acc[m][2][3])};
        u32x2 ext = (g == 2) ? rem : ((g == 3) ? u32x2{0x00003F80u, 0u} : u32x2{0u, 0u});
        *(LAS u32x2*)(v_img + tok * 72 + 32 + (g ^ 2) * 8) = ext;
      } else {
        u32x2 z = u32x2{0u, 0u};
        *(LAS u32x2*)(pk_img + tok * 104 + g * 8) = z;
        *(LAS u32x2*)(pk_img + tok * 104 + 32 + g * 8) = z;
        *(LAS u32x2*)(pk_img + tok * 104 + 64 + g * 8) = z;
        *(LAS u32x2*)(v_img + tok * 72 + g * 8) = z;
        *(LAS u32x2*)(v_img + tok * 72 + 32 + g * 8) = z;
      }
    }
    LGKM0();
    STAMPAT(5);
    f32x4 kv[3][2];
#pragma unroll
    for (int ft = 0; ft < 3; ++ft)
#pragma unroll
      for (int dt = 0; dt < 2; ++dt) kv[ft][dt] = f32x4{0.f, 0.f, 0.f, 0.f};
    {
      const int q = fr >> 2, p = lane & 3;
#pragma unroll
      for (int s = 0; s < 3; ++s) {
        int row = 32 * s + 8 * g + q;
        bf16x8 a[3], b[2];
#pragma unroll
        for (int ft = 0; ft < 3; ++ft) a[ft] = tr2(pk_img + row * 104 + ft * 32 + p * 8, pk_img + (row + 4) * 104 + ft * 32 + p * 8);
#pragma unroll
        for (int dt = 0; dt < 2; ++dt) b[dt] = tr2(v_img + row * 72 + dt * 32 + p * 8, v_img + (row + 4) * 72 + dt * 32 + p * 8);
#pragma unroll
        for (int ft = 0; ft < 3; ++ft)
#pragma unroll
          for (int dt = 0; dt < 2; ++dt) kv[ft][dt] = mfma32(a[ft], b[dt], kv[ft][dt]);
      }
    }
    ldsp ex = sm + (4 + wn) * 17408;
    if (mh == 1) {
      LGKM0();
#pragma unroll
      for (int ft = 0; ft < 3; ++ft)
#pragma unroll
        for (int dt = 0; dt < 2; ++dt) *(LAS f32x4*)(ex + (ft * 2 + dt) * 1024 + lane * 16) = kv[ft][dt];
    }
    LGKM_BAR();
    if (mh == 0) {
      char* kp = ws + WS_KV + (size_t)(rg * NH + head) * 3360;
#pragma unroll
      for (int dt = 0; dt < 2; ++dt) {
#pragma unroll
        for (int ft = 0; ft < 3; ++ft) kv[ft][dt] += *(const LAS f32x4*)(ex + (ft * 2 + dt) * 1024 + lane * 16);
        int d = dt * 16 + fr;
        if (d <= 20) {
          float* o = (float*)(kp + (d * 4 + g) * 40);
          *(f32x2*)(o + 0) = f32x2{kv[0][dt][0], kv[0][dt][1]};
          *(f32x2*)(o + 2) = f32x2{kv[0][dt][2], kv[0][dt][3]};
          *(f32x2*)(o + 4) = f32x2{kv[1][dt][0], kv[1][dt][1]};
          *(f32x2*)(o + 6) = f32x2{kv[1][dt][2], kv[1][dt][3]};
          *(f32x2*)(o + 8) = f32x2{kv[2][dt][0], kv[2][dt][1]};
        }
      }
    }
  }
  asm volatile("s_waitcnt vmcnt(0)" ::: "memory");
  STAMPAT(6);
}

__global__ __launch_bounds__(512, 2) void k1(const char* __restrict__ x, const char* __restrict__ wqkv,
                                             const char* __restrict__ wphi, const char* __restrict__ wg,
                                             const char* __restrict__ wu, char* __restrict__ ws) {
  extern __shared__ __attribute__((aligned(16))) char smem[];
  ldsp sm = (ldsp)smem;
  const int b = blockIdx.x, xcd = b & 7, idx = b >> 3;
  if (idx >= 30) {
    if (b == 240 && threadIdx.x < 32) ((unsigned*)(ws + WS_FLAG))[threadIdx.x] = 0u;
    return;
  }
  const int rg = (xcd >> 2) * 6 + idx / 5, c5 = idx % 5, cq = xcd & 3;
  const int w = __builtin_amdgcn_readfirstlane(threadIdx.x >> 6), lane = threadIdx.x & 63;
  if (c5 < 3) {
    int ct = cq * 3 + c5;
    if (w < 4) k1_body<6, true>(sm, x, wqkv, wphi, wg, wu, ws, rg, ct, w, lane);
    else k1_body<5, true>(sm, x, wqkv, wphi, wg, wu, ws, rg, ct, w, lane);
  } else {
    int ct = cq * 2 + (c5 - 3);
    if (w < 4) k1_body<6, false>(sm, x, wqkv, wphi, wg, wu, ws, rg, ct, w, lane);
    else k1_body<5, false>(sm, x, wqkv, wphi, wg, wu, ws, rg, ct, w, lane);
  }
}

constexpr int K2_LDS = 163840;
constexpr int W_REG = 40960;

DI void k2_oblock(ldsp sm, char* ws, int head, int w, int lane) {
  constexpr int KID=1;
  STAMPAT(0);
  const int g = lane >> 4, fr = lane & 15;
  const int d1 = (fr < 4) ? 16 + fr : 20;
  float a0[10], a1[10];
#pragma unroll
  for (int i = 0; i < 10; ++i) { a0[i] = 0.f; a1[i] = 0.f; }
#pragma unroll
  for (int c = 0; c < 3; ++c) {
    const char* kp = ws + WS_KV + (size_t)((3 * w + c) * NH + head) * 3360;
    const float* p0 = (const float*)(kp + (fr * 4 + g) * 40);
    const float* p1 = (const float*)(kp + (d1 * 4 + g) * 40);
#pragma unroll
    for (int i = 0; i < 5; ++i) {
      f32x2 u = *(const f32x2*)(p0 + 2 * i), v = *(const f32x2*)(p1 + 2 * i);
      a0[2 * i] += u[0]; a0[2 * i + 1] += u[1];
      a1[2 * i] += v[0]; a1[2 * i + 1] += v[1];
    }
  }
#pragma unroll
  for (int i = 0; i < 10; ++i) {
    *(LAS float*)(sm + ((w * 20 + i) * 64 + lane) * 4) = a0[i];
    *(LAS float*)(sm + ((w * 20 + 10 + i) * 64 + lane) * 4) = a1[i];
  }
  __syncthreads();
#pragma unroll
  for (int i = 0; i < 10; ++i) {
    float s = 0.f, t = 0.f;
#pragma unroll
    for (int ww = 0; ww < 4; ++ww) {
      s += *(const LAS float*)(sm + ((ww * 20 + i) * 64 + lane) * 4);
      t += *(const LAS float*)(sm + ((ww * 20 + 10 + i) * 64 + lane) * 4);
    }
    a0[i] = s; a1[i] = t;
  }
  const bf16x8 ka0 = __builtin_bit_cast(bf16x8, u32x4{pk2(a0[0], a0[1]), pk2(a0[2], a0[3]), pk2(a0[4], a0[5]), pk2(a0[6], a0[7])});
  const bf16x8 kb0 = __builtin_bit_cast(bf16x8, u32x4{pk2(a0[8], a0[9]), 0u, 0u, 0u});
  const bf16x8 ka1 = __builtin_bit_cast(bf16x8, u32x4{pk2(a1[0], a1[1]), pk2(a1[2], a1[3]), pk2(a1[4], a1[5]), pk2(a1[6], a1[7])});
  const bf16x8 kb1 = __builtin_bit_cast(bf16x8, u32x4{pk2(a1[8], a1[9]), 0u, 0u, 0u});
  STAMPAT(1);
  char* om = ws + WS_OM + (size_t)head * S_ * 32;
  char* orr = ws + WS_OR + (size_t)(head >> 3) * S_ * 64 + (head & 7) * 8;
#pragma unroll 1
  for (int c = 0; c < 3; ++c) {
    const int rg = 3 * w + c;
    const int r0 = rs_of(rg), nrow = rs_of(rg + 1) - r0;
    const char* pq = ws + WS_PQ + (size_t)(rg * NH + head) * 11 * 1024 + lane * 16;
    const char* pq2 = ws + WS_PQ2 + (size_t)(rg * NH + head) * 11 * 256 + lane * 4;
    u32x4 pa[11];
    unsigned pb[11];
#pragma unroll
    for (int m = 0; m < 11; ++m) { pa[m] = *(const u32x4*)(pq + m * 1024); pb[m] = *(const unsigned*)(pq2 + m * 256); }
#pragma unroll
    for (int m = 0; m < 11; ++m) {
      f32x4 z4 = f32x4{0.f, 0.f, 0.f, 0.f};
      bf16x8 b0 = __builtin_bit_cast(bf16x8, pa[m]);
      bf16x8 b1 = __builtin_bit_cast(bf16x8, u32x4{pb[m], 0u, 0u, 0u});
      f32x4 n0 = mfma32(kb0, b1, mfma32(ka0, b0, z4));
      f32x4 n1 = mfma32(kb1, b1, mfma32(ka1, b0, z4));
      auto r = __builtin_amdgcn_permlane16_swap(__float_as_uint(n1[0]), __float_as_uint(n1[0]), false, false);
      float den = __uint_as_float(r[1]);
      float inv = __builtin_amdgcn_rcpf(den + 1e-6f);
      int slot = m * 16 + fr;
      if (slot < nrow) {
        size_t tok = r0 + slot;
        unsigned long long v0 = (unsigned long long)pk2(n0[0] * inv, n0[1] * inv) | ((unsigned long long)pk2(n0[2] * inv, n0[3] * inv) << 32);
        __hip_atomic_store((GAS unsigned long long*)(om + tok * 32 + g * 8), v0, __ATOMIC_RELAXED, __HIP_MEMORY_SCOPE_AGENT);
        if (g == 0) {
          unsigned long long v1 = (unsigned long long)pk2(n1[0] * inv, n1[1] * inv) | ((unsigned long long)pk2(n1[2] * inv, n1[3] * inv) << 32);
          __hip_atomic_store((GAS unsigned long long*)(orr + tok * 64), v1, __ATOMIC_RELAXED, __HIP_MEMORY_SCOPE_AGENT);
        }
      }
    }
  }
  STAMPAT(2);
  asm volatile("s_waitcnt vmcnt(0)" ::: "memory");
  STAMPAT(3);
  __syncthreads();
  if (threadIdx.x == 0) __hip_atomic_store((GAS unsigned*)(ws + WS_FLAG) + head, 1u, __ATOMIC_RELAXED, __HIP_MEMORY_SCOPE_AGENT);
}

__global__ __launch_bounds__(256, 1) void k2(const char* __restrict__ x, const char* __restrict__ wo,
                                             const char* __restrict__ wd, char* __restrict__ ws, char* __restrict__ out) {
  extern __shared__ __attribute__((aligned(16))) char smem[];
  ldsp sm = (ldsp)smem;
  const int b = blockIdx.x, xcd = b & 7, idx = b >> 3;
  const int w = __builtin_amdgcn_readfirstlane(threadIdx.x >> 6), lane = threadIdx.x & 63;
  if (idx < 4) { k2_oblock(sm, ws, xcd * 4 + idx, w, lane); return; }
  const int tstart = (220 * xcd) / 8, tend = (220 * (xcd + 1)) / 8;
  const int T = tstart + (idx - 4);
  if (T >= tend) return;
  const int chh = T / 110, rem = T % 110, rg = rem / 5, ct = chh * 5 + rem % 5;
  const int r0 = rg * 96, c0 = ct * 64;
  const int g = lane >> 4, fr = lane & 15, l3 = lane >> 3, l7 = lane & 7;
  const unsigned smb = (unsigned)(size_t)sm + w * W_REG;
  ldsp my = sm + w * W_REG;

  const u32x4 srd_h = make_srd(ws + WS_H, S_ * DF_ * 2);
  const u32x4 srd_wd = make_srd(wd, DF_ * D_ * 2);
  const u32x4 srd_wo = make_srd(wo, D_ * D_ * 2);
  const u32x4 srd_om = make_srd(ws + WS_OM, NH * S_ * 32);
  const u32x4 srd_or = make_srd(ws + WS_OR, 4 * S_ * 64);
  unsigned vA[2], vB[2], vR[2];
#pragma unroll
  for (int p = 0; p < 2; ++p) {
    int fa = (4 * p + (l3 >> 1)) & 7;
    vA[p] = l3 * 3072 + ((l7 ^ fa) * 16);
    int fb = ((l3 >> 1) & 1) | (p << 1);
    int ch = (((l7 >> 1) ^ fb) << 1) | (l7 & 1);
    vB[p] = l3 * 1280 + ch * 16;
    vR[p] = (20 * (lane >> 5) + 16 + (l3 & 3)) * 1280 + ch * 16;
  }
  const unsigned vL = lane * 16;

  auto issue_down = [&](int v, int slot) {
    const unsigned kb = (6 * w + v) * 64;
    const unsigned da = smb + slot * 20480, db = da + 12288;
#pragma unroll
    for (int i = 0; i < 12; ++i) dma_b(srd_h, vA[i & 1], (r0 + 8 * i) * 3072 + kb * 2, da + i * 1024);
#pragma unroll
    for (int i = 0; i < 8; ++i) dma_b(srd_wd, vB[i & 1], (kb + 8 * i) * 1280 + c0 * 2, db + i * 1024);
  };
  auto issue_om_B = [&](int a, int slot) {
    const unsigned db = smb + slot * 20480 + 12288;
#pragma unroll
    for (int i = 0; i < 8; ++i) dma_b(srd_wo, vB[i & 1], (80 * a + 20 * (i >> 1) + 8 * (i & 1)) * 1280 + c0 * 2, db + i * 1024);
  };
  auto issue_om_A = [&](int a, int slot) {
    const unsigned da = smb + slot * 20480;
#pragma unroll
    for (int i = 0; i < 12; ++i) dma_b_sc1(srd_om, vL, ((4 * a + i / 3) * S_ + r0) * 32 + (i % 3) * 1024, da + i * 1024);
  };
  auto issue_or = [&](int slot) {
    const unsigned da = smb + slot * 20480, db = da + 12288;
#pragma unroll
    for (int i = 0; i < 6; ++i) dma_b_sc1(srd_or, vL, (w * S_ + r0) * 64 + i * 1024, da + i * 1024);
#pragma unroll
    for (int i = 0; i < 4; ++i) dma_b(srd_wo, vR[i & 1], (160 * w + 40 * i) * 1280 + c0 * 2, db + i * 1024);
  };

  f32x4 acc[6][4];
#pragma unroll
  for (int m = 0; m < 6; ++m)
#pragma unroll
    for (int j = 0; j < 4; ++j) acc[m][j] = f32x4{0.f, 0.f, 0.f, 0.f};
  const int aD0 = fr * 128 + ((g ^ ((fr >> 1) & 7)) * 16), aD1 = aD0 ^ 64;
  const int aM0 = (g >> 1) * 3072 + fr * 32 + (g & 1) * 16;
  const int aR0 = fr * 64 + g * 16;
  int bO[4];
  {
    const int q = fr >> 2, p = lane & 3;
    const int f = ((q >> 1) & 1) | ((g & 1) << 1);
#pragma unroll
    for (int j = 0; j < 4; ++j) bO[j] = 12288 + (8 * g + q) * 128 + ((j ^ f) * 32) + p * 8;
  }
  bf16x8 fa[2][6], fb[2][4];
  auto read_unit = [&](int slot, int a0, int a1, int mstride, int nsub) {
    ldsp p = my + slot * 20480;
#pragma unroll
    for (int s = 0; s < 2; ++s) {
      if (s < nsub) {
#pragma unroll
        for (int m = 0; m < 6; ++m) fa[s][m] = ld128(p + (s ? a1 : a0) + m * mstride);
#pragma unroll
        for (int j = 0; j < 4; ++j) fb[s][j] = tr2(p + bO[j] + s * 4096, p + bO[j] + s * 4096 + 512);
      }
    }
    LGKM0();
    SCHED0();
  };
  auto mma_unit = [&](int nsub) {
#pragma unroll
    for (int s = 0; s < 2; ++s)
      if (s < nsub) {
#pragma unroll
        for (int m = 0; m < 6; ++m)
#pragma unroll
          for (int j = 0; j < 4; ++j) acc[m][j] = mfma32(fb[s][j], fa[s][m], acc[m][j]);
      }
    SCHED0();
  };

  constexpr int KID=1;
  STAMPAT(0);
  issue_down(0, 0);
  issue_down(1, 1);
#pragma unroll 1
  for (int v = 0; v < 4; ++v) {
    WAIT_VM(20);
    read_unit(v & 1, aD0, aD1, 2048, 2);
    issue_down(v + 2, v & 1);
    mma_unit(2);
  }
  WAIT_VM(20);
  read_unit(0, aD0, aD1, 2048, 2);
  issue_om_B(2 * w, 0);
  mma_unit(2);
  STAMPAT(1);
  {
    GAS unsigned* fl = (GAS unsigned*)(ws + WS_FLAG) + 8 * w + l7;
    for (unsigned spins = 0; spins < (1u << 22); ++spins) {
      unsigned v = __hip_atomic_load(fl, __ATOMIC_RELAXED, __HIP_MEMORY_SCOPE_AGENT);
      if (__all(v == 1u)) break;
      __builtin_amdgcn_s_sleep(2);
    }
    asm volatile("" ::: "memory");
  }
  STAMPAT(2);
  issue_om_A(2 * w, 0);
  WAIT_VM(12);
  read_unit(1, aD0, aD1, 2048, 2);
  issue_om_B(2 * w + 1, 1);
  issue_om_A(2 * w + 1, 1);
  mma_unit(2);
  WAIT_VM(20);
  read_unit(0, aM0, aM0 + 6144, 512, 2);
  issue_or(0);
  mma_unit(2);
  WAIT_VM(10);
  read_unit(1, aM0, aM0 + 6144, 512, 2);
  mma_unit(2);
  WAIT_VM(0);
  read_unit(0, aR0, aR0, 1024, 1);
  mma_unit(1);

  STAMPAT(3);
#pragma unroll
  for (int m = 0; m < 6; ++m)
#pragma unroll
    for (int j = 0; j < 4; ++j) *(LAS f32x4*)(my + (m * 16 + fr) * 272 + j * 64 + g * 16) = acc[m][j];
  u32x4 xr[3];
#pragma unroll
  for (int i = 0; i < 3; ++i) {
    int row = r0 + (i * 4 + w) * 8 + l3;
    if (row > S_ - 1) row = S_ - 1;
    xr[i] = *(const u32x4*)(x + (size_t)row * 1280 + c0 * 2 + l7 * 16);
  }
  LGKM_BAR();
#pragma unroll
  for (int i = 0; i < 3; ++i) {
    int rl = (i * 4 + w) * 8 + l3;
    f32x4 s0 = f32x4{0.f, 0.f, 0.f, 0.f}, s1 = s0;
#pragma unroll
    for (int ww = 0; ww < 4; ++ww) {
      s0 += *(const LAS f32x4*)(sm + ww * W_REG + rl * 272 + l7 * 32);
      s1 += *(const LAS f32x4*)(sm + ww * W_REG + rl * 272 + l7 * 32 + 16);
    }
    u32x4 o;
#pragma unroll
    for (int e = 0; e < 2; ++e) {
      o[e] = pk2(s0[2 * e] + __uint_as_float(xr[i][e] << 16), s0[2 * e + 1] + __uint_as_float(xr[i][e] & 0xffff0000u));
      o[2 + e] = pk2(s1[2 * e] + __uint_as_float(xr[i][2 + e] << 16), s1[2 * e + 1] + __uint_as_float(xr[i][2 + e] & 0xffff0000u));
    }
    if (r0 + rl < S_) *(u32x4*)(out + (size_t)(r0 + rl) * 1280 + c0 * 2 + l7 * 16) = o;
  }
  asm volatile("s_waitcnt vmcnt(0)" ::: "memory");
  STAMPAT(4);
}

extern "C" void kernel_launch(void* const* d_in, const int* in_sizes, int n_in, void* d_out, int out_size, void* d_ws,
                              size_t ws_size, hipStream_t stream) {
  const char* x = (const char*)d_in[0];
  const char* wqkv = (const char*)d_in[1];
  const char* wphi = (const char*)d_in[2];
  const char* wo = (const char*)d_in[3];
  const char* wg = (const char*)d_in[5];
  const char* wu = (const char*)d_in[6];
  const char* wd = (const char*)d_in[7];
  (void)hipFuncSetAttribute((const void*)k1, hipFuncAttributeMaxDynamicSharedMemorySize, K1_LDS);
  (void)hipFuncSetAttribute((const void*)k2, hipFuncAttributeMaxDynamicSharedMemorySize, K2_LDS);
  k1<<<256, 512, K1_LDS, stream>>>(x, wqkv, wphi, wg, wu, (char*)d_ws);
  k2<<<256, 256, K2_LDS, stream>>>(x, wo, wd, (char*)d_ws, (char*)d_out);
}
```

```cpp
#include <hip/hip_runtime.h>
#include <hip/hip_bf16.h>
#include <stdint.h>

typedef __attribute__((ext_vector_type(8))) __bf16 bf16x8;
typedef __attribute__((ext_vector_type(2))) __bf16 bf16x2;
typedef __attribute__((ext_vector_type(4))) short s16x4;
typedef __attribute__((ext_vector_type(8))) short s16x8;
typedef __attribute__((ext_vector_type(4))) float f32x4;
typedef __attribute__((ext_vector_type(2))) float f32x2;
typedef __attribute__((ext_vector_type(4))) unsigned u32x4;
typedef __attribute__((ext_vector_type(2))) unsigned u32x2;
#define LAS __attribute__((address_space(3)))
#define GAS __attribute__((address_space(1)))
#define DI __device__ __forceinline__
typedef LAS char* ldsp;

constexpr int S_ = 2048, D_ = 640, DF_ = 1536, NH = 32;
constexpr int NRG = 12;
constexpr size_t WS_H = 0;
constexpr size_t WS_PQ = WS_H + (size_t)S_ * DF_ * 2;
constexpr size_t WS_PQ2 = WS_PQ + (size_t)NRG * NH * 11 * 1024;
constexpr size_t WS_KV = WS_PQ2 + (size_t)NRG * NH * 4096;
constexpr size_t WS_OM = WS_KV + (size_t)NRG * NH * 5120;
constexpr size_t WS_OR = WS_OM + (size_t)NH * S_ * 32;
constexpr size_t WS_FLAG = WS_OR + (size_t)NH * S_ * 8;
constexpr size_t WS_DBG = 64u << 20;

constexpr int A_SLOT = 22528, B_SLOT = 16384;
constexpr int L_A = 0;
constexpr int L_B = 3 * A_SLOT;
constexpr int L_MISC = L_B + 5 * B_SLOT;
constexpr int L_DUMMY = L_MISC;
constexpr int L_WPHI = L_MISC + 1024;
constexpr int L_STAT = L_WPHI + 4096;
constexpr int L_CS = L_STAT + 1536;
constexpr int K1_LDS = L_CS + 2048;
constexpr int K2_LDS = 163840;
constexpr int W_REG = 40960;

#if defined(__HIP_DEVICE_COMPILE__)
#pragma clang attribute push(__attribute__((target("no-packed-fp32-ops"))), apply_to = function)
#endif

#define STAMPAT(i)

DI float u2f(unsigned u) { return __builtin_bit_cast(float, u); }
DI unsigned f2u(float f) { return __builtin_bit_cast(unsigned, f); }
DI u32x4 make_srd(const void* p, unsigned bytes) {
  uint64_t a = (uint64_t)p;
  u32x4 r;
  r[0] = (unsigned)a;
  r[1] = (unsigned)(a >> 32) & 0xffffu;
  r[2] = bytes;
  r[3] = 0x00020000u;
  return r;
}
DI void dma_b(u32x4 srd, unsigned voff, unsigned soff, unsigned m0v) {
  asm volatile("s_mov_b32 m0, %3\n\ts_nop 0\n\tbuffer_load_dwordx4 %0, %1, %2 offen lds" ::"v"(voff), "s"(srd), "s"(soff), "s"(m0v) : "memory");
}
DI void dma_b_mask(u32x4 srd, unsigned voff, unsigned soff, unsigned m0v, unsigned long long mask) {
  asm volatile("s_mov_b32 m0, %3\n\ts_mov_b64 exec, %4\n\tbuffer_load_dwordx4 %0, %1, %2 offen lds\n\ts_mov_b64 exec, -1" ::"v"(voff), "s"(srd), "s"(soff), "s"(m0v), "s"(mask) : "memory");
}
DI void dma_b_sc1(u32x4 srd, unsigned voff, unsigned soff, unsigned m0v) {
  asm volatile("s_mov_b32 m0, %3\n\ts_nop 0\n\tbuffer_load_dwordx4 %0, %1, %2 offen sc1 lds" ::"v"(voff), "s"(srd), "s"(soff), "s"(m0v) : "memory");
}
#define WAIT_BAR(N) asm volatile("s_waitcnt vmcnt(" #N ") lgkmcnt(0)\n\ts_barrier" ::: "memory")
#define WAIT_VM(N) asm volatile("s_waitcnt vmcnt(" #N ")" ::: "memory")
#define LGKM_BAR() asm volatile("s_waitcnt lgkmcnt(0)\n\ts_barrier" ::: "memory")
#define LGKM0() asm volatile("s_waitcnt lgkmcnt(0)" ::: "memory")
#define MEMFENCE() asm volatile("" ::: "memory")
#define SCHED0() __builtin_amdgcn_sched_barrier(0)

DI bf16x8 ld128(ldsp p) { return *(const LAS bf16x8*)p; }
DI s16x4 tr1(ldsp p) { return __builtin_amdgcn_ds_read_tr16_b64_v4i16((LAS s16x4*)p); }
DI bf16x8 cat(s16x4 a, s16x4 b) { return __builtin_bit_cast(bf16x8, __builtin_shufflevector(a, b, 0, 1, 2, 3, 4, 5, 6, 7)); }
DI bf16x8 tr2(ldsp p0, ldsp p1) { return cat(tr1(p0), tr1(p1)); }
DI f32x4 mfma32(bf16x8 a, bf16x8 b, f32x4 c) { return __builtin_amdgcn_mfma_f32_16x16x32_bf16(a, b, c, 0, 0, 0); }
DI unsigned pk2(float a, float b) {
  bf16x2 v;
  v[0] = (__bf16)a;
  v[1] = (__bf16)b;
  return __builtin_bit_cast(unsigned, v);
}
DI float dot2(unsigned a, unsigned b, float c) {
  return __builtin_amdgcn_fdot2_f32_bf16(__builtin_bit_cast(bf16x2, a), __builtin_bit_cast(bf16x2, b), c, false);
}
DI void swap16(float& a, float& b) {
  auto r = __builtin_amdgcn_permlane16_swap(f2u(a), f2u(b), false, false);
  a = u2f(r[0]); b = u2f(r[1]);
}
DI void swap32(float& a, float& b) {
  auto r = __builtin_amdgcn_permlane32_swap(f2u(a), f2u(b), false, false);
  a = u2f(r[0]); b = u2f(r[1]);
}
DI void swap16u(unsigned& a, unsigned& b) {
  auto r = __builtin_amdgcn_permlane16_swap(a, b, false, false);
  a = r[0]; b = r[1];
}
template <int CTRL> DI float dpp(float x) { return u2f(__builtin_amdgcn_update_dpp(0u, f2u(x), CTRL, 0xf, 0xf, false)); }
DI float fmx(float a, float b) { return __builtin_fmaxf(a, b); }
DI float fmn(float a, float b) { return __builtin_fminf(a, b); }
DI int rs_of(int rg) { return (512 * rg) / 3; }

template <int MT, bool GU>
DI void k1_body(ldsp sm, const char* x, const char* wqkv, const char* wphi, const char* wg, const char* wu, char* ws,
                int rg, int ct, int w, int lane) {
  constexpr int KID = 0;
  STAMPAT(0);
  constexpr int mh = (MT == 6) ? 0 : 1;
  const int wn = w & 3;
  const int r0 = rs_of(rg), nrow = rs_of(rg + 1) - r0;
  const unsigned smb = (unsigned)(size_t)sm;
  const int g = lane >> 4, fr = lane & 15;

  const u32x4 srdx = make_srd(x, S_ * D_ * 2);
  unsigned voffA[3];
#pragma unroll
  for (int c = 0; c < 3; ++c) {
    int slot = 8 * (w + 8 * c) + (lane >> 3);
    int ch = (lane & 7) ^ ((slot >> 1) & 7);
    int ri = slot < 95 ? slot : slot - 1;
    ri = ri < nrow ? ri : nrow - 1;
    voffA[c] = (unsigned)((r0 + ri) * 1280 + ch * 16);
  }
  if (w >= 6) voffA[2] = 0x40000000u;
  const unsigned long long FULL = ~0ull, PART = 0x00FFFFFFFFFFFFFFull;
  const unsigned long long mask1 = (w == 3) ? PART : FULL, mask2 = (w == 5) ? PART : FULL;
  const unsigned adst0 = smb + L_A + w * 1024;
  const unsigned dummy = smb + L_DUMMY;
  u32x4 srd0, srd1;
  unsigned voff0, voff1;
  constexpr unsigned SSTEP = GU ? 32 * 3072 : 32 * 3840;
  if (GU) {
    srd0 = make_srd(wg, D_ * DF_ * 2);
    srd1 = make_srd(wu, D_ * DF_ * 2);
    int krow = 4 * w + (lane >> 4);
    int chp = lane & 15;
    int f = (krow & 3) | (((krow >> 3) & 1) << 2);
    int ch = (((chp >> 1) ^ f) << 1) | (chp & 1);
    voff0 = voff1 = krow * 3072 + ct * 256 + ch * 16;
  } else {
    srd0 = srd1 = make_srd(wqkv, D_ * 3 * D_ * 2);
    unsigned vo[2];
#pragma unroll
    for (int c = 0; c < 2; ++c) {
      int cidx = (w + 8 * c) * 64 + lane;
      int rho = cidx / 30, cc = cidx % 30;
      int seg = cc / 10, ch = cc % 10;
      int k = (rho & 16) | ((rho & 4) << 1) | ((rho & 8) >> 1) | (rho & 3);
      vo[c] = (cidx < 960) ? (unsigned)(k * 3840 + seg * 1280 + ct * 160 + ch * 16) : 0x40000000u;
    }
    voff0 = vo[0];
    voff1 = vo[1];
  }
  const unsigned bdst0 = smb + L_B + w * 1024;

#define XDMA_B0(soff, dst) dma_b(srd0, voff0, (soff), (dst))
#define XDMA_B1(soff, dst) dma_b(srd1, voff1, (soff), (dst))
#define XDMA_A0(soff, dst) dma_b(srdx, voffA[0], (soff), (dst))
#define XDMA_A1(soff, dst) dma_b_mask(srdx, voffA[1], (soff), (dst), mask1)
#define XDMA_A2(soff, dst) dma_b_mask(srdx, voffA[2], (soff), (dst), mask2)
#define DMA_B0 XDMA_B0
#define DMA_B1 XDMA_B1
#define DMA_A0 XDMA_A0
#define DMA_A1 XDMA_A1
#define DMA_A2 XDMA_A2
  const unsigned a2base = (w >= 6) ? dummy : adst0 + 16384;
  const unsigned a2mul = (w >= 6) ? 0u : 1u;
  DMA_B0(0, bdst0); DMA_B1(0, bdst0 + 8192);
  DMA_A0(0, adst0); DMA_A1(0, adst0 + 8192);
  DMA_A2(0, a2base);
  DMA_B0(SSTEP, bdst0 + B_SLOT); DMA_B1(SSTEP, bdst0 + B_SLOT + 8192);
  DMA_B0(2 * SSTEP, bdst0 + 2 * B_SLOT); DMA_B1(2 * SSTEP, bdst0 + 2 * B_SLOT + 8192);
  DMA_A0(128, adst0 + A_SLOT); DMA_A1(128, adst0 + A_SLOT + 8192);
  DMA_A2(128, a2base + a2mul * A_SLOT);
  DMA_B0(3 * SSTEP, bdst0 + 3 * B_SLOT); DMA_B1(3 * SSTEP, bdst0 + 3 * B_SLOT + 8192);
  DMA_B0(4 * SSTEP, bdst0 + 4 * B_SLOT); DMA_B1(4 * SSTEP, bdst0 + 4 * B_SLOT + 8192);
  DMA_A0(256, adst0 + 2 * A_SLOT); DMA_A1(256, adst0 + 2 * A_SLOT + 8192);
  if (w == 0 && lane < 48) {
    int rr = lane >> 3;
    *(LAS u32x4*)(sm + L_A + (rr >> 1) * A_SLOT + ((rr & 1) ? 175 : 95) * 128 + (lane & 7) * 16) =
        u32x4{0x3F803F80u, 0x3F803F80u, 0x3F803F80u, 0x3F803F80u};
  }

  const int aoff0 = L_A + mh * 6 * 2048 + fr * 128 + ((g ^ ((fr >> 1) & 7)) * 16);
  const int aoff1 = aoff0 ^ 64;
  int boff[4];
  constexpr int TSTEP = GU ? 1024 : 3840;
  {
    const int q = fr >> 2, p = lane & 3;
    if (GU) {
      int f = q | ((g & 1) << 2);
#pragma unroll
      for (int j = 0; j < 4; ++j)
        boff[j] = L_B + (j >> 1) * 8192 + (8 * g + q) * 256 + (((2 * wn + (j & 1)) ^ f) * 32) + p * 8;
    } else {
      int rho = 16 * (g >> 1) + 4 * (g & 1) + q;
#pragma unroll
      for (int j = 0; j < 3; ++j) boff[j] = L_B + rho * 480 + (20 * j + 5 * wn + p) * 8;
      boff[3] = L_B + rho * 480 + (20 * (p == 3 ? 0 : p) + 5 * wn + 4) * 8;
    }
  }
  const int soff = L_A + w * 1024 + lane * 16;

  f32x4 acc[MT][4];
#pragma unroll
  for (int m = 0; m < MT; ++m)
#pragma unroll
    for (int j = 0; j < 4; ++j) acc[m][j] = f32x4{0.f, 0.f, 0.f, 0.f};
  float s1[3] = {0.f, 0.f, 0.f}, s2[3] = {0.f, 0.f, 0.f};
  bf16x8 fa0[MT], fa1[MT];
  s16x4 fb0[8], fb1[8];
  u32x4 st[3];

  STAMPAT(1);
  WAIT_BAR(13);
  STAMPAT(2);
#pragma unroll
  for (int m = 0; m < MT; ++m) fa0[m] = ld128(sm + aoff0 + m * 2048);
#pragma unroll
  for (int j = 0; j < 8; ++j) fb0[j] = tr1(sm + boff[j >> 1] + (j & 1) * TSTEP);
#pragma unroll
  for (int c = 0; c < 3; ++c) st[c] = *(const LAS u32x4*)(sm + soff + c * 8192);
  MEMFENCE();

  constexpr int NM = MT * 4;
#define PURE 8
#define MMA(i, FA, FB) acc[(i) >> 2][(i) & 3] = mfma32(cat(FB[2 * ((i) & 3)], FB[2 * ((i) & 3) + 1]), FA[(i) >> 2], acc[(i) >> 2][(i) & 3])
#define DOT(i) do { s1[(i) >> 2] = dot2(st[(i) >> 2][(i) & 3], 0x3F803F80u, s1[(i) >> 2]); s2[(i) >> 2] = dot2(st[(i) >> 2][(i) & 3], st[(i) >> 2][(i) & 3], s2[(i) >> 2]); } while (0)

#define FINE_A()
#define FINE_B()
  int a_rd = 0;
  int b_rd = 0;
  unsigned bso = 5 * SSTEP;
#pragma unroll 1
  for (int t = 0; t < 10; ++t) {
    {
      SCHED0();
      FINE_A();
      WAIT_BAR(11);
      FINE_B();
      int b_nx = b_rd + B_SLOT; if (b_nx == 5 * B_SLOT) b_nx = 0;
      int a_p2 = a_rd + 2 * A_SLOT; if (a_p2 >= 3 * A_SLOT) a_p2 -= 3 * A_SLOT;
      const bool va = t < 8, vb = t < 8;
      const unsigned dA = va ? a2base + a2mul * a_p2 : dummy, sA = va ? (unsigned)(t + 2) * 128u : 0x40000000u;
      const unsigned dB0 = vb ? bdst0 + b_rd : dummy, dB1 = vb ? bdst0 + b_rd + 8192 : dummy;
      ldsp pa = sm + a_rd + aoff1;
      ldsp pb = sm + b_nx;
      if (MT == 6) {
#pragma unroll
        for (int i = 0; i < NM; ++i) {
          MMA(i, fa0, fb0);
          if (i >= PURE && i < PURE + 8) fb1[i - PURE] = tr1(pb + boff[(i - PURE) >> 1] + ((i - PURE) & 1) * TSTEP);
          else if (i >= PURE + 8 && i < PURE + 8 + MT) fa1[i - PURE - 8] = ld128(pa + (i - PURE - 8) * 2048);
          SCHED0();
        }
        DMA_A2(sA, dA);
#pragma unroll
        for (int i = 0; i < 6; ++i) DOT(i);
        SCHED0();
        DMA_B0(bso, dB0);
#pragma unroll
        for (int i = 6; i < 12; ++i) DOT(i);
        SCHED0();
        DMA_B1(bso, dB1);
      } else {
#pragma unroll
        for (int i = 0; i < 8; ++i) fb1[i] = tr1(pb + boff[i >> 1] + (i & 1) * TSTEP);
#pragma unroll
        for (int m = 0; m < MT; ++m) fa1[m] = ld128(pa + m * 2048);
        DMA_A2(sA, dA);
#pragma unroll
        for (int i = 0; i < 6; ++i) DOT(i);
        SCHED0();
        DMA_B0(bso, dB0);
#pragma unroll
        for (int i = 6; i < 12; ++i) DOT(i);
        SCHED0();
        DMA_B1(bso, dB1);
        SCHED0();
#pragma unroll
        for (int i = 0; i < NM; ++i) MMA(i, fa0, fb0);
      }
      bso += SSTEP;
      b_rd = b_nx;
      if (t == 9) {
#pragma unroll
        for (int c = 0; c < 3; ++c) {
          float a = s1[c], b = s2[c];
          a += dpp<0xB1>(a); b += dpp<0xB1>(b);
          a += dpp<0x4E>(a); b += dpp<0x4E>(b);
          a += dpp<0x141>(a); b += dpp<0x141>(b);
          float mean = a * (1.0f / 640.0f);
          float var = fmx(b * (1.0f / 640.0f) - mean * mean, 0.f);
          float rstd = __builtin_amdgcn_rsqf(var + 1e-5f);
          int slot = 8 * (w + 8 * c) + (lane >> 3);
          if ((lane & 7) == 0) *(LAS f32x2*)(sm + L_STAT + slot * 8) = f32x2{mean, rstd};
        }
      }
    }
    {
      SCHED0();
      FINE_A();
      WAIT_BAR(9);
      FINE_B();
      int b_nx = b_rd + B_SLOT; if (b_nx == 5 * B_SLOT) b_nx = 0;
      int a_nx = a_rd + A_SLOT; if (a_nx == 3 * A_SLOT) a_nx = 0;
      const bool v = t < 7;
      const unsigned dB0 = v ? bdst0 + b_rd : dummy, dB1 = v ? bdst0 + b_rd + 8192 : dummy;
      const unsigned dA0 = v ? adst0 + a_rd : dummy, dA1 = v ? adst0 + a_rd + 8192 : dummy;
      const unsigned sA = v ? (unsigned)(t + 3) * 128u : 0x40000000u;
      ldsp pa = sm + a_nx + aoff0;
      ldsp pb = sm + b_nx;
      ldsp ps = sm + a_nx + soff;
      if (MT == 6) {
#pragma unroll
        for (int i = 0; i < NM; ++i) {
          MMA(i, fa1, fb1);
          constexpr int P2 = PURE > 7 ? 7 : PURE;
          if (i >= P2 && i < P2 + 8) fb0[i - P2] = tr1(pb + boff[(i - P2) >> 1] + ((i - P2) & 1) * TSTEP);
          else if (i >= P2 + 8 && i < P2 + 8 + MT) fa0[i - P2 - 8] = ld128(pa + (i - P2 - 8) * 2048);
          else if (i >= P2 + 8 + MT && i < P2 + 11 + MT) st[i - P2 - 8 - MT] = *(const LAS u32x4*)(ps + (i - P2 - 8 - MT) * 8192);
          SCHED0();
        }
        DMA_B0(bso, dB0);
        DMA_B1(bso, dB1);
        DMA_A0(sA, dA0);
        DMA_A1(sA, dA1);
      } else {
#pragma unroll
        for (int i = 0; i < 8; ++i) fb0[i] = tr1(pb + boff[i >> 1] + (i & 1) * TSTEP);
#pragma unroll
        for (int m = 0; m < MT; ++m) fa0[m] = ld128(pa + m * 2048);
#pragma unroll
        for (int c = 0; c < 3; ++c) st[c] = *(const LAS u32x4*)(ps + c * 8192);
        DMA_B0(bso, dB0);
        DMA_B1(bso, dB1);
        DMA_A0(sA, dA0);
        DMA_A1(sA, dA1);
        SCHED0();
#pragma unroll
        for (int i = 0; i < NM; ++i) MMA(i, fa1, fb1);
      }
      bso += SSTEP;
      b_rd = b_nx;
      a_rd = a_nx;
      MEMFENCE();
    }
  }
  SCHED0();
  STAMPAT(3);

  if (fr == 15) {
#pragma unroll
    for (int j = 0; j < 4; ++j) *(LAS f32x4*)(sm + L_CS + w * 256 + j * 64 + g * 16) = acc[MT - 1][j];
  }
  LGKM0();
  f32x4 cs[4];
#pragma unroll
  for (int j = 0; j < 4; ++j) cs[j] = *(const LAS f32x4*)(sm + L_CS + w * 256 + j * 64 + g * 16);
  float rstd[MT];
#pragma unroll
  for (int m = 0; m < MT; ++m) {
    f32x2 sv = *(const LAS f32x2*)(sm + L_STAT + ((mh * 6 + m) * 16 + fr) * 8);
    rstd[m] = sv[1];
#pragma unroll
    for (int j = 0; j < 4; ++j)
#pragma unroll
      for (int r = 0; r < 4; ++r) acc[m][j][r] = __builtin_fmaf(-sv[0], cs[j][r], acc[m][j][r]);
  }
  STAMPAT(4);

  if (GU) {
    const float K0 = -2.0f * 0.7978845608028654f * 1.4426950408889634f;
    const float K1c = K0 * 0.044715f;
    u32x2 hp[MT][2];
#pragma unroll
    for (int m = 0; m < MT; ++m) {
      const float r = rstd[m], r2 = r * r;
      const float c0 = K0 * r, c1 = K1c * r2 * r, ir2 = __builtin_amdgcn_rcpf(r2);
#pragma unroll
      for (int j = 0; j < 2; ++j) {
        float hv[4];
#pragma unroll
        for (int q = 0; q < 4; ++q) {
          float a = acc[m][j][q], b = acc[m][j + 2][q];
          float e = __builtin_amdgcn_exp2f(a * __builtin_fmaf(a * a, c1, c0));
          hv[q] = a * b * __builtin_amdgcn_rcpf(__builtin_fmaf(e, ir2, ir2));
        }
        hp[m][j] = u32x2{pk2(hv[0], hv[1]), pk2(hv[2], hv[3])};
      }
    }
    SCHED0();
#pragma unroll
    for (int m = 0; m < MT; ++m) {
      int slot = (mh * 6 + m) * 16 + fr;
#pragma unroll
      for (int j = 0; j < 2; ++j) {
        int chunk = 4 * wn + 2 * j + (g >> 1);
        *(LAS u32x2*)(sm + slot * 256 + ((chunk ^ fr) * 16) + (g & 1) * 8) = hp[m][j];
      }
    }
    LGKM_BAR();
    STAMPAT(5);
    char* hg = ws + WS_H;
#pragma unroll
    for (int i = 0; i < 6; ++i) {
      int s = 4 * (w + 8 * i) + g;
      int ri = s < 95 ? s : s - 1;
      if (s != 95 && ri < nrow) {
        u32x4 v = *(const LAS u32x4*)(sm + s * 256 + ((fr ^ (s & 15)) * 16));
        *(u32x4*)(hg + (size_t)(r0 + ri) * 3072 + ct * 256 + fr * 16) = v;
      }
    }
  } else {
    const int head = ct * 4 + wn;
    bf16x8 wq[2], wk[2];
    {
      const unsigned short* wp = (const unsigned short*)(wphi + head * 800);
#pragma unroll
      for (int ft = 0; ft < 2; ++ft) {
        int f = (ft == 0) ? (5 * (fr >> 2) + (fr & 3)) : (5 * (fr >> 2) + 4);
        bool valid = (ft == 0) || ((fr & 3) == 0);
        s16x8 a, b;
#pragma unroll
        for (int j = 0; j < 4; ++j) {
          short lo = (short)wp[(4 * g + j) * 20 + f];
          short hi = (short)wp[(16 + j) * 20 + f];
          lo = valid ? lo : (short)0;
          hi = valid ? hi : (short)0;
          a[j] = lo; b[j] = lo;
          a[4 + j] = (g == 0) ? hi : (short)0;
          b[4 + j] = (g == 1) ? hi : (short)0;
        }
        wq[ft] = __builtin_bit_cast(bf16x8, a);
        wk[ft] = __builtin_bit_cast(bf16x8, b);
      }
    }
    ldsp pk_img = sm + w * 17408;
    ldsp v_img = pk_img + 96 * 104;
    char* pq = ws + WS_PQ + ((size_t)(rg * NH + head) * 11 + mh * 6) * 1024 + lane * 16;
    char* pq2 = ws + WS_PQ2 + ((size_t)(rg * NH + head) * 2 + mh) * 2048 + lane * 16;
    u32x2 ka[6], kb[6], vv[6], ve[6];
    unsigned kc[6], q5[8];
#pragma unroll
    for (int m = 0; m < 8; ++m) q5[m] = 0u;
#pragma unroll
    for (int m = 0; m < 6; ++m) {
      if (m < MT) {
        const float r = rstd[m];
        u32x2 rem = u32x2{pk2(acc[m][3][0], acc[m][3][1]), pk2(acc[m][3][2], acc[m][3][3])};
        u32x4 qb = u32x4{pk2(acc[m][0][0], acc[m][0][1]), pk2(acc[m][0][2], acc[m][0][3]), rem[0], rem[1]};
        u32x4 kbb = u32x4{pk2(acc[m][1][0], acc[m][1][1]), pk2(acc[m][1][2], acc[m][1][3]), rem[0], rem[1]};
        f32x4 z4 = f32x4{0.f, 0.f, 0.f, 0.f};
        f32x4 q0 = mfma32(wq[0], __builtin_bit_cast(bf16x8, qb), z4);
        f32x4 q1 = mfma32(wq[1], __builtin_bit_cast(bf16x8, qb), z4);
        f32x4 k0 = mfma32(wk[0], __builtin_bit_cast(bf16x8, kbb), z4);
        f32x4 k1 = mfma32(wk[1], __builtin_bit_cast(bf16x8, kbb), z4);
        vv[m] = u32x2{pk2(acc[m][2][0] * r, acc[m][2][1] * r), pk2(acc[m][2][2] * r, acc[m][2][3] * r)};
        u32x2 vr = u32x2{pk2(acc[m][3][0] * r, acc[m][3][1] * r), pk2(acc[m][3][2] * r, acc[m][3][3] * r)};
        ve[m] = (g == 2) ? vr : u32x2{0x3F803F80u, 0x3F803F80u};
        const float vq[5] = {q0[0], q0[1], q0[2], q0[3], q1[0]};
        const float vk[5] = {k0[0], k0[1], k0[2], k0[3], k1[0]};
        const float c = r * 1.4426950408889634f;
        float aq = fmx(fmx(fmx(vq[0], vq[1]), fmx(vq[2], vq[3])), vq[4]);
        float bq = -fmn(fmn(fmn(vq[0], vq[1]), fmn(vq[2], vq[3])), vq[4]);
        float ak = fmx(fmx(fmx(vk[0], vk[1]), fmx(vk[2], vk[3])), vk[4]);
        float bk = -fmn(fmn(fmn(vk[0], vk[1]), fmn(vk[2], vk[3])), vk[4]);
        swap16(aq, bq); swap16(ak, bk);
        float cq = fmx(aq, bq), ck = fmx(ak, bk);
        swap32(cq, ck);
        float d0 = fmx(cq, ck) * c, d1 = d0;
        swap32(d0, d1);
        float Mq = d0, Nq = d0, Mk = d1, Nk = d1;
        swap16(Mq, Nq); swap16(Mk, Nk);
        float pq_[5], nq_[5], pk_[5], nk_[5];
#pragma unroll
        for (int i = 0; i < 5; ++i) {
          pq_[i] = __builtin_amdgcn_exp2f(__builtin_fmaf(vq[i], c, -Mq));
          nq_[i] = __builtin_amdgcn_exp2f(__builtin_fmaf(vq[i], -c, -Nq));
          pk_[i] = __builtin_amdgcn_exp2f(__builtin_fmaf(vk[i], c, -Mk));
          nk_[i] = __builtin_amdgcn_exp2f(__builtin_fmaf(vk[i], -c, -Nk));
        }
        aq = ((pq_[0] + pq_[1]) + (pq_[2] + pq_[3])) + pq_[4];
        bq = ((nq_[0] + nq_[1]) + (nq_[2] + nq_[3])) + nq_[4];
        ak = ((pk_[0] + pk_[1]) + (pk_[2] + pk_[3])) + pk_[4];
        bk = ((nk_[0] + nk_[1]) + (nk_[2] + nk_[3])) + nk_[4];
        swap16(aq, bq); swap16(ak, bk);
        cq = aq + bq; ck = ak + bk;
        swap32(cq, ck);
        d0 = __builtin_amdgcn_rcpf(cq + ck);
        {
          int s = mh * 96 + m * 16 + fr;
          int ri = s < 95 ? s : s - 1;
          bool live = (s != 95) && (ri < nrow);
          if (!live && g >= 2) d0 = 0.f;
        }
        d1 = d0;
        swap32(d0, d1);
        Mq = d0; Nq = d0; Mk = d1; Nk = d1;
        swap16(Mq, Nq); swap16(Mk, Nk);
        *(u32x4*)(pq + m * 1024) = u32x4{pk2(pq_[0] * Mq, pq_[1] * Mq), pk2(pq_[2] * Mq, pq_[3] * Mq),
                                         pk2(nq_[0] * Nq, nq_[1] * Nq), pk2(nq_[2] * Nq, nq_[3] * Nq)};
        q5[m] = pk2(pq_[4] * Mq, nq_[4] * Nq);
        ka[m] = u32x2{pk2(pk_[0] * Mk, pk_[1] * Mk), pk2(pk_[2] * Mk, pk_[3] * Mk)};
        kb[m] = u32x2{pk2(nk_[0] * Nk, nk_[1] * Nk), pk2(nk_[2] * Nk, nk_[3] * Nk)};
        kc[m] = pk2(pk_[4] * Mk, nk_[4] * Nk);
      } else {
        ka[m] = u32x2{0u, 0u}; kb[m] = ka[m]; kc[m] = 0u; vv[m] = ka[m]; ve[m] = ka[m];
      }
    }
    *(u32x4*)(pq2) = u32x4{q5[0], q5[1], q5[2], q5[3]};
    *(u32x4*)(pq2 + 1024) = u32x4{q5[4], q5[5], q5[6], q5[7]};
    SCHED0();
#pragma unroll
    for (int m = 0; m < 6; ++m) {
      int tok = m * 16 + fr;
      *(LAS u32x2*)(pk_img + tok * 104 + g * 8) = ka[m];
      *(LAS u32x2*)(pk_img + tok * 104 + 32 + g * 8) = kb[m];
      *(LAS u32x2*)(pk_img + tok * 104 + 64 + g * 8) = u32x2{kc[m], 0u};
      *(LAS u32x2*)(v_img + tok * 72 + g * 8) = vv[m];
      *(LAS u32x2*)(v_img + tok * 72 + 32 + (g ^ 2) * 8) = ve[m];
    }
    LGKM0();
    SCHED0();
    STAMPAT(5);
    f32x4 kv[3][2];
#pragma unroll
    for (int ft = 0; ft < 3; ++ft)
#pragma unroll
      for (int dt = 0; dt < 2; ++dt) kv[ft][dt] = f32x4{0.f, 0.f, 0.f, 0.f};
    {
      const int q = fr >> 2, p = lane & 3;
#pragma unroll
      for (int s = 0; s < 3; ++s) {
        int row = 32 * s + 8 * g + q;
        bf16x8 a[3], b[2];
#pragma unroll
        for (int ft = 0; ft < 3; ++ft) a[ft] = tr2(pk_img + row * 104 + ft * 32 + p * 8, pk_img + (row + 4) * 104 + ft * 32 + p * 8);
#pragma unroll
        for (int dt = 0; dt < 2; ++dt) b[dt] = tr2(v_img + row * 72 + dt * 32 + p * 8, v_img + (row + 4) * 72 + dt * 32 + p * 8);
#pragma unroll
        for (int ft = 0; ft < 3; ++ft)
#pragma unroll
          for (int dt = 0; dt < 2; ++dt) kv[ft][dt] = mfma32(a[ft], b[dt], kv[ft][dt]);
      }
    }
    f32x4 o5[5] = {kv[0][0], kv[1][0], kv[0][1], kv[1][1], f32x4{kv[2][0][0], kv[2][0][1], kv[2][1][0], kv[2][1][1]}};
    ldsp ex = sm + (4 + wn) * 17408;
    if (mh == 1) {
      LGKM0();
#pragma unroll
      for (int i = 0; i < 5; ++i) *(LAS f32x4*)(ex + i * 1024 + lane * 16) = o5[i];
    }
    LGKM_BAR();
    if (mh == 0) {
      char* kp = ws + WS_KV + (size_t)(rg * NH + head) * 5120 + lane * 16;
#pragma unroll
      for (int i = 0; i < 5; ++i) {
        f32x4 e = *(const LAS f32x4*)(ex + i * 1024 + lane * 16);
        *(f32x4*)(kp + i * 1024) = f32x4{o5[i][0] + e[0], o5[i][1] + e[1], o5[i][2] + e[2], o5[i][3] + e[3]};
      }
    }
  }
}

__global__ __launch_bounds__(512, 2) void k1(const char* __restrict__ x, const char* __restrict__ wqkv,
                                             const char* __restrict__ wphi, const char* __restrict__ wg,
                                             const char* __restrict__ wu, char* __restrict__ ws) {
  extern __shared__ __attribute__((aligned(16))) char smem[];
  ldsp sm = (ldsp)smem;
  const int tid = __builtin_amdgcn_workitem_id_x();
  const int b = __builtin_amdgcn_workgroup_id_x(), xcd = b & 7, idx = b >> 3;
  if (idx >= 30) {
    if (b == 240 && tid < 32) ((unsigned*)(ws + WS_FLAG))[tid] = 0u;
    return;
  }
  const int rg = (xcd >> 2) * 6 + idx / 5, c5 = idx % 5, cq = xcd & 3;
  const int w = __builtin_amdgcn_readfirstlane(tid >> 6), lane = tid & 63;
  if (c5 < 3) {
    int ct = cq * 3 + c5;
    if (w < 4) k1_body<6, true>(sm, x, wqkv, wphi, wg, wu, ws, rg, ct, w, lane);
    else k1_body<5, true>(sm, x, wqkv, wphi, wg, wu, ws, rg, ct, w, lane);
  } else {
    int ct = cq * 2 + (c5 - 3);
    if (w < 4) k1_body<6, false>(sm, x, wqkv, wphi, wg, wu, ws, rg, ct, w, lane);
    else k1_body<5, false>(sm, x, wqkv, wphi, wg, wu, ws, rg, ct, w, lane);
  }
}

DI void st128_sc1(char* p, u32x4 v) { asm volatile("global_store_dwordx4 %0, %1, off sc1\n\ts_nop 1" ::"v"(p), "v"(v) : "memory"); }
DI void st64_sc1(char* p, u32x2 v) { asm volatile("global_store_dwordx2 %0, %1, off sc1" ::"v"(p), "v"(v) : "memory"); }

DI void k2_oblock(ldsp sm, char* ws, int head, int w, int lane) {
  constexpr int KID = 1;
  STAMPAT(0);
  const int g = lane >> 4, fr = lane & 15;
  f32x4 part[3][5];
  u32x4 pa[3][11], pb[3][4];
#pragma unroll
  for (int c = 0; c < 3; ++c) {
    const char* kp = ws + WS_KV + (size_t)((3 * w + c) * NH + head) * 5120 + lane * 16;
#pragma unroll
    for (int i = 0; i < 5; ++i) part[c][i] = *(const f32x4*)(kp + i * 1024);
  }
#pragma unroll
  for (int c = 0; c < 3; ++c) {
    const char* pq = ws + WS_PQ + (size_t)((3 * w + c) * NH + head) * 11 * 1024 + lane * 16;
    const char* pq2 = ws + WS_PQ2 + (size_t)((3 * w + c) * NH + head) * 4096 + lane * 16;
#pragma unroll
    for (int i = 0; i < 4; ++i) pb[c][i] = *(const u32x4*)(pq2 + i * 1024);
#pragma unroll
    for (int m = 0; m < 11; ++m) pa[c][m] = *(const u32x4*)(pq + m * 1024);
  }
  SCHED0();
#pragma unroll
  for (int i = 0; i < 5; ++i) {
    f32x4 s;
#pragma unroll
    for (int e = 0; e < 4; ++e) s[e] = part[0][i][e] + part[1][i][e] + part[2][i][e];
    *(LAS f32x4*)(sm + (w * 5 + i) * 1024 + lane * 16) = s;
  }
  LGKM_BAR();
  float kvs[5][4];
#pragma unroll
  for (int i = 0; i < 5; ++i) {
    f32x4 a = *(const LAS f32x4*)(sm + (0 * 5 + i) * 1024 + lane * 16);
    f32x4 b = *(const LAS f32x4*)(sm + (1 * 5 + i) * 1024 + lane * 16);
    f32x4 c = *(const LAS f32x4*)(sm + (2 * 5 + i) * 1024 + lane * 16);
    f32x4 d = *(const LAS f32x4*)(sm + (3 * 5 + i) * 1024 + lane * 16);
#pragma unroll
    for (int e = 0; e < 4; ++e) kvs[i][e] = (a[e] + b[e]) + (c[e] + d[e]);
  }
  const bf16x8 ka0 = __builtin_bit_cast(bf16x8, u32x4{pk2(kvs[0][0], kvs[0][1]), pk2(kvs[0][2], kvs[0][3]), pk2(kvs[1][0], kvs[1][1]), pk2(kvs[1][2], kvs[1][3])});
  const bf16x8 ka1 = __builtin_bit_cast(bf16x8, u32x4{pk2(kvs[2][0], kvs[2][1]), pk2(kvs[2][2], kvs[2][3]), pk2(kvs[3][0], kvs[3][1]), pk2(kvs[3][2], kvs[3][3])});
  const bf16x8 kb0 = __builtin_bit_cast(bf16x8, u32x4{pk2(kvs[4][0], kvs[4][1]), 0u, 0u, 0u});
  const bf16x8 kb1 = __builtin_bit_cast(bf16x8, u32x4{pk2(kvs[4][2], kvs[4][3]), 0u, 0u, 0u});
  STAMPAT(1);
  char* om = ws + WS_OM + (size_t)head * S_ * 32;
  char* orr = ws + WS_OR + (size_t)head * S_ * 8;
#pragma unroll
  for (int c = 0; c < 3; ++c) {
    const int rg = 3 * w + c;
    const int r0 = rs_of(rg), nrow = rs_of(rg + 1) - r0;
#pragma unroll
    for (int mp = 0; mp < 6; ++mp) {
      unsigned o0[2][2];
#pragma unroll
      for (int hh = 0; hh < 2; ++hh) {
        const int m = 2 * mp + hh;
        if (m < 11) {
          const int mm = m < 6 ? m : m + 2;
          f32x4 z4 = f32x4{0.f, 0.f, 0.f, 0.f};
          bf16x8 b0 = __builtin_bit_cast(bf16x8, pa[c][m]);
          bf16x8 b1 = __builtin_bit_cast(bf16x8, u32x4{pb[c][mm >> 2][mm & 3], 0u, 0u, 0u});
          f32x4 n0 = mfma32(kb0, b1, mfma32(ka0, b0, z4));
          f32x4 n1 = mfma32(kb1, b1, mfma32(ka1, b0, z4));
          float da = n1[0], db = n1[0];
          swap16(da, db);
          float inv = __builtin_amdgcn_rcpf(db + 1e-6f);
          o0[hh][0] = pk2(n0[0] * inv, n0[1] * inv);
          o0[hh][1] = pk2(n0[2] * inv, n0[3] * inv);
          int s = m * 16 + fr;
          int ri = s < 95 ? s : s - 1;
          if (g == 0 && s != 95 && ri < nrow)
            st64_sc1(orr + (size_t)(r0 + ri) * 8, u32x2{pk2(n1[0] * inv, n1[1] * inv), pk2(n1[2] * inv, n1[3] * inv)});
        } else {
          o0[hh][0] = 0u; o0[hh][1] = 0u;
        }
      }
      swap16u(o0[0][0], o0[1][0]);
      swap16u(o0[0][1], o0[1][1]);
      int s = (2 * mp + (g & 1)) * 16 + fr;
      int ri = s < 95 ? s : s - 1;
      if (s != 95 && ri < nrow)
        st128_sc1(om + (size_t)(r0 + ri) * 32 + (g >> 1) * 16, u32x4{o0[0][0], o0[0][1], o0[1][0], o0[1][1]});
    }
  }
  STAMPAT(2);
  asm volatile("s_waitcnt vmcnt(0)\n\ts_barrier" ::: "memory");
  STAMPAT(3);
  if (w == 0 && lane == 0) __hip_atomic_store((GAS unsigned*)(ws + WS_FLAG) + head, 1u, __ATOMIC_RELAXED, __HIP_MEMORY_SCOPE_AGENT);
}

__global__ __launch_bounds__(256, 1) void k2(const char* __restrict__ x, const char* __restrict__ wo,
                                             const char* __restrict__ wd, char* __restrict__ ws, char* __restrict__ out) {
  extern __shared__ __attribute__((aligned(16))) char smem[];
  ldsp sm = (ldsp)smem;
  const int tid = __builtin_amdgcn_workitem_id_x();
  const int b = __builtin_amdgcn_workgroup_id_x(), xcd = b & 7, idx = b >> 3;
  const int w = __builtin_amdgcn_readfirstlane(tid >> 6), lane = tid & 63;
  if (idx < 4) { k2_oblock(sm, ws, xcd * 4 + idx, w, lane); return; }
  const int tstart = (220 * xcd) / 8, tend = (220 * (xcd + 1)) / 8;
  const int T = tstart + (idx - 4);
  if (T >= tend) return;
  constexpr int KID = 1;
  STAMPAT(0);
  const int chh = T / 110, rem = T % 110, rg = rem / 5, ct = chh * 5 + rem % 5;
  const int r0 = rg * 96, c0 = ct * 64;
  const int g = lane >> 4, fr = lane & 15, l3 = lane >> 3, l7 = lane & 7;
  const unsigned smb = (unsigned)(size_t)sm + w * W_REG;
  ldsp my = sm + w * W_REG;

  const u32x4 srd_h = make_srd(ws + WS_H, S_ * DF_ * 2);
  const u32x4 srd_wd = make_srd(wd, DF_ * D_ * 2);
  const u32x4 srd_wo = make_srd(wo, D_ * D_ * 2);
  const u32x4 srd_om = make_srd(ws + WS_OM, NH * S_ * 32);
  const u32x4 srd_or = make_srd(ws + WS_OR, NH * S_ * 8);
  unsigned vA[2], vB[2], vR[2];
#pragma unroll
  for (int p = 0; p < 2; ++p) {
    int fa = (4 * p + (l3 >> 1)) & 7;
    vA[p] = l3 * 3072 + ((l7 ^ fa) * 16);
    int fb = ((l3 >> 1) & 1) | (p << 1);
    int ch = (((l7 >> 1) ^ fb) << 1) | (l7 & 1);
    vB[p] = l3 * 1280 + ch * 16;
    vR[p] = (20 * (lane >> 5) + 16 + (l3 & 3)) * 1280 + ch * 16;
  }
  const unsigned vL = lane * 16;

#define DN_PIECE(i, v, slot) do { const unsigned kb_ = (6 * w + (v)) * 64; \
    if ((i) < 12) dma_b(srd_h, vA[(i) & 1], (r0 + 8 * (i)) * 3072 + kb_ * 2, smb + (slot) * 20480 + (i) * 1024); \
    else dma_b(srd_wd, vB[(i) & 1], (kb_ + 8 * ((i) - 12)) * 1280 + c0 * 2, smb + (slot) * 20480 + (i) * 1024); } while (0)
#define OMB_PIECE(i, a, slot) dma_b(srd_wo, vB[(i) & 1], (80 * (a) + 20 * ((i) >> 1) + 8 * ((i) & 1)) * 1280 + c0 * 2, smb + (slot) * 20480 + 12288 + (i) * 1024)
#define OMA_PIECE(i, a, slot) dma_b_sc1(srd_om, vL, ((4 * (a) + (i) / 3) * S_ + r0) * 32 + ((i) % 3) * 1024, smb + (slot) * 20480 + (i) * 1024)

  f32x4 acc[6][4];
#pragma unroll
  for (int m = 0; m < 6; ++m)
#pragma unroll
    for (int j = 0; j < 4; ++j) acc[m][j] = f32x4{0.f, 0.f, 0.f, 0.f};
  const int aD0 = fr * 128 + ((g ^ ((fr >> 1) & 7)) * 16), aD1 = aD0 ^ 64;
  const int aM0 = (g >> 1) * 3072 + fr * 32 + (g & 1) * 16;
  int bO[4];
  {
    const int q = fr >> 2, p = lane & 3;
    const int f = ((q >> 1) & 1) | ((g & 1) << 1);
#pragma unroll
    for (int j = 0; j < 4; ++j) bO[j] = 12288 + (8 * g + q) * 128 + ((j ^ f) * 32) + p * 8;
  }
  bf16x8 fa[2][6], fb[2][4];
#define READ_UNIT(slot, a0, a1, mstride) do { ldsp p_ = my + (slot) * 20480; \
    _Pragma("unroll") for (int s = 0; s < 2; ++s) { \
      _Pragma("unroll") for (int j = 0; j < 4; ++j) fb[s][j] = tr2(p_ + bO[j] + s * 4096, p_ + bO[j] + s * 4096 + 512); \
      _Pragma("unroll") for (int m = 0; m < 6; ++m) fa[s][m] = ld128(p_ + (s ? (a1) : (a0)) + m * (mstride)); } \
    LGKM0(); SCHED0(); } while (0)
#define MMA2(i) acc[((i) % 24) >> 2][(i) & 3] = mfma32(fb[(i) / 24][(i) & 3], fa[(i) / 24][((i) % 24) >> 2], acc[((i) % 24) >> 2][(i) & 3])

#pragma unroll
  for (int i = 0; i < 20; ++i) DN_PIECE(i, 0, 0);
#pragma unroll
  for (int i = 0; i < 20; ++i) DN_PIECE(i, 1, 1);
#pragma unroll 1
  for (int v = 0; v < 4; ++v) {
    WAIT_VM(20);
    READ_UNIT(v & 1, aD0, aD1, 2048);
#pragma unroll
    for (int i = 0; i < 24; ++i) {
      if (i < 20) DN_PIECE(i, v + 2, v & 1);
      MMA2(2 * i); MMA2(2 * i + 1);
      SCHED0();
    }
  }
  WAIT_VM(20);
  READ_UNIT(0, aD0, aD1, 2048);
#pragma unroll
  for (int i = 0; i < 24; ++i) {
    if (i < 8) OMB_PIECE(i, 2 * w, 0);
    MMA2(2 * i); MMA2(2 * i + 1);
    SCHED0();
  }
  STAMPAT(1);
  {
    GAS unsigned* fl = (GAS unsigned*)(ws + WS_FLAG) + 8 * w + l7;
    for (unsigned spins = 0; spins < (1u << 22); ++spins) {
      unsigned v = __hip_atomic_load(fl, __ATOMIC_RELAXED, __HIP_MEMORY_SCOPE_AGENT);
      if (__builtin_amdgcn_ballot_w64(v == 1u) == ~0ull) break;
      __builtin_amdgcn_s_sleep(2);
    }
    MEMFENCE();
  }
  STAMPAT(2);
#pragma unroll
  for (int i = 0; i < 12; ++i) OMA_PIECE(i, 2 * w, 0);
  WAIT_VM(12);
  READ_UNIT(1, aD0, aD1, 2048);
#pragma unroll
  for (int i = 0; i < 24; ++i) {
    if (i < 12) OMA_PIECE(i, 2 * w + 1, 1);
    else if (i < 20) OMB_PIECE(i - 12, 2 * w + 1, 1);
    MMA2(2 * i); MMA2(2 * i + 1);
    SCHED0();
  }
  WAIT_VM(20);
  READ_UNIT(0, aM0, aM0 + 6144, 512);
  {
    const unsigned vO = (lane < 48) ? (unsigned)(lane * 16) : 0x40000000u;
#pragma unroll
    for (int i = 0; i < 24; ++i) {
      if (i < 8) dma_b_sc1(srd_or, vO, ((8 * w + i) * S_ + r0) * 8, smb + i * 1024);
      else if (i < 12) dma_b(srd_wo, vR[i & 1], (160 * w + 40 * (i - 8)) * 1280 + c0 * 2, smb + 12288 + (i - 8) * 1024);
      MMA2(2 * i); MMA2(2 * i + 1);
      SCHED0();
    }
  }
  WAIT_VM(12);
  READ_UNIT(1, aM0, aM0 + 6144, 512);
#pragma unroll
  for (int i = 0; i < 48; ++i) MMA2(i);
  SCHED0();
  WAIT_VM(0);
  {
#pragma unroll
    for (int j = 0; j < 4; ++j) fb[0][j] = tr2(my + bO[j], my + bO[j] + 512);
#pragma unroll
    for (int m = 0; m < 6; ++m) {
      s16x4 lo = *(const LAS s16x4*)(my + (2 * g) * 1024 + (m * 16 + fr) * 8);
      s16x4 hi = *(const LAS s16x4*)(my + (2 * g + 1) * 1024 + (m * 16 + fr) * 8);
      fa[0][m] = cat(lo, hi);
    }
    LGKM0(); SCHED0();
#pragma unroll
    for (int i = 0; i < 24; ++i) MMA2(i);
  }
  STAMPAT(3);

  u32x4 xr[3];
#pragma unroll
  for (int i = 0; i < 3; ++i) {
    int row = r0 + (i * 4 + w) * 8 + l3;
    if (row > S_ - 1) row = S_ - 1;
    xr[i] = *(const u32x4*)(x + (size_t)row * 1280 + c0 * 2 + l7 * 16);
  }
#pragma unroll
  for (int m = 0; m < 6; ++m)
#pragma unroll
    for (int j = 0; j < 4; ++j) *(LAS f32x4*)(my + (m * 16 + fr) * 272 + j * 64 + g * 16) = acc[m][j];
  LGKM_BAR();
#pragma unroll
  for (int i = 0; i < 3; ++i) {
    int rl = (i * 4 + w) * 8 + l3;
    float s[8];
#pragma unroll
    for (int e = 0; e < 8; ++e) s[e] = 0.f;
#pragma unroll
    for (int ww = 0; ww < 4; ++ww) {
      f32x4 a = *(const LAS f32x4*)(sm + ww * W_REG + rl * 272 + l7 * 32);
      f32x4 bb = *(const LAS f32x4*)(sm + ww * W_REG + rl * 272 + l7 * 32 + 16);
#pragma unroll
      for (int e = 0; e < 4; ++e) { s[e] += a[e]; s[4 + e] += bb[e]; }
    }
    u32x4 o;
#pragma unroll
    for (int e = 0; e < 4; ++e) o[e] = pk2(s[2 * e] + u2f(xr[i][e] << 16), s[2 * e + 1] + u2f(xr[i][e] & 0xffff0000u));
    if (r0 + rl < S_) *(u32x4*)(out + (size_t)(r0 + rl) * 1280 + c0 * 2 + l7 * 16) = o;
  }
}

#if defined(__HIP_DEVICE_COMPILE__)
#pragma clang attribute pop
#endif

extern "C" void kernel_launch(void* const* d_in, const int* in_sizes, int n_in, void* d_out, int out_size, void* d_ws,
                              size_t ws_size, hipStream_t stream) {
  const char* x = (const char*)d_in[0];
  const char* wqkv = (const char*)d_in[1];
  const char* wphi = (const char*)d_in[2];
  const char* wo = (const char*)d_in[3];
  const char* wg = (const char*)d_in[5];
  const char* wu = (const char*)d_in[6];
  const char* wd = (const char*)d_in[7];
  (void)hipFuncSetAttribute((const void*)k1, hipFuncAttributeMaxDynamicSharedMemorySize, K1_LDS);
  (void)hipFuncSetAttribute((const void*)k2, hipFuncAttributeMaxDynamicSharedMemorySize, K2_LDS);
  k1<<<256, 512, K1_LDS, stream>>>(x, wqkv, wphi, wg, wu, (char*)d_ws);
  k2<<<256, 256, K2_LDS, stream>>>(x, wo, wd, (char*)d_ws, (char*)d_out);
}
```

```cpp
#include <hip/hip_runtime.h>
#include <hip/hip_bf16.h>
#include <stdint.h>

typedef __attribute__((ext_vector_type(8))) __bf16 bf16x8;
typedef __attribute__((ext_vector_type(2))) __bf16 bf16x2;
typedef __attribute__((ext_vector_type(4))) short s16x4;
typedef __attribute__((ext_vector_type(8))) short s16x8;
typedef __attribute__((ext_vector_type(4))) float f32x4;
typedef __attribute__((ext_vector_type(2))) float f32x2;
typedef __attribute__((ext_vector_type(4))) unsigned u32x4;
typedef __attribute__((ext_vector_type(2))) unsigned u32x2;
#define LAS __attribute__((address_space(3)))
#define DI __device__ __forceinline__
typedef LAS char* ldsp;

constexpr int S_ = 2048, D_ = 640, DF_ = 1536, NH = 32;
constexpr int NRG = 12;
constexpr size_t WS_H = 0;
constexpr size_t WS_KV = WS_H + (size_t)S_ * DF_ * 2;
constexpr size_t WS_OM = WS_KV + (size_t)NRG * NH * 3072;
constexpr size_t WS_OR = WS_OM + (size_t)NH * S_ * 32;
constexpr size_t WS_REQ = WS_OR + (size_t)NH * S_ * 8;
constexpr size_t WS_ACK = WS_REQ + 256 * 64;
constexpr size_t WS_DONE = WS_ACK + 256 * 64;
constexpr size_t WS_STAT = WS_DONE + 256 * 64;
constexpr size_t WS_HF = WS_STAT + 12 * 176 * 16;
constexpr size_t WS_KF = WS_HF + 12 * 128;
constexpr size_t WS_OF = WS_KF + 8 * 128;
constexpr size_t WS_DBG = 64u << 20;

constexpr int A_SLOT = 22528, B_SLOT = 16384;
constexpr int L_A = 0;
constexpr int L_B = 3 * A_SLOT;
constexpr int L_MISC = L_B + 5 * B_SLOT;
constexpr int L_DUMMY = L_MISC;
constexpr int L_NONCE = L_MISC + 1024;
constexpr int L_CS = L_NONCE + 64;
constexpr int K_LDS = L_CS + 2048;
constexpr int P2_A = 0, P2_B = 98304;
constexpr unsigned SPIN_MAX = 1u << 20;

#if defined(__HIP_DEVICE_COMPILE__)
#pragma clang attribute push(__attribute__((target("no-packed-fp32-ops"))), apply_to = function)
#endif

#define STAMPAT(i)

DI float u2f(unsigned u) { return __builtin_bit_cast(float, u); }
DI unsigned f2u(float f) { return __builtin_bit_cast(unsigned, f); }
DI u32x4 make_srd(const void* p, unsigned bytes) {
  uint64_t a = (uint64_t)p;
  u32x4 r;
  r[0] = (unsigned)a;
  r[1] = (unsigned)(a >> 32) & 0xffffu;
  r[2] = bytes;
  r[3] = 0x00020000u;
  return r;
}
DI void dma_b(u32x4 srd, unsigned voff, unsigned soff, unsigned m0v) {
  asm volatile("s_mov_b32 m0, %3\n\ts_nop 0\n\tbuffer_load_dwordx4 %0, %1, %2 offen lds" ::"v"(voff), "s"(srd), "s"(soff), "s"(m0v) : "memory");
}
DI void dma_b_mask(u32x4 srd, unsigned voff, unsigned soff, unsigned m0v, unsigned long long mask) {
  asm volatile("s_mov_b32 m0, %3\n\ts_mov_b64 exec, %4\n\tbuffer_load_dwordx4 %0, %1, %2 offen lds\n\ts_mov_b64 exec, -1" ::"v"(voff), "s"(srd), "s"(soff), "s"(m0v), "s"(mask) : "memory");
}
DI void dma_b_sc1(u32x4 srd, unsigned voff, unsigned soff, unsigned m0v) {
  asm volatile("s_mov_b32 m0, %3\n\ts_nop 0\n\tbuffer_load_dwordx4 %0, %1, %2 offen sc1 lds" ::"v"(voff), "s"(srd), "s"(soff), "s"(m0v) : "memory");
}
DI u32x4 ld128_sc1(const char* p) { u32x4 v; asm volatile("global_load_dwordx4 %0, %1, off sc1\n\ts_waitcnt vmcnt(0)" : "=&v"(v) : "v"(p) : "memory"); return v; }
DI u32x2 ld64_sc1(const char* p) { u32x2 v; asm volatile("global_load_dwordx2 %0, %1, off sc1\n\ts_waitcnt vmcnt(0)" : "=&v"(v) : "v"(p) : "memory"); return v; }
DI void st128_sc1(char* p, u32x4 v) { asm volatile("global_store_dwordx4 %0, %1, off sc1\n\ts_nop 1" ::"v"(p), "v"(v) : "memory"); }
DI void st64_sc1(char* p, u32x2 v) { asm volatile("global_store_dwordx2 %0, %1, off sc1" ::"v"(p), "v"(v) : "memory"); }
#define WAITN_BAR(N) asm volatile("s_waitcnt vmcnt(%0) lgkmcnt(0)\n\ts_barrier" ::"n"(N) : "memory")
#define LGKM_BAR() asm volatile("s_waitcnt lgkmcnt(0)\n\ts_barrier" ::: "memory")
#define LGKM0() asm volatile("s_waitcnt lgkmcnt(0)" ::: "memory")
#define MEMFENCE() asm volatile("" ::: "memory")
#define SCHED0() __builtin_amdgcn_sched_barrier(0)

DI bf16x8 ld128(ldsp p) { return *(const LAS bf16x8*)p; }
DI s16x4 tr1(ldsp p) { return __builtin_amdgcn_ds_read_tr16_b64_v4i16((LAS s16x4*)p); }
DI bf16x8 cat(s16x4 a, s16x4 b) { return __builtin_bit_cast(bf16x8, __builtin_shufflevector(a, b, 0, 1, 2, 3, 4, 5, 6, 7)); }
DI bf16x8 tr2(ldsp p0, ldsp p1) { return cat(tr1(p0), tr1(p1)); }
DI f32x4 mfma32(bf16x8 a, bf16x8 b, f32x4 c) { return __builtin_amdgcn_mfma_f32_16x16x32_bf16(a, b, c, 0, 0, 0); }
DI bf16x8 asb(u32x4 v) { return __builtin_bit_cast(bf16x8, v); }
DI unsigned pk2(float a, float b) {
  bf16x2 v;
  v[0] = (__bf16)a;
  v[1] = (__bf16)b;
  return __builtin_bit_cast(unsigned, v);
}
DI void swap16(float& a, float& b) {
  auto r = __builtin_amdgcn_permlane16_swap(f2u(a), f2u(b), false, false);
  a = u2f(r[0]); b = u2f(r[1]);
}
DI void swap32(float& a, float& b) {
  auto r = __builtin_amdgcn_permlane32_swap(f2u(a), f2u(b), false, false);
  a = u2f(r[0]); b = u2f(r[1]);
}
template <int CTRL> DI float dpp(float x) { return u2f(__builtin_amdgcn_update_dpp(0u, f2u(x), CTRL, 0xf, 0xf, false)); }
DI float fmx(float a, float b) { return __builtin_fmaxf(a, b); }
DI float fmn(float a, float b) { return __builtin_fminf(a, b); }
DI float rawmax(float a, float b) { float r; asm("v_max_f32 %0, %1, %2" : "=v"(r) : "v"(a), "v"(b)); return r; }
DI int rs_of(int rg) { return (512 * rg) / 3; }
DI int rg_of(int row) { return (3 * row + 2) >> 9; }
DI bool all_ok(bool ok) { return __builtin_amdgcn_ballot_w64(ok) == ~0ull; }

DI void wait_flags(const char* p, bool need, u32x2 ep) {
  for (unsigned spins = 0; spins < SPIN_MAX; ++spins) {
    u32x2 v = ld64_sc1(p);
    if (all_ok(!need || (v[0] == ep[0] && v[1] == ep[1]))) break;
    __builtin_amdgcn_s_sleep(2);
  }
}

DI void featmap2(const float (&ya)[5], const float (&yb)[5], bool livea, bool liveb, int g, unsigned (&oa)[5], unsigned (&ob)[5]) {
  float a0 = fmx(fmx(fmx(ya[0], ya[1]), fmx(ya[2], ya[3])), ya[4]);
  float a1 = -fmn(fmn(fmn(ya[0], ya[1]), fmn(ya[2], ya[3])), ya[4]);
  float b0 = fmx(fmx(fmx(yb[0], yb[1]), fmx(yb[2], yb[3])), yb[4]);
  float b1 = -fmn(fmn(fmn(yb[0], yb[1]), fmn(yb[2], yb[3])), yb[4]);
  swap16(a0, a1); swap16(b0, b1);
  float ca = rawmax(a0, a1), cb = rawmax(b0, b1);
  swap32(ca, cb);
  float d0 = rawmax(ca, cb), d1 = d0;
  swap32(d0, d1);
  float Ma = d0, Na = d0, Mb = d1, Nb = d1;
  swap16(Ma, Na); swap16(Mb, Nb);
  float pa[5], na[5], pb[5], nb[5];
#pragma unroll
  for (int i = 0; i < 5; ++i) {
    pa[i] = __builtin_amdgcn_exp2f(ya[i] - Ma);
    na[i] = __builtin_amdgcn_exp2f(-ya[i] - Na);
    pb[i] = __builtin_amdgcn_exp2f(yb[i] - Mb);
    nb[i] = __builtin_amdgcn_exp2f(-yb[i] - Nb);
  }
  a0 = ((pa[0] + pa[1]) + (pa[2] + pa[3])) + pa[4];
  a1 = ((na[0] + na[1]) + (na[2] + na[3])) + na[4];
  b0 = ((pb[0] + pb[1]) + (pb[2] + pb[3])) + pb[4];
  b1 = ((nb[0] + nb[1]) + (nb[2] + nb[3])) + nb[4];
  swap16(a0, a1); swap16(b0, b1);
  ca = a0 + a1; cb = b0 + b1;
  swap32(ca, cb);
  d0 = __builtin_amdgcn_rcpf(ca + cb);
  if (!(g < 2 ? livea : liveb)) d0 = 0.f;
  d1 = d0;
  swap32(d0, d1);
  Ma = d0; Na = d0; Mb = d1; Nb = d1;
  swap16(Ma, Na); swap16(Mb, Nb);
  oa[0] = pk2(pa[0] * Ma, pa[1] * Ma); oa[1] = pk2(pa[2] * Ma, pa[3] * Ma);
  oa[2] = pk2(na[0] * Na, na[1] * Na); oa[3] = pk2(na[2] * Na, na[3] * Na);
  oa[4] = pk2(pa[4] * Ma, na[4] * Na);
  ob[0] = pk2(pb[0] * Mb, pb[1] * Mb); ob[1] = pk2(pb[2] * Mb, pb[3] * Mb);
  ob[2] = pk2(nb[0] * Nb, nb[1] * Nb); ob[3] = pk2(nb[2] * Nb, nb[3] * Nb);
  ob[4] = pk2(pb[4] * Mb, nb[4] * Nb);
}

struct P2Ctx { unsigned vB, vR; int c0; };
DI unsigned p2_vB(int w, int lane) {
  const int l3 = lane >> 3, l7 = lane & 7;
  int fb = ((l3 >> 1) & 1) | ((w & 1) << 1);
  int ch = (((l7 >> 1) ^ fb) << 1) | (l7 & 1);
  return l3 * 1280 + ch * 16;
}
DI void p2_issueB_down(u32x4 srd_wd, unsigned vB, int c0, int w, unsigned smb, int s) {
  dma_b(srd_wd, vB, (128 * s + 8 * w) * 1280 + c0 * 2, smb + P2_B + (s % 3) * 16384 + w * 1024);
  dma_b(srd_wd, vB, (128 * s + 8 * w + 64) * 1280 + c0 * 2, smb + P2_B + (s % 3) * 16384 + w * 1024 + 8192);
}

DI void phase2(ldsp sm, const char* x, const char* wo, const u32x4 srd_wd, char* ws, char* out, int rt, int ct, int w, int lane, u32x2 ep) {
  const int r0 = rt * 128, c0 = ct * 64;
  const int g = lane >> 4, fr = lane & 15, l3 = lane >> 3, l7 = lane & 7;
  const int kq = w & 3, mh = w >> 2;
  const unsigned smb = (unsigned)(size_t)sm;
  const u32x4 srd_h = make_srd(ws + WS_H, S_ * DF_ * 2);
  const u32x4 srd_wo = make_srd(wo, D_ * D_ * 2);
  const u32x4 srd_om = make_srd(ws + WS_OM, NH * S_ * 32);
  const u32x4 srd_or = make_srd(ws + WS_OR, NH * S_ * 8);
  const unsigned vB = p2_vB(w, lane);
  const unsigned vA = l3 * 3072 + ((l7 ^ ((4 * (w & 1) + (l3 >> 1)) & 7)) * 16);
  const unsigned vR = vB - l3 * 1280 + (20 * (lane >> 5) + 16 + (l3 & 3)) * 1280;
  const unsigned vL = lane * 16;
  const int rgl = rg_of(r0), rgh = rg_of(r0 + 127);

#define P2_A_DOWN(s) do { _Pragma("unroll") for (int i_ = 0; i_ < 4; ++i_) \
    dma_b_sc1(srd_h, vA, (r0 + 8 * w + 64 * (i_ & 1)) * 3072 + (s) * 256 + (i_ >> 1) * 128, smb + P2_A + ((s) % 3) * 32768 + (i_ >> 1) * 16384 + (i_ & 1) * 8192 + w * 1024); } while (0)
#define P2_A_OM(s) do { _Pragma("unroll") for (int i_ = 0; i_ < 4; ++i_) \
    dma_b_sc1(srd_om, vL, ((8 * ((s) - 12) + 2 * i_ + (w >> 2)) * S_ + r0) * 32 + (w & 3) * 1024, smb + P2_A + ((s) % 3) * 32768 + i_ * 8192 + w * 1024); } while (0)
#define P2_B_OM(s) do { _Pragma("unroll") for (int i_ = 0; i_ < 2; ++i_) \
    dma_b(srd_wo, vB, (160 * ((s) - 12) + 20 * ((w + 8 * i_) >> 1) + 8 * (w & 1)) * 1280 + c0 * 2, smb + P2_B + ((s) % 3) * 16384 + i_ * 8192 + w * 1024); } while (0)
#define P2_A_OR(s) do { _Pragma("unroll") for (int i_ = 0; i_ < 4; ++i_) \
    dma_b_sc1(srd_or, vL, ((8 * i_ + w) * S_ + r0) * 8, smb + P2_A + ((s) % 3) * 32768 + i_ * 8192 + w * 1024); } while (0)
#define P2_B_OR(s) do { _Pragma("unroll") for (int i_ = 0; i_ < 2; ++i_) \
    dma_b(srd_wo, vR, (40 * (w + 8 * i_)) * 1280 + c0 * 2, smb + P2_B + ((s) % 3) * 16384 + i_ * 8192 + w * 1024); } while (0)

  const int aD = P2_A + (kq >> 1) * 16384 + (64 * mh + fr) * 128 + (((4 * (kq & 1) + g) ^ ((fr >> 1) & 7)) * 16);
  const int aM = P2_A + (2 * kq + (g >> 1)) * 4096 + (64 * mh + fr) * 32 + (g & 1) * 16;
  const int aR = P2_A + (8 * kq + 2 * g) * 1024 + (64 * mh + fr) * 8;
  int bO[4];
  {
    const int q = fr >> 2, p = lane & 3;
    const int f = ((q >> 1) & 1) | ((g & 1) << 1);
#pragma unroll
    for (int j = 0; j < 4; ++j) bO[j] = P2_B + (32 * kq + 8 * g + q) * 128 + ((j ^ f) * 32) + p * 8;
  }
  f32x4 acc[4][4];
#pragma unroll
  for (int m = 0; m < 4; ++m)
#pragma unroll
    for (int j = 0; j < 4; ++j) acc[m][j] = f32x4{0.f, 0.f, 0.f, 0.f};

  STAMPAT(8);
  wait_flags(ws + WS_HF + (size_t)(lane < 16 ? rgl : rgh) * 128 + (lane & 15) * 8, lane < 32 && (lane & 15) < 12, ep);
  STAMPAT(9);
  P2_A_DOWN(0); P2_A_DOWN(1); P2_A_DOWN(2);
  u32x4 xr[2];
#pragma unroll
  for (int s = 0; s < 17; ++s) {
    SCHED0();
    WAITN_BAR(s == 0 ? 8 : (s == 1 ? 4 : (s < 16 ? 6 : 0)));
    SCHED0();
    bf16x8 fa[4], fb[4];
#pragma unroll
    for (int j = 0; j < 4; ++j) fb[j] = tr2(sm + (s % 3) * 16384 + bO[j], sm + (s % 3) * 16384 + bO[j] + 512);
#pragma unroll
    for (int m = 0; m < 4; ++m) {
      if (s < 12) fa[m] = ld128(sm + (s % 3) * 32768 + aD + m * 2048);
      else if (s < 16) fa[m] = ld128(sm + (s % 3) * 32768 + aM + m * 512);
      else fa[m] = cat(*(const LAS s16x4*)(sm + (s % 3) * 32768 + aR + m * 128), *(const LAS s16x4*)(sm + (s % 3) * 32768 + aR + 1024 + m * 128));
    }
    SCHED0();
    if (s >= 1 && s + 2 < 17) {
      constexpr int dummy_ = 0; (void)dummy_;
      if (s + 2 < 12) { p2_issueB_down(srd_wd, vB, c0, w, smb, s + 2); P2_A_DOWN(s + 2); }
      else if (s + 2 < 16) {
        P2_B_OM(s + 2);
        if (s + 2 == 12) {
          STAMPAT(10);
          wait_flags(ws + WS_OF + (size_t)(lane < 16 ? rgl : rgh) * 128 + (lane & 15) * 8, lane < 32 && (lane & 15) < 8, ep);
          STAMPAT(11);
        }
        P2_A_OM(s + 2);
      } else { P2_B_OR(s + 2); P2_A_OR(s + 2); }
    }
    SCHED0();
#pragma unroll
    for (int m = 0; m < 4; ++m)
#pragma unroll
      for (int j = 0; j < 4; ++j) acc[m][j] = mfma32(fb[j], fa[m], acc[m][j]);
  }
  SCHED0();
  STAMPAT(12);
#pragma unroll
  for (int i = 0; i < 2; ++i) xr[i] = *(const u32x4*)(x + (size_t)(r0 + (i * 8 + w) * 8 + l3) * 1280 + c0 * 2 + l7 * 16);
  LGKM_BAR();
  {
    ldsp my = sm + w * 17408;
#pragma unroll
    for (int m = 0; m < 4; ++m)
#pragma unroll
      for (int j = 0; j < 4; ++j) *(LAS f32x4*)(my + (m * 16 + fr) * 272 + j * 64 + g * 16) = acc[m][j];
  }
  LGKM_BAR();
#pragma unroll
  for (int i = 0; i < 2; ++i) {
    const int rl = w * 8 + l3;
    float s[8];
#pragma unroll
    for (int e = 0; e < 8; ++e) s[e] = 0.f;
#pragma unroll
    for (int kk = 0; kk < 4; ++kk) {
      f32x4 a = *(const LAS f32x4*)(sm + (i * 4 + kk) * 17408 + rl * 272 + l7 * 32);
      f32x4 bb = *(const LAS f32x4*)(sm + (i * 4 + kk) * 17408 + rl * 272 + l7 * 32 + 16);
#pragma unroll
      for (int e = 0; e < 4; ++e) { s[e] += a[e]; s[4 + e] += bb[e]; }
    }
    u32x4 o;
#pragma unroll
    for (int e = 0; e < 4; ++e) o[e] = pk2(s[2 * e] + u2f(xr[i][e] << 16), s[2 * e + 1] + u2f(xr[i][e] & 0xffff0000u));
    *(u32x4*)(out + (size_t)(r0 + i * 64 + rl) * 1280 + c0 * 2 + l7 * 16) = o;
  }
}

template <int MT, bool GU>
DI u32x2 phase1(ldsp sm, const char* x, const char* wqkv, const char* wphi, const char* wg, const char* wu, const u32x4 srd_wd,
                char* ws, int rg, int ct, int c0p2, int w, int lane) {
  STAMPAT(0);
  constexpr int mh = (MT == 6) ? 0 : 1;
  const int wn = w & 3;
  const int r0 = rs_of(rg), nrow = rs_of(rg + 1) - r0;
  const unsigned smb = (unsigned)(size_t)sm;
  const int g = lane >> 4, fr = lane & 15;

  const u32x4 srdx = make_srd(x, S_ * D_ * 2);
  unsigned voffA[3];
#pragma unroll
  for (int c = 0; c < 3; ++c) {
    int slot = 8 * (w + 8 * c) + (lane >> 3);
    int ch = (lane & 7) ^ ((slot >> 1) & 7);
    int ri = slot < 95 ? slot : slot - 1;
    ri = ri < nrow ? ri : nrow - 1;
    voffA[c] = (unsigned)((r0 + ri) * 1280 + ch * 16);
  }
  if (w >= 6) voffA[2] = 0x40000000u;
  const unsigned long long FULL = ~0ull, PART = 0x00FFFFFFFFFFFFFFull;
  const unsigned long long mask1 = (w == 3) ? PART : FULL, mask2 = (w == 5) ? PART : FULL;
  const unsigned adst0 = smb + L_A + w * 1024;
  u32x4 srd0, srd1;
  unsigned voff0, voff1;
  constexpr unsigned SSTEP = GU ? 32 * 3072 : 32 * 3840;
  if (GU) {
    srd0 = make_srd(wg, D_ * DF_ * 2);
    srd1 = make_srd(wu, D_ * DF_ * 2);
    int krow = 4 * w + (lane >> 4);
    int chp = lane & 15;
    int f = (krow & 3) | (((krow >> 3) & 1) << 2);
    int ch = (((chp >> 1) ^ f) << 1) | (chp & 1);
    voff0 = voff1 = krow * 3072 + ct * 256 + ch * 16;
  } else {
    srd0 = srd1 = make_srd(wqkv, D_ * 3 * D_ * 2);
    unsigned vo[2];
#pragma unroll
    for (int c = 0; c < 2; ++c) {
      int cidx = (w + 8 * c) * 64 + lane;
      int rho = cidx / 30, cc = cidx % 30;
      int seg = cc / 10, ch = cc % 10;
      int k = (rho & 16) | ((rho & 4) << 1) | ((rho & 8) >> 1) | (rho & 3);
      vo[c] = (cidx < 960) ? (unsigned)(k * 3840 + seg * 1280 + ct * 160 + ch * 16) : 0x40000000u;
    }
    voff0 = vo[0];
    voff1 = vo[1];
  }
  const unsigned bdst0 = smb + L_B + w * 1024;
#define DMA_B0(soff, dst) dma_b(srd0, voff0, (soff), (dst))
#define DMA_B1(soff, dst) dma_b(srd1, voff1, (soff), (dst))
#define DMA_A0(soff, dst) dma_b(srdx, voffA[0], (soff), (dst))
#define DMA_A1(soff, dst) dma_b_mask(srdx, voffA[1], (soff), (dst), mask1)
#define DMA_A2(soff, dst) dma_b_mask(srdx, voffA[2], (soff), (dst), mask2)
  const unsigned a2base = (w >= 6) ? smb + L_DUMMY : adst0 + 16384;
  const unsigned a2mul = (w >= 6) ? 0u : 1u;
  DMA_B0(0, bdst0); DMA_B1(0, bdst0 + 8192);
  DMA_A0(0, adst0); DMA_A1(0, adst0 + 8192);
  DMA_A2(0, a2base);
  DMA_B0(SSTEP, bdst0 + B_SLOT); DMA_B1(SSTEP, bdst0 + B_SLOT + 8192);
  DMA_B0(2 * SSTEP, bdst0 + 2 * B_SLOT); DMA_B1(2 * SSTEP, bdst0 + 2 * B_SLOT + 8192);
  DMA_A0(128, adst0 + A_SLOT); DMA_A1(128, adst0 + A_SLOT + 8192);
  DMA_A2(128, a2base + a2mul * A_SLOT);
  DMA_B0(3 * SSTEP, bdst0 + 3 * B_SLOT); DMA_B1(3 * SSTEP, bdst0 + 3 * B_SLOT + 8192);
  DMA_B0(4 * SSTEP, bdst0 + 4 * B_SLOT); DMA_B1(4 * SSTEP, bdst0 + 4 * B_SLOT + 8192);
  DMA_A0(256, adst0 + 2 * A_SLOT); DMA_A1(256, adst0 + 2 * A_SLOT + 8192);
  if (w == 0 && lane < 48) {
    int rr = lane >> 3;
    *(LAS u32x4*)(sm + L_A + (rr >> 1) * A_SLOT + ((rr & 1) ? 175 : 95) * 128 + (lane & 7) * 16) =
        u32x4{0x3F803F80u, 0x3F803F80u, 0x3F803F80u, 0x3F803F80u};
  }
  const int bid = __builtin_amdgcn_workgroup_id_x();
  if (w == 1) {
    unsigned long long nn = __builtin_amdgcn_s_memrealtime();
    u32x2 nv = u32x2{(unsigned)nn, (unsigned)(nn >> 32)};
    if (lane == 0) {
      st64_sc1(ws + WS_REQ + bid * 64, nv);
      *(LAS u32x2*)(sm + L_NONCE) = nv;
    }
  }

  const int aoff0 = L_A + mh * 6 * 2048 + fr * 128 + ((g ^ ((fr >> 1) & 7)) * 16);
  const int aoff1 = aoff0 ^ 64;
  int boff[4];
  constexpr int TSTEP = GU ? 1024 : 3840;
  {
    const int q = fr >> 2, p = lane & 3;
    if (GU) {
      int f = q | ((g & 1) << 2);
#pragma unroll
      for (int j = 0; j < 4; ++j)
        boff[j] = L_B + (j >> 1) * 8192 + (8 * g + q) * 256 + (((2 * wn + (j & 1)) ^ f) * 32) + p * 8;
    } else {
      int rho = 16 * (g >> 1) + 4 * (g & 1) + q;
#pragma unroll
      for (int j = 0; j < 3; ++j) boff[j] = L_B + rho * 480 + (20 * j + 5 * wn + p) * 8;
      boff[3] = L_B + rho * 480 + (20 * (p == 3 ? 0 : p) + 5 * wn + 4) * 8;
    }
  }

  f32x4 acc[MT][4];
#pragma unroll
  for (int m = 0; m < MT; ++m)
#pragma unroll
    for (int j = 0; j < 4; ++j) acc[m][j] = f32x4{0.f, 0.f, 0.f, 0.f};
  bf16x8 fa0[MT], fa1[MT];
  s16x4 fb0[8], fb1[8];

  STAMPAT(1);
  WAITN_BAR(13);
  STAMPAT(2);
#pragma unroll
  for (int m = 0; m < MT; ++m) fa0[m] = ld128(sm + aoff0 + m * 2048);
#pragma unroll
  for (int j = 0; j < 8; ++j) fb0[j] = tr1(sm + boff[j >> 1] + (j & 1) * TSTEP);
  MEMFENCE();

  constexpr int NM = MT * 4;
  constexpr int DS = (MT == 6) ? 6 : 5;
#define MMA(i, FA, FB) acc[(i) >> 2][(i) & 3] = mfma32(cat(FB[2 * ((i) & 3)], FB[2 * ((i) & 3) + 1]), FA[(i) >> 2], acc[(i) >> 2][(i) & 3])
#define BADDR(sl, i) (sm + (sl) * B_SLOT + boff[(i) >> 1] + ((i) & 1) * TSTEP)
  u32x4 ackv, stv[MT];
  const char* ackp = ws + WS_ACK + bid * 64;
  const char* stp = ws + WS_STAT + (size_t)(rg * 176 + mh * 96 + fr) * 16;
#pragma unroll
  for (int t = 0; t < 10; ++t) {
    SCHED0();
    WAITN_BAR(t < 8 ? 11 : (t == 8 ? 7 : 1 + MT));
    SCHED0();
#pragma unroll
    for (int i = 0; i < NM; ++i) {
      MMA(i, fa0, fb0);
      SCHED0();
      if (i < 8) fb1[i] = tr1(BADDR((2 * t + 1) % 5, i));
      else if (i < 8 + MT) fa1[i - 8] = ld128(sm + (t % 3) * A_SLOT + aoff1 + (i - 8) * 2048);
      if (t < 8) {
        if (i == 2) { if (MT == 6) DMA_A2((t + 2) * 128, adst0 + 16384 + ((t + 2) % 3) * A_SLOT); else DMA_A2((t + 2) * 128, a2base + a2mul * (((t + 2) % 3) * A_SLOT)); }
        if (i == 2 + DS) DMA_B0((2 * t + 5) * SSTEP, bdst0 + ((2 * t) % 5) * B_SLOT);
        if (i == 2 + 2 * DS) DMA_B1((2 * t + 5) * SSTEP, bdst0 + ((2 * t) % 5) * B_SLOT + 8192);
      }
      SCHED0();
    }
    if (t < 9) WAITN_BAR(t < 8 ? 9 : 2); else LGKM_BAR();
    SCHED0();
#pragma unroll
    for (int i = 0; i < NM; ++i) {
      MMA(i, fa1, fb1);
      SCHED0();
      if (t < 9) {
        if (i < 8) fb0[i] = tr1(BADDR((2 * t + 2) % 5, i));
        else if (i < 8 + MT) fa0[i - 8] = ld128(sm + ((t + 1) % 3) * A_SLOT + aoff0 + (i - 8) * 2048);
      }
      if (t < 7) {
        if (i == 1) DMA_B0((2 * t + 6) * SSTEP, bdst0 + ((2 * t + 1) % 5) * B_SLOT);
        if (i == 1 + DS) DMA_B1((2 * t + 6) * SSTEP, bdst0 + ((2 * t + 1) % 5) * B_SLOT + 8192);
        if (i == 1 + 2 * DS) DMA_A0((t + 3) * 128, adst0 + (t % 3) * A_SLOT);
        if (i == 1 + 3 * DS) DMA_A1((t + 3) * 128, adst0 + (t % 3) * A_SLOT + 8192);
      }
      if (i == 4 && t == 8) {
        asm volatile("global_load_dwordx4 %0, %1, off sc1" : "=&v"(ackv) : "v"(ackp) : "memory");
#pragma unroll
        for (int m = 0; m < MT; ++m) asm volatile("global_load_dwordx4 %0, %1, off offset:%2 sc1" : "=&v"(stv[m]) : "v"(stp), "n"(m * 256) : "memory");
      }
      SCHED0();
    }
  }
  SCHED0();
  STAMPAT(3);

  if (fr == 15) {
#pragma unroll
    for (int j = 0; j < 4; ++j) *(LAS f32x4*)(sm + L_CS + w * 256 + j * 64 + g * 16) = acc[MT - 1][j];
  }
  if (MT == 6) asm volatile("s_waitcnt vmcnt(0) lgkmcnt(0)" : "+v"(ackv), "+v"(stv[0]), "+v"(stv[1]), "+v"(stv[2]), "+v"(stv[3]), "+v"(stv[4]), "+v"(stv[MT - 1]) :: "memory");
  else asm volatile("s_waitcnt vmcnt(0) lgkmcnt(0)" : "+v"(ackv), "+v"(stv[0]), "+v"(stv[1]), "+v"(stv[2]), "+v"(stv[3]), "+v"(stv[4]) :: "memory");
  f32x4 cs[4];
#pragma unroll
  for (int j = 0; j < 4; ++j) cs[j] = *(const LAS f32x4*)(sm + L_CS + w * 256 + j * 64 + g * 16);
  {
    const u32x2 nv = *(const LAS u32x2*)(sm + L_NONCE);
    for (unsigned spins = 0; spins < SPIN_MAX; ++spins) {
      bool ok = (ackv[0] == nv[0]) && (ackv[1] == nv[1]);
#pragma unroll
      for (int m = 0; m < MT; ++m) ok = ok && (stv[m][2] == ackv[2]) && (stv[m][3] == ackv[3]);
      if (all_ok(ok)) break;
      __builtin_amdgcn_s_sleep(4);
      ackv = ld128_sc1(ackp);
#pragma unroll
      for (int m = 0; m < MT; ++m) stv[m] = ld128_sc1(stp + m * 256);
    }
  }
  const u32x2 ep = u32x2{ackv[2], ackv[3]};
  if (w == 0 && lane == 0) st64_sc1(ws + WS_DONE + bid * 64, ep);
  if (GU) {
    const unsigned vB = p2_vB(w, lane);
    p2_issueB_down(srd_wd, vB, c0p2, w, smb, 0);
    p2_issueB_down(srd_wd, vB, c0p2, w, smb, 1);
    p2_issueB_down(srd_wd, vB, c0p2, w, smb, 2);
  }
  float rstd[MT];
#pragma unroll
  for (int m = 0; m < MT; ++m) {
    const float mean = u2f(stv[m][0]);
    rstd[m] = u2f(stv[m][1]);
#pragma unroll
    for (int j = 0; j < 4; ++j)
#pragma unroll
      for (int r = 0; r < 4; ++r) acc[m][j][r] = __builtin_fmaf(-mean, cs[j][r], acc[m][j][r]);
  }
  STAMPAT(4);

  if (GU) {
    const float K0 = -2.0f * 0.7978845608028654f * 1.4426950408889634f;
    const float K1c = K0 * 0.044715f;
    u32x2 hp[MT][2];
#pragma unroll
    for (int m = 0; m < MT; ++m) {
      const float r = rstd[m], r2 = r * r;
      const float c0 = K0 * r, c1 = K1c * r2 * r, ir2 = __builtin_amdgcn_rcpf(r2);
#pragma unroll
      for (int j = 0; j < 2; ++j) {
        float hv[4];
#pragma unroll
        for (int q = 0; q < 4; ++q) {
          float a = acc[m][j][q], b = acc[m][j + 2][q];
          float e = __builtin_amdgcn_exp2f(a * __builtin_fmaf(a * a, c1, c0));
          hv[q] = a * b * __builtin_amdgcn_rcpf(__builtin_fmaf(e, ir2, ir2));
        }
        hp[m][j] = u32x2{pk2(hv[0], hv[1]), pk2(hv[2], hv[3])};
      }
    }
    SCHED0();
#pragma unroll
    for (int m = 0; m < MT; ++m) {
      int slot = (mh * 6 + m) * 16 + fr;
#pragma unroll
      for (int j = 0; j < 2; ++j) {
        int chunk = 4 * wn + 2 * j + (g >> 1);
        *(LAS u32x2*)(sm + slot * 256 + ((chunk ^ fr) * 16) + (g & 1) * 8) = hp[m][j];
      }
    }
    LGKM_BAR();
    STAMPAT(5);
    char* hg = ws + WS_H;
    u32x4 hv[6];
#pragma unroll
    for (int i = 0; i < 6; ++i) {
      int s = 4 * (w + 8 * i) + g;
      s = s < 176 ? s : 175;
      hv[i] = *(const LAS u32x4*)(sm + s * 256 + ((fr ^ (s & 15)) * 16));
    }
#pragma unroll
    for (int i = 0; i < 6; ++i) {
      int s = 4 * (w + 8 * i) + g;
      int ri = s < 95 ? s : s - 1;
      if (s != 95 && ri < nrow) st128_sc1(hg + (size_t)(r0 + ri) * 3072 + ct * 256 + fr * 16, hv[i]);
    }
    WAITN_BAR(0);
    if (w == 0 && lane == 0) st64_sc1(ws + WS_HF + rg * 128 + ct * 8, ep);
    STAMPAT(6);
  } else {
    const int head = ct * 4 + wn;
    bf16x8 wq[2], wk[2];
    {
      const unsigned short* wp = (const unsigned short*)(wphi + head * 800);
#pragma unroll
      for (int ft = 0; ft < 2; ++ft) {
        int f = (ft == 0) ? (5 * (fr >> 2) + (fr & 3)) : (5 * (fr >> 2) + 4);
        bool valid = (ft == 0) || ((fr & 3) == 0);
        s16x8 a, b;
#pragma unroll
        for (int j = 0; j < 4; ++j) {
          short lo = (short)wp[(4 * g + j) * 20 + f];
          short hi = (short)wp[(16 + j) * 20 + f];
          lo = valid ? lo : (short)0;
          hi = valid ? hi : (short)0;
          a[j] = lo; b[j] = lo;
          a[4 + j] = (g == 0) ? hi : (short)0;
          b[4 + j] = (g == 1) ? hi : (short)0;
        }
        wq[ft] = __builtin_bit_cast(bf16x8, a);
        wk[ft] = __builtin_bit_cast(bf16x8, b);
      }
    }
    const float L2E = 1.4426950408889634f;
    const f32x4 z4 = f32x4{0.f, 0.f, 0.f, 0.f};
    bool live[6];
    u32x2 rem[6];
#pragma unroll
    for (int m = 0; m < 6; ++m) {
      int s = mh * 96 + m * 16 + fr;
      int ri = s < 95 ? s : s - 1;
      live[m] = (m < MT) && (s != 95) && (ri < nrow);
      if (m < MT) rem[m] = u32x2{pk2(acc[m][3][0], acc[m][3][1]), pk2(acc[m][3][2], acc[m][3][3])};
    }
    ldsp pk_img = sm + w * 17408;
    ldsp v_img = pk_img + 96 * 104;
    {
      unsigned kf[6][5];
      u32x2 vv[6], ve[6];
#pragma unroll
      for (int mp = 0; mp < 3; ++mp) {
        float y[2][5];
#pragma unroll
        for (int hh = 0; hh < 2; ++hh) {
          const int m = 2 * mp + hh;
          if (m < MT) {
            const float r = rstd[m], c = r * L2E;
            u32x4 kb = u32x4{pk2(acc[m][1][0], acc[m][1][1]), pk2(acc[m][1][2], acc[m][1][3]), rem[m][0], rem[m][1]};
            f32x4 k0 = mfma32(wk[0], asb(kb), z4);
            f32x4 k1 = mfma32(wk[1], asb(kb), z4);
            y[hh][0] = k0[0] * c; y[hh][1] = k0[1] * c; y[hh][2] = k0[2] * c; y[hh][3] = k0[3] * c; y[hh][4] = k1[0] * c;
            vv[m] = u32x2{pk2(acc[m][2][0] * r, acc[m][2][1] * r), pk2(acc[m][2][2] * r, acc[m][2][3] * r)};
            u32x2 vr = u32x2{pk2(acc[m][3][0] * r, acc[m][3][1] * r), pk2(acc[m][3][2] * r, acc[m][3][3] * r)};
            ve[m] = (g == 2) ? vr : u32x2{0x3F803F80u, 0x3F803F80u};
          } else {
#pragma unroll
            for (int i = 0; i < 5; ++i) y[hh][i] = 0.f;
            vv[m] = u32x2{0u, 0u}; ve[m] = vv[m];
          }
        }
        featmap2(y[0], y[1], live[2 * mp], live[2 * mp + 1], g, kf[2 * mp], kf[2 * mp + 1]);
      }
      SCHED0();
#pragma unroll
      for (int m = 0; m < 6; ++m) {
        int tok = m * 16 + fr;
        *(LAS u32x2*)(pk_img + tok * 104 + g * 8) = u32x2{kf[m][0], kf[m][1]};
        *(LAS u32x2*)(pk_img + tok * 104 + 32 + g * 8) = u32x2{kf[m][2], kf[m][3]};
        *(LAS u32x2*)(pk_img + tok * 104 + 64 + g * 8) = u32x2{kf[m][4], 0u};
        *(LAS u32x2*)(v_img + tok * 72 + g * 8) = vv[m];
        *(LAS u32x2*)(v_img + tok * 72 + 32 + (g ^ 2) * 8) = ve[m];
      }
      LGKM0();
      SCHED0();
    }
    f32x4 kv[3][2];
#pragma unroll
    for (int ft = 0; ft < 3; ++ft)
#pragma unroll
      for (int dt = 0; dt < 2; ++dt) kv[ft][dt] = z4;
    {
      const int q = fr >> 2, p = lane & 3;
#pragma unroll
      for (int s = 0; s < 3; ++s) {
        int row = 32 * s + 8 * g + q;
        bf16x8 a[3], b[2];
#pragma unroll
        for (int ft = 0; ft < 3; ++ft) a[ft] = tr2(pk_img + row * 104 + ft * 32 + p * 8, pk_img + (row + 4) * 104 + ft * 32 + p * 8);
#pragma unroll
        for (int dt = 0; dt < 2; ++dt) b[dt] = tr2(v_img + row * 72 + dt * 32 + p * 8, v_img + (row + 4) * 72 + dt * 32 + p * 8);
#pragma unroll
        for (int ft = 0; ft < 3; ++ft)
#pragma unroll
          for (int dt = 0; dt < 2; ++dt) kv[ft][dt] = mfma32(a[ft], b[dt], kv[ft][dt]);
      }
    }
    {
      f32x4 o5[5] = {kv[0][0], kv[1][0], kv[0][1], kv[1][1], f32x4{kv[2][0][0], kv[2][0][1], kv[2][1][0], kv[2][1][1]}};
      ldsp ex = sm + (4 + wn) * 17408;
      if (mh == 1) {
        LGKM0();
#pragma unroll
        for (int i = 0; i < 5; ++i) *(LAS f32x4*)(ex + i * 1024 + lane * 16) = o5[i];
      }
      LGKM_BAR();
      if (mh == 0) {
        unsigned pp[10];
#pragma unroll
        for (int i = 0; i < 5; ++i) {
          f32x4 e = *(const LAS f32x4*)(ex + i * 1024 + lane * 16);
          pp[2 * i] = pk2(o5[i][0] + e[0], o5[i][1] + e[1]);
          pp[2 * i + 1] = pk2(o5[i][2] + e[2], o5[i][3] + e[3]);
        }
        char* kp = ws + WS_KV + (size_t)(rg * NH + head) * 3072 + lane * 16;
        st128_sc1(kp, u32x4{pp[0], pp[1], pp[2], pp[3]});
        st128_sc1(kp + 1024, u32x4{pp[4], pp[5], pp[6], pp[7]});
        st128_sc1(kp + 2048, u32x4{pp[8], pp[9], 0u, 0u});
      }
      WAITN_BAR(0);
      if (w == 0 && lane == 0) st64_sc1(ws + WS_KF + ct * 128 + rg * 8, ep);
    }
    STAMPAT(5);
    unsigned qf[6][5];
#pragma unroll
    for (int mp = 0; mp < 3; ++mp) {
      float y[2][5];
#pragma unroll
      for (int hh = 0; hh < 2; ++hh) {
        const int m = 2 * mp + hh;
        if (m < MT) {
          const float c = rstd[m] * L2E;
          u32x4 qb = u32x4{pk2(acc[m][0][0], acc[m][0][1]), pk2(acc[m][0][2], acc[m][0][3]), rem[m][0], rem[m][1]};
          f32x4 q0 = mfma32(wq[0], asb(qb), z4);
          f32x4 q1 = mfma32(wq[1], asb(qb), z4);
          y[hh][0] = q0[0] * c; y[hh][1] = q0[1] * c; y[hh][2] = q0[2] * c; y[hh][3] = q0[3] * c; y[hh][4] = q1[0] * c;
        } else {
#pragma unroll
          for (int i = 0; i < 5; ++i) y[hh][i] = 0.f;
        }
      }
      featmap2(y[0], y[1], true, true, g, qf[2 * mp], qf[2 * mp + 1]);
    }
    STAMPAT(6);
    wait_flags(ws + WS_KF + ct * 128 + (lane & 15) * 8, lane < 12, ep);
    STAMPAT(7);
    float ks[20];
    {
      u32x4 pv[6][3];
      const char* kp = ws + WS_KV + (size_t)((mh * 6) * NH + head) * 3072 + lane * 16;
#pragma unroll
      for (int c = 0; c < 6; ++c) {
        const char* p = kp + (size_t)c * NH * 3072;
        asm volatile("global_load_dwordx4 %0, %3, off sc1\n\tglobal_load_dwordx4 %1, %3, off offset:1024 sc1\n\tglobal_load_dwordx4 %2, %3, off offset:2048 sc1"
                     : "=&v"(pv[c][0]), "=&v"(pv[c][1]), "=&v"(pv[c][2]) : "v"(p) : "memory");
      }
      asm volatile("s_waitcnt vmcnt(0)" : "+v"(pv[0][0]), "+v"(pv[0][1]), "+v"(pv[0][2]), "+v"(pv[1][0]), "+v"(pv[1][1]), "+v"(pv[1][2]),
                   "+v"(pv[2][0]), "+v"(pv[2][1]), "+v"(pv[2][2]), "+v"(pv[3][0]), "+v"(pv[3][1]), "+v"(pv[3][2]),
                   "+v"(pv[4][0]), "+v"(pv[4][1]), "+v"(pv[4][2]), "+v"(pv[5][0]), "+v"(pv[5][1]), "+v"(pv[5][2]) :: "memory");
#pragma unroll
      for (int i = 0; i < 10; ++i) {
        float lo = 0.f, hi = 0.f;
#pragma unroll
        for (int c = 0; c < 6; ++c) {
          unsigned u = pv[c][i >> 2][i & 3];
          lo += u2f(u << 16); hi += u2f(u & 0xffff0000u);
        }
        ks[2 * i] = lo; ks[2 * i + 1] = hi;
      }
      ldsp mine = sm + w * 17408, other = sm + (w ^ 4) * 17408;
      SCHED0();
#pragma unroll
      for (int i = 0; i < 5; ++i) *(LAS f32x4*)(mine + i * 1024 + lane * 16) = f32x4{ks[4 * i], ks[4 * i + 1], ks[4 * i + 2], ks[4 * i + 3]};
      LGKM_BAR();
#pragma unroll
      for (int i = 0; i < 5; ++i) {
        f32x4 e = *(const LAS f32x4*)(other + i * 1024 + lane * 16);
#pragma unroll
        for (int q = 0; q < 4; ++q) ks[4 * i + q] = (mh == 0) ? ks[4 * i + q] + e[q] : e[q] + ks[4 * i + q];
      }
    }
    const bf16x8 ka0 = asb(u32x4{pk2(ks[0], ks[1]), pk2(ks[2], ks[3]), pk2(ks[4], ks[5]), pk2(ks[6], ks[7])});
    const bf16x8 ka1 = asb(u32x4{pk2(ks[8], ks[9]), pk2(ks[10], ks[11]), pk2(ks[12], ks[13]), pk2(ks[14], ks[15])});
    const bf16x8 kb0 = asb(u32x4{pk2(ks[16], ks[17]), 0u, 0u, 0u});
    const bf16x8 kb1 = asb(u32x4{pk2(ks[18], ks[19]), 0u, 0u, 0u});
    {
      u32x2 om_[MT], or_[MT];
#pragma unroll
      for (int m = 0; m < MT; ++m) {
        bf16x8 b0 = asb(u32x4{qf[m][0], qf[m][1], qf[m][2], qf[m][3]});
        bf16x8 b1 = asb(u32x4{qf[m][4], 0u, 0u, 0u});
        f32x4 n0 = mfma32(kb0, b1, mfma32(ka0, b0, z4));
        f32x4 n1 = mfma32(kb1, b1, mfma32(ka1, b0, z4));
        float da = n1[0], db = n1[0];
        swap16(da, db);
        float inv = __builtin_amdgcn_rcpf(db + 1e-6f);
        om_[m] = u32x2{pk2(n0[0] * inv, n0[1] * inv), pk2(n0[2] * inv, n0[3] * inv)};
        or_[m] = u32x2{pk2(n1[0] * inv, n1[1] * inv), pk2(n1[2] * inv, n1[3] * inv)};
      }
      SCHED0();
      ldsp stg = sm + w * 17408 + 8192;
#pragma unroll
      for (int m = 0; m < MT; ++m) {
        *(LAS u32x2*)(stg + (m * 16 + fr) * 32 + g * 8) = om_[m];
        if (g == 0) *(LAS u32x2*)(stg + 3072 + (m * 16 + fr) * 8) = or_[m];
      }
      LGKM0();
      const int tok0 = r0 + mh * 95;
      const int ntok = mh == 0 ? 95 : nrow - 95;
      char* om = ws + WS_OM + ((size_t)head * S_ + tok0) * 32;
      char* orr = ws + WS_OR + ((size_t)head * S_ + tok0) * 8;
      u32x4 tv[3];
      u32x2 tr[2];
#pragma unroll
      for (int i = 0; i < 3; ++i) tv[i] = *(const LAS u32x4*)(stg + i * 1024 + lane * 16);
#pragma unroll
      for (int i = 0; i < 2; ++i) tr[i] = *(const LAS u32x2*)(stg + 3072 + ((i * 64 + lane) < 96 ? (i * 64 + lane) : 0) * 8);
#pragma unroll
      for (int i = 0; i < 3; ++i)
        if (i * 32 + (lane >> 1) < ntok) st128_sc1(om + i * 1024 + lane * 16, tv[i]);
#pragma unroll
      for (int i = 0; i < 2; ++i)
        if (i * 64 + lane < ntok) st64_sc1(orr + (i * 64 + lane) * 8, tr[i]);
    }
    WAITN_BAR(0);
    if (w == 0 && lane == 0) st64_sc1(ws + WS_OF + rg * 128 + ct * 8, ep);
    STAMPAT(8);
  }
  return ep;
}

DI u32x2 service(ldsp sm, const char* x, char* ws, int sid, int w, int lane) {
  const int bid = __builtin_amdgcn_workgroup_id_x();
  const int g = lane >> 4, fr = lane & 15;
  STAMPAT(0);
  if (w == 0) {
    unsigned long long nn = __builtin_amdgcn_s_memrealtime();
    u32x2 nv = u32x2{(unsigned)nn, (unsigned)(nn >> 32)};
    if (lane == 0) {
      if (sid != 15) st64_sc1(ws + WS_REQ + bid * 64, nv);
      *(LAS u32x2*)(sm + L_NONCE) = nv;
    }
  }
  float mean[6], rstd[6];
  if (sid < 12) {
    const int r0 = rs_of(sid), nrow = rs_of(sid + 1) - r0;
    u32x4 v[6][5];
#pragma unroll
    for (int i = 0; i < 6; ++i) {
      int slot = (i * 8 + w) * 4 + g;
      int ri = slot < 95 ? slot : slot - 1;
      ri = ri < nrow ? ri : nrow - 1;
      const char* p = x + (size_t)(r0 + ri) * 1280 + fr * 16;
#pragma unroll
      for (int c = 0; c < 5; ++c) v[i][c] = *(const u32x4*)(p + c * 256);
    }
#pragma unroll
    for (int i = 0; i < 6; ++i) {
      float a = 0.f;
#pragma unroll
      for (int c = 0; c < 5; ++c)
#pragma unroll
        for (int e = 0; e < 4; ++e) a += u2f(v[i][c][e] << 16) + u2f(v[i][c][e] & 0xffff0000u);
      a += dpp<0xB1>(a); a += dpp<0x4E>(a); a += dpp<0x141>(a); a += dpp<0x140>(a);
      mean[i] = a * (1.0f / 640.0f);
      float b = 0.f;
#pragma unroll
      for (int c = 0; c < 5; ++c)
#pragma unroll
        for (int e = 0; e < 4; ++e) {
          float lo = u2f(v[i][c][e] << 16) - mean[i], hi = u2f(v[i][c][e] & 0xffff0000u) - mean[i];
          b = __builtin_fmaf(lo, lo, b);
          b = __builtin_fmaf(hi, hi, b);
        }
      b += dpp<0xB1>(b); b += dpp<0x4E>(b); b += dpp<0x141>(b); b += dpp<0x140>(b);
      rstd[i] = __builtin_amdgcn_rsqf(b * (1.0f / 640.0f) + 1e-5f);
    }
  }
  LGKM_BAR();
  const u32x2 nv = *(const LAS u32x2*)(sm + L_NONCE);
  u32x2 ep;
  if (sid == 15) {
    ep = u32x2{nv[0] ^ 0x5bd1e995u, nv[1] ^ 0x80000000u};
    if (w < 4) {
      const int cb = w * 64 + lane;
      const bool me = (cb == bid);
      for (unsigned spins = 0; spins < SPIN_MAX; ++spins) {
        u32x2 rq = ld64_sc1(ws + WS_REQ + cb * 64);
        st128_sc1(ws + WS_ACK + cb * 64, u32x4{rq[0], rq[1], ep[0], ep[1]});
        u32x2 dn = ld64_sc1(ws + WS_DONE + cb * 64);
        if (all_ok(me || (dn[0] == ep[0] && dn[1] == ep[1]))) break;
        __builtin_amdgcn_s_sleep(16);
      }
    }
  } else {
    u32x4 ack;
    for (unsigned spins = 0; spins < SPIN_MAX; ++spins) {
      ack = ld128_sc1(ws + WS_ACK + bid * 64);
      if (all_ok(ack[0] == nv[0] && ack[1] == nv[1])) break;
      __builtin_amdgcn_s_sleep(4);
    }
    ep = u32x2{ack[2], ack[3]};
    if (w == 0 && lane == 0) st64_sc1(ws + WS_DONE + bid * 64, ep);
    if (sid < 12) {
#pragma unroll
      for (int i = 0; i < 6; ++i) {
        int slot = (i * 8 + w) * 4 + g;
        if (fr == 0 && slot < 176) st128_sc1(ws + WS_STAT + (size_t)(sid * 176 + slot) * 16, u32x4{f2u(mean[i]), f2u(rstd[i]), ep[0], ep[1]});
      }
    }
  }
  STAMPAT(1);
  return ep;
}

__global__ __launch_bounds__(512, 2) void fused(const char* __restrict__ x, const char* __restrict__ wqkv,
                                                const char* __restrict__ wphi, const char* __restrict__ wo,
                                                const char* __restrict__ wg, const char* __restrict__ wu,
                                                const char* __restrict__ wd, char* __restrict__ ws, char* __restrict__ out) {
  extern __shared__ __attribute__((aligned(16))) char smem[];
  ldsp sm = (ldsp)smem;
  const int tid = __builtin_amdgcn_workitem_id_x();
  const int b = __builtin_amdgcn_workgroup_id_x(), xcd = b & 7, idx = b >> 3;
  const int w = __builtin_amdgcn_readfirstlane(tid >> 6), lane = tid & 63;
  const u32x4 srd_wd = make_srd(wd, DF_ * D_ * 2);
  const int rgl = idx / 5, c5 = idx % 5;
  const int rg = (xcd >> 2) * 6 + rgl, cq = xcd & 3;
  if (idx < 30 && c5 >= 3) {
    const int ct = cq * 2 + (c5 - 3);
    if (w < 4) phase1<6, false>(sm, x, wqkv, wphi, wg, wu, srd_wd, ws, rg, ct, 0, w, lane);
    else phase1<5, false>(sm, x, wqkv, wphi, wg, wu, srd_wd, ws, rg, ct, 0, w, lane);
    return;
  }
  const int p2i = idx < 30 ? rgl * 3 + c5 : 18 + (idx - 30);
  const int rt = (xcd >> 1) * 4 + p2i / 5, ct2 = (xcd & 1) * 5 + p2i % 5;
  u32x2 ep;
  if (idx < 30) {
    const int ct = cq * 3 + c5;
    if (w < 4) ep = phase1<6, true>(sm, x, wqkv, wphi, wg, wu, srd_wd, ws, rg, ct, ct2 * 64, w, lane);
    else ep = phase1<5, true>(sm, x, wqkv, wphi, wg, wu, srd_wd, ws, rg, ct, ct2 * 64, w, lane);
  } else {
    ep = service(sm, x, ws, b - 240, w, lane);
    const unsigned vB = p2_vB(w, lane);
    const unsigned smb = (unsigned)(size_t)sm;
    p2_issueB_down(srd_wd, vB, ct2 * 64, w, smb, 0);
    p2_issueB_down(srd_wd, vB, ct2 * 64, w, smb, 1);
    p2_issueB_down(srd_wd, vB, ct2 * 64, w, smb, 2);
  }
  phase2(sm, x, wo, srd_wd, ws, out, rt, ct2, w, lane, ep);
}

#if defined(__HIP_DEVICE_COMPILE__)
#pragma clang attribute pop
#endif

extern "C" void kernel_launch(void* const* d_in, const int* in_sizes, int n_in, void* d_out, int out_size, void* d_ws,
                              size_t ws_size, hipStream_t stream) {
  (void)hipFuncSetAttribute((const void*)fused, hipFuncAttributeMaxDynamicSharedMemorySize, K_LDS);
  fused<<<256, 512, K_LDS, stream>>>((const char*)d_in[0], (const char*)d_in[1], (const char*)d_in[2], (const char*)d_in[3],
                                     (const char*)d_in[5], (const char*)d_in[6], (const char*)d_in[7], (char*)d_ws, (char*)d_out);
}
```

```cpp
#include <hip/hip_runtime.h>
#include <hip/hip_bf16.h>
#include <stdint.h>

typedef __attribute__((ext_vector_type(8))) __bf16 bf16x8;
typedef __attribute__((ext_vector_type(2))) __bf16 bf16x2;
typedef __attribute__((ext_vector_type(4))) short s16x4;
typedef __attribute__((ext_vector_type(8))) short s16x8;
typedef __attribute__((ext_vector_type(4))) float f32x4;
typedef __attribute__((ext_vector_type(2))) float f32x2;
typedef __attribute__((ext_vector_type(4))) unsigned u32x4;
typedef __attribute__((ext_vector_type(2))) unsigned u32x2;
#define LAS __attribute__((address_space(3)))
#define DI __device__ __forceinline__
typedef LAS char* ldsp;

constexpr int S_ = 2048, D_ = 640, DF_ = 1536, NH = 32;
constexpr int NRG = 12;
constexpr size_t WS_H = 0;
constexpr size_t WS_KV = WS_H + (size_t)S_ * DF_ * 2;
constexpr size_t WS_OM = WS_KV + (size_t)NRG * NH * 4096;
constexpr size_t WS_OR = WS_OM + (size_t)NH * S_ * 32;
constexpr size_t WS_REQ = WS_OR + (size_t)NH * S_ * 8;
constexpr size_t WS_ACK = WS_REQ + 256 * 64;
constexpr size_t WS_DONE = WS_ACK + 256 * 64;
constexpr size_t WS_STAT = WS_DONE + 256 * 64;
constexpr size_t WS_HF = WS_STAT + 12 * 176 * 16;
constexpr size_t WS_KF = WS_HF + 12 * 128;
constexpr size_t WS_OF = WS_KF + 8 * 128;
constexpr size_t WS_DBG = 64u << 20;

constexpr int A_SLOT = 22528, B_SLOT = 16384;
constexpr int L_A = 0;
constexpr int L_B = 3 * A_SLOT;
constexpr int L_MISC = L_B + 5 * B_SLOT;
constexpr int L_DUMMY = L_MISC;
constexpr int L_NONCE = L_MISC + 1024;
constexpr int L_CS = L_NONCE + 64;
constexpr int K_LDS = L_CS + 2048;
constexpr int P2_A = 0, P2_B = 98304;
constexpr unsigned SPIN_MAX = 1u << 20;

#if defined(__HIP_DEVICE_COMPILE__)

#endif

#define STAMPAT(i)

DI float u2f(unsigned u) { return __builtin_bit_cast(float, u); }
DI unsigned f2u(float f) { return __builtin_bit_cast(unsigned, f); }
DI u32x4 make_srd(const void* p, unsigned bytes) {
  uint64_t a = (uint64_t)p;
  u32x4 r;
  r[0] = (unsigned)a;
  r[1] = (unsigned)(a >> 32) & 0xffffu;
  r[2] = bytes;
  r[3] = 0x00020000u;
  return r;
}
DI void dma_b(u32x4 srd, unsigned voff, unsigned soff, unsigned m0v) {
  asm volatile("s_mov_b32 m0, %3\n\ts_nop 0\n\tbuffer_load_dwordx4 %0, %1, %2 offen lds" ::"v"(voff), "s"(srd), "s"(soff), "s"(m0v) : "memory");
}
DI void dma_b_mask(u32x4 srd, unsigned voff, unsigned soff, unsigned m0v, unsigned long long mask) {
  asm volatile("s_mov_b32 m0, %3\n\ts_mov_b64 exec, %4\n\tbuffer_load_dwordx4 %0, %1, %2 offen lds\n\ts_mov_b64 exec, -1" ::"v"(voff), "s"(srd), "s"(soff), "s"(m0v), "s"(mask) : "memory");
}
DI void dma_b_sc1(u32x4 srd, unsigned voff, unsigned soff, unsigned m0v) {
  asm volatile("s_mov_b32 m0, %3\n\ts_nop 0\n\tbuffer_load_dwordx4 %0, %1, %2 offen sc1 lds" ::"v"(voff), "s"(srd), "s"(soff), "s"(m0v) : "memory");
}
DI u32x4 ld128_sc1(const char* p) { u32x4 v; asm volatile("global_load_dwordx4 %0, %1, off sc1\n\ts_waitcnt vmcnt(0)" : "=&v"(v) : "v"(p) : "memory"); return v; }
DI u32x2 ld64_sc1(const char* p) { u32x2 v; asm volatile("global_load_dwordx2 %0, %1, off sc1\n\ts_waitcnt vmcnt(0)" : "=&v"(v) : "v"(p) : "memory"); return v; }
DI void st128_sc1(char* p, u32x4 v) { asm volatile("global_store_dwordx4 %0, %1, off sc1\n\ts_nop 1" ::"v"(p), "v"(v) : "memory"); }
DI void st64_sc1(char* p, u32x2 v) { asm volatile("global_store_dwordx2 %0, %1, off sc1" ::"v"(p), "v"(v) : "memory"); }
#define WAITN_BAR(N) asm volatile("s_waitcnt vmcnt(%0) lgkmcnt(0)\n\ts_barrier" ::"n"(N) : "memory")
#define LGKM_BAR() asm volatile("s_waitcnt lgkmcnt(0)\n\ts_barrier" ::: "memory")
#define LGKM0() asm volatile("s_waitcnt lgkmcnt(0)" ::: "memory")
#define MEMFENCE() asm volatile("" ::: "memory")
#define SCHED0() __builtin_amdgcn_sched_barrier(0)

DI bf16x8 ld128(ldsp p) { return *(const LAS bf16x8*)p; }
DI s16x4 tr1(ldsp p) { return __builtin_amdgcn_ds_read_tr16_b64_v4i16((LAS s16x4*)p); }
DI bf16x8 cat(s16x4 a, s16x4 b) { return __builtin_bit_cast(bf16x8, __builtin_shufflevector(a, b, 0, 1, 2, 3, 4, 5, 6, 7)); }
DI bf16x8 tr2(ldsp p0, ldsp p1) { return cat(tr1(p0), tr1(p1)); }
DI f32x4 mfma32(bf16x8 a, bf16x8 b, f32x4 c) { return __builtin_amdgcn_mfma_f32_16x16x32_bf16(a, b, c, 0, 0, 0); }
DI bf16x8 asb(u32x4 v) { return __builtin_bit_cast(bf16x8, v); }
DI unsigned pk2(float a, float b) {
  bf16x2 v;
  v[0] = (__bf16)a;
  v[1] = (__bf16)b;
  return __builtin_bit_cast(unsigned, v);
}
DI void swap16(float& a, float& b) {
  auto r = __builtin_amdgcn_permlane16_swap(f2u(a), f2u(b), false, false);
  a = u2f(r[0]); b = u2f(r[1]);
}
DI void swap32(float& a, float& b) {
  auto r = __builtin_amdgcn_permlane32_swap(f2u(a), f2u(b), false, false);
  a = u2f(r[0]); b = u2f(r[1]);
}
template <int CTRL> DI float dpp(float x) { return u2f(__builtin_amdgcn_update_dpp(0u, f2u(x), CTRL, 0xf, 0xf, false)); }
DI float fmx(float a, float b) { return __builtin_fmaxf(a, b); }
DI float fmn(float a, float b) { return __builtin_fminf(a, b); }
DI float rawmax(float a, float b) { float r; asm("v_max_f32 %0, %1, %2" : "=v"(r) : "v"(a), "v"(b)); return r; }
DI float opq(float x) { asm("" : "+v"(x)); return x; }
DI f32x2 lo2(f32x4 v) { return __builtin_shufflevector(v, v, 0, 1); }
DI f32x2 hi2(f32x4 v) { return __builtin_shufflevector(v, v, 2, 3); }
DI f32x2 sp2(float x) { return f32x2{x, x}; }
DI f32x2 fma2(f32x2 a, f32x2 b, f32x2 c) { return __builtin_elementwise_fma(a, b, c); }
DI int rs_of(int rg) { return (512 * rg) / 3; }
DI int rg_of(int row) { return (3 * row + 2) >> 9; }
DI bool all_ok(bool ok) { return __builtin_amdgcn_ballot_w64(ok) == ~0ull; }

DI void wait_flags(const char* p, bool need, u32x2 ep) {
  for (unsigned spins = 0; spins < SPIN_MAX; ++spins) {
    u32x2 v = ld64_sc1(p);
    if (all_ok(!need || (v[0] == ep[0] && v[1] == ep[1]))) break;
    __builtin_amdgcn_s_sleep(2);
  }
}

DI f32x2 ex2(f32x2 v) { return f32x2{__builtin_amdgcn_exp2f(v[0]), __builtin_amdgcn_exp2f(v[1])}; }
DI unsigned pk2v(f32x2 v) { return pk2(v[0], v[1]); }
DI void featmap2(f32x2 a01, f32x2 a23, f32x2 b01, f32x2 b23, f32x2 e4, bool livea, bool liveb, int g, unsigned (&oa)[5], unsigned (&ob)[5]) {
  float a0 = fmx(fmx(fmx(a01[0], a01[1]), a23[0]), fmx(a23[1], e4[0]));
  float a1 = -fmn(fmn(fmn(a01[0], a01[1]), a23[0]), fmn(a23[1], e4[0]));
  float b0 = fmx(fmx(fmx(b01[0], b01[1]), b23[0]), fmx(b23[1], e4[1]));
  float b1 = -fmn(fmn(fmn(b01[0], b01[1]), b23[0]), fmn(b23[1], e4[1]));
  swap16(a0, a1); swap16(b0, b1);
  float ca = rawmax(a0, a1), cb = rawmax(b0, b1);
  swap32(ca, cb);
  float d0 = rawmax(ca, cb), d1 = d0;
  swap32(d0, d1);
  float Ma = d0, Na = d0, Mb = d1, Nb = d1;
  swap16(Ma, Na); swap16(Mb, Nb);
  const f32x2 pa01 = ex2(a01 - sp2(Ma)), pa23 = ex2(a23 - sp2(Ma)), na01 = ex2(-a01 - sp2(Na)), na23 = ex2(-a23 - sp2(Na));
  const f32x2 pb01 = ex2(b01 - sp2(Mb)), pb23 = ex2(b23 - sp2(Mb)), nb01 = ex2(-b01 - sp2(Nb)), nb23 = ex2(-b23 - sp2(Nb));
  const float pa4 = __builtin_amdgcn_exp2f(e4[0] - Ma), na4 = __builtin_amdgcn_exp2f(-e4[0] - Na);
  const float pb4 = __builtin_amdgcn_exp2f(e4[1] - Mb), nb4 = __builtin_amdgcn_exp2f(-e4[1] - Nb);
  f32x2 t;
  t = pa01 + pa23; a0 = (t[0] + t[1]) + pa4;
  t = na01 + na23; a1 = (t[0] + t[1]) + na4;
  t = pb01 + pb23; b0 = (t[0] + t[1]) + pb4;
  t = nb01 + nb23; b1 = (t[0] + t[1]) + nb4;
  swap16(a0, a1); swap16(b0, b1);
  ca = a0 + a1; cb = b0 + b1;
  swap32(ca, cb);
  d0 = __builtin_amdgcn_rcpf(ca + cb);
  if (!(g < 2 ? livea : liveb)) d0 = 0.f;
  d1 = d0;
  swap32(d0, d1);
  Ma = d0; Na = d0; Mb = d1; Nb = d1;
  swap16(Ma, Na); swap16(Mb, Nb);
  oa[0] = pk2v(pa01 * sp2(Ma)); oa[1] = pk2v(pa23 * sp2(Ma));
  oa[2] = pk2v(na01 * sp2(Na)); oa[3] = pk2v(na23 * sp2(Na));
  oa[4] = pk2(pa4 * Ma, na4 * Na);
  ob[0] = pk2v(pb01 * sp2(Mb)); ob[1] = pk2v(pb23 * sp2(Mb));
  ob[2] = pk2v(nb01 * sp2(Nb)); ob[3] = pk2v(nb23 * sp2(Nb));
  ob[4] = pk2(pb4 * Mb, nb4 * Nb);
}

struct P2Ctx { unsigned vB, vR; int c0; };
DI unsigned p2_vB(int w, int lane) {
  const int l3 = lane >> 3, l7 = lane & 7;
  int fb = ((l3 >> 1) & 1) | ((w & 1) << 1);
  int ch = (((l7 >> 1) ^ fb) << 1) | (l7 & 1);
  return l3 * 1280 + ch * 16;
}
DI int p2_rot(int s, int rot) { int r = s + rot; return r >= 12 ? r - 12 : r; }
DI void p2_issueB_down(u32x4 srd_wd, unsigned vB, int c0, int w, unsigned smb, int s, int rot) {
  const int sr = p2_rot(s, rot);
  dma_b(srd_wd, vB, (128 * sr + 8 * w) * 1280 + c0 * 2, smb + P2_B + (s % 3) * 16384 + w * 1024);
  dma_b(srd_wd, vB, (128 * sr + 8 * w + 64) * 1280 + c0 * 2, smb + P2_B + (s % 3) * 16384 + w * 1024 + 8192);
}

DI void phase2(ldsp sm, const char* x, const char* wo, const u32x4 srd_wd, char* ws, char* out, int rt, int ct, int rot, int w, int lane, u32x2 ep) {
  const int r0 = rt * 128, c0 = ct * 64;
  const int g = lane >> 4, fr = lane & 15, l3 = lane >> 3, l7 = lane & 7;
  const int kq = w & 3, mh = w >> 2;
  const unsigned smb = (unsigned)(size_t)sm;
  const u32x4 srd_h = make_srd(ws + WS_H, S_ * DF_ * 2);
  const u32x4 srd_wo = make_srd(wo, D_ * D_ * 2);
  const u32x4 srd_om = make_srd(ws + WS_OM, NH * S_ * 32);
  const u32x4 srd_or = make_srd(ws + WS_OR, NH * S_ * 8);
  const unsigned vB = p2_vB(w, lane);
  const unsigned vA = l3 * 3072 + ((l7 ^ ((4 * (w & 1) + (l3 >> 1)) & 7)) * 16);
  const unsigned vR = vB - l3 * 1280 + (20 * (lane >> 5) + 16 + (l3 & 3)) * 1280;
  const unsigned vL = lane * 16;
  const int rgl = rg_of(r0), rgh = rg_of(r0 + 127);

#define P2_PIECE(s, p) do { \
    constexpr int i_ = (p) < 2 ? (p) : (p) - 2; \
    if ((s) < 12) { \
      if ((p) < 2) dma_b(srd_wd, vB, (128 * p2_rot((s), rot) + 8 * w + 64 * i_) * 1280 + c0 * 2, smb + P2_B + ((s) % 3) * 16384 + i_ * 8192 + w * 1024); \
      else dma_b_sc1(srd_h, vA, (r0 + 8 * w + 64 * (i_ & 1)) * 3072 + p2_rot((s), rot) * 256 + (i_ >> 1) * 128, smb + P2_A + ((s) % 3) * 32768 + (i_ >> 1) * 16384 + (i_ & 1) * 8192 + w * 1024); \
    } else if ((s) < 16) { \
      if ((p) < 2) dma_b(srd_wo, vB, (160 * ((s) - 12) + 20 * ((w + 8 * i_) >> 1) + 8 * (w & 1)) * 1280 + c0 * 2, smb + P2_B + ((s) % 3) * 16384 + i_ * 8192 + w * 1024); \
      else dma_b_sc1(srd_om, vL, ((8 * ((s) - 12) + 2 * i_ + (w >> 2)) * S_ + r0) * 32 + (w & 3) * 1024, smb + P2_A + ((s) % 3) * 32768 + i_ * 8192 + w * 1024); \
    } else { \
      if ((p) < 2) dma_b(srd_wo, vR, (40 * (w + 8 * i_)) * 1280 + c0 * 2, smb + P2_B + ((s) % 3) * 16384 + i_ * 8192 + w * 1024); \
      else dma_b_sc1(srd_or, vL, ((8 * i_ + w) * S_ + r0) * 8, smb + P2_A + ((s) % 3) * 32768 + i_ * 8192 + w * 1024); \
    } } while (0)

  const int aD = P2_A + (kq >> 1) * 16384 + (64 * mh + fr) * 128 + (((4 * (kq & 1) + g) ^ ((fr >> 1) & 7)) * 16);
  const int aM = P2_A + (2 * kq + (g >> 1)) * 4096 + (64 * mh + fr) * 32 + (g & 1) * 16;
  const int aR = P2_A + (8 * kq + 2 * g) * 1024 + (64 * mh + fr) * 8;
  int bO[4];
  {
    const int q = fr >> 2, p = lane & 3;
    const int f = ((q >> 1) & 1) | ((g & 1) << 1);
#pragma unroll
    for (int j = 0; j < 4; ++j) bO[j] = P2_B + (32 * kq + 8 * g + q) * 128 + ((j ^ f) * 32) + p * 8;
  }
  f32x4 acc[4][4];
#pragma unroll
  for (int m = 0; m < 4; ++m)
#pragma unroll
    for (int j = 0; j < 4; ++j) acc[m][j] = f32x4{0.f, 0.f, 0.f, 0.f};

  STAMPAT(8);
  wait_flags(ws + WS_HF + (size_t)(lane < 16 ? rgl : rgh) * 128 + (lane & 15) * 8, lane < 32 && (lane & 15) < 12, ep);
  STAMPAT(9);
#define P2_A3(s) do { P2_PIECE(s, 2); P2_PIECE(s, 3); P2_PIECE(s, 4); P2_PIECE(s, 5); } while (0)
  P2_A3(0); P2_A3(1); P2_A3(2);
  u32x4 xr[2];
  bf16x8 fa[2][4];
  s16x4 fb[2][8];
#define P2_RD(s, i) do { \
    if ((i) < 8) fb[(s) & 1][i] = tr1(sm + ((s) % 3) * 16384 + bO[(i) >> 1] + ((i) & 1) * 512); \
    else if ((s) < 12) fa[(s) & 1][(i) - 8] = ld128(sm + ((s) % 3) * 32768 + aD + ((i) - 8) * 2048); \
    else if ((s) < 16) fa[(s) & 1][(i) - 8] = ld128(sm + ((s) % 3) * 32768 + aM + ((i) - 8) * 512); \
    else fa[(s) & 1][(i) - 8] = cat(*(const LAS s16x4*)(sm + ((s) % 3) * 32768 + aR + ((i) - 8) * 128), *(const LAS s16x4*)(sm + ((s) % 3) * 32768 + aR + 1024 + ((i) - 8) * 128)); } while (0)
#define P2_MM(s, i) acc[(i) >> 2][(i) & 3] = mfma32(cat(fb[(s) & 1][2 * ((i) & 3)], fb[(s) & 1][2 * ((i) & 3) + 1]), fa[(s) & 1][(i) >> 2], acc[(i) >> 2][(i) & 3])
#define P2_GROUP(s, q) do { \
    if ((s) >= 1 && (s) + 2 < 17) { \
      if ((s) + 2 == 12 && (q) == 2) { \
        STAMPAT(10); \
        wait_flags(ws + WS_OF + (size_t)(lane < 16 ? rgl : rgh) * 128 + (lane & 15) * 8, lane < 32 && (lane & 15) < 8, ep); \
        STAMPAT(11); \
      } \
      P2_PIECE((s) + 2, q); } \
    SCHED0(); \
    if ((s) < 17) { P2_RD(s, 2 * (q)); P2_RD(s, 2 * (q) + 1); } \
    SCHED0(); \
    if ((s) >= 1) { P2_MM((s) - 1, ((q) * 8) / 3); P2_MM((s) - 1, ((q) * 8) / 3 + 1); if (((q) * 8) / 3 + 2 < (((q) + 1) * 8) / 3) P2_MM((s) - 1, ((q) * 8) / 3 + 2); } \
    SCHED0(); } while (0)
#define P2_STAGE(s) do { \
    SCHED0(); \
    if ((s) < 17) WAITN_BAR((s) == 0 ? 8 : ((s) == 1 ? 4 : ((s) < 16 ? 6 : 0))); \
    SCHED0(); \
    P2_GROUP(s, 0); P2_GROUP(s, 1); P2_GROUP(s, 2); P2_GROUP(s, 3); P2_GROUP(s, 4); P2_GROUP(s, 5); } while (0)
  P2_STAGE(0); P2_STAGE(1); P2_STAGE(2); P2_STAGE(3); P2_STAGE(4); P2_STAGE(5); P2_STAGE(6); P2_STAGE(7); P2_STAGE(8);
  P2_STAGE(9); P2_STAGE(10); P2_STAGE(11); P2_STAGE(12); P2_STAGE(13); P2_STAGE(14); P2_STAGE(15); P2_STAGE(16); P2_STAGE(17);
  SCHED0();
  STAMPAT(12);
#pragma unroll
  for (int i = 0; i < 2; ++i) xr[i] = *(const u32x4*)(x + (size_t)(r0 + (i * 8 + w) * 8 + l3) * 1280 + c0 * 2 + l7 * 16);
  LGKM_BAR();
  {
    ldsp my = sm + w * 17408;
#pragma unroll
    for (int m = 0; m < 4; ++m)
#pragma unroll
      for (int j = 0; j < 4; ++j) *(LAS f32x4*)(my + (m * 16 + fr) * 272 + j * 64 + g * 16) = acc[m][j];
  }
  LGKM_BAR();
#pragma unroll
  for (int i = 0; i < 2; ++i) {
    const int rl = w * 8 + l3;
    float s[8];
#pragma unroll
    for (int e = 0; e < 8; ++e) s[e] = 0.f;
#pragma unroll
    for (int kk = 0; kk < 4; ++kk) {
      f32x4 a = *(const LAS f32x4*)(sm + (i * 4 + kk) * 17408 + rl * 272 + l7 * 32);
      f32x4 bb = *(const LAS f32x4*)(sm + (i * 4 + kk) * 17408 + rl * 272 + l7 * 32 + 16);
#pragma unroll
      for (int e = 0; e < 4; ++e) { s[e] += a[e]; s[4 + e] += bb[e]; }
    }
    u32x4 o;
#pragma unroll
    for (int e = 0; e < 4; ++e) o[e] = pk2(s[2 * e] + u2f(xr[i][e] << 16), s[2 * e + 1] + u2f(xr[i][e] & 0xffff0000u));
    *(u32x4*)(out + (size_t)(r0 + i * 64 + rl) * 1280 + c0 * 2 + l7 * 16) = o;
  }
}

template <int MT, bool GU>
DI u32x2 phase1(ldsp sm, const char* x, const char* wqkv, const char* wphi, const char* wg, const char* wu, const u32x4 srd_wd,
                char* ws, int rg, int ct, int c0p2, int rot, int w, int lane) {
  STAMPAT(0);
  constexpr int mh = (MT == 6) ? 0 : 1;
  const int wn = w & 3;
  const int r0 = rs_of(rg), nrow = rs_of(rg + 1) - r0;
  const unsigned smb = (unsigned)(size_t)sm;
  const int g = lane >> 4, fr = lane & 15;

  const u32x4 srdx = make_srd(x, S_ * D_ * 2);
  unsigned voffA[3];
#pragma unroll
  for (int c = 0; c < 3; ++c) {
    int slot = 8 * (w + 8 * c) + (lane >> 3);
    int ch = (lane & 7) ^ ((slot >> 1) & 7);
    int ri = slot < 95 ? slot : slot - 1;
    ri = ri < nrow ? ri : nrow - 1;
    voffA[c] = (unsigned)((r0 + ri) * 1280 + ch * 16);
  }
  if (w >= 6) voffA[2] = 0x40000000u;
  const unsigned long long FULL = ~0ull, PART = 0x00FFFFFFFFFFFFFFull;
  const unsigned long long mask1 = (w == 3) ? PART : FULL, mask2 = (w == 5) ? PART : FULL;
  const unsigned adst0 = smb + L_A + w * 1024;
  u32x4 srd0, srd1;
  unsigned voff0, voff1;
  constexpr unsigned SSTEP = GU ? 32 * 3072 : 32 * 3840;
  if (GU) {
    srd0 = make_srd(wg, D_ * DF_ * 2);
    srd1 = make_srd(wu, D_ * DF_ * 2);
    int krow = 4 * w + (lane >> 4);
    int chp = lane & 15;
    int f = (krow & 3) | (((krow >> 3) & 1) << 2);
    int ch = (((chp >> 1) ^ f) << 1) | (chp & 1);
    voff0 = voff1 = krow * 3072 + ct * 256 + ch * 16;
  } else {
    srd0 = srd1 = make_srd(wqkv, D_ * 3 * D_ * 2);
    unsigned vo[2];
#pragma unroll
    for (int c = 0; c < 2; ++c) {
      int cidx = (w + 8 * c) * 64 + lane;
      int rho = cidx / 30, cc = cidx % 30;
      int seg = cc / 10, ch = cc % 10;
      int k = (rho & 16) | ((rho & 4) << 1) | ((rho & 8) >> 1) | (rho & 3);
      vo[c] = (cidx < 960) ? (unsigned)(k * 3840 + seg * 1280 + ct * 160 + ch * 16) : 0x40000000u;
    }
    voff0 = vo[0];
    voff1 = vo[1];
  }
  const unsigned bdst0 = smb + L_B + w * 1024;
#define KROT (((rg%6)*5/3+ct*2)%10)
  const unsigned krot = KROT;
#define RA(so) ((so) + krot * 128u >= 1280u ? (so) + krot * 128u - 1280u : (so) + krot * 128u)
#define RB(so) ((so) + krot * 2u * SSTEP >= 20u * SSTEP ? (so) + krot * 2u * SSTEP - 20u * SSTEP : (so) + krot * 2u * SSTEP)
#define DMA_B0(soff, dst) dma_b(srd0, voff0, RB(soff), (dst))
#define DMA_B1(soff, dst) dma_b(srd1, voff1, RB(soff), (dst))
#define DMA_A0(soff, dst) dma_b(srdx, voffA[0], RA(soff), (dst))
#define DMA_A1(soff, dst) dma_b_mask(srdx, voffA[1], RA(soff), (dst), mask1)
#define DMA_A2(soff, dst) dma_b_mask(srdx, voffA[2], RA(soff), (dst), mask2)
  const unsigned a2base = (w >= 6) ? smb + L_DUMMY : adst0 + 16384;
  const unsigned a2mul = (w >= 6) ? 0u : 1u;
  unsigned wraw[16];
  if (!GU) {
    const char* wp = wphi + (ct * 4 + wn) * 800;
    const char* p0 = wp + (5 * (fr >> 2) + (fr & 3)) * 2;
    const char* p1 = wp + (5 * (fr >> 2) + 4) * 2;
    const char* q0 = p0 + g * 160;
    const char* q1 = p1 + g * 160;
#pragma unroll
    for (int j = 0; j < 4; ++j) {
      asm volatile("global_load_ushort %0, %1, off offset:%2" : "=&v"(wraw[j]) : "v"(q0), "n"(j * 40) : "memory");
      asm volatile("global_load_ushort %0, %1, off offset:%2" : "=&v"(wraw[4 + j]) : "v"(p0), "n"(640 + j * 40) : "memory");
      asm volatile("global_load_ushort %0, %1, off offset:%2" : "=&v"(wraw[8 + j]) : "v"(q1), "n"(j * 40) : "memory");
      asm volatile("global_load_ushort %0, %1, off offset:%2" : "=&v"(wraw[12 + j]) : "v"(p1), "n"(640 + j * 40) : "memory");
    }
  }
  DMA_B0(0, bdst0); DMA_B1(0, bdst0 + 8192);
  DMA_A0(0, adst0); DMA_A1(0, adst0 + 8192);
  DMA_A2(0, a2base);
  DMA_B0(SSTEP, bdst0 + B_SLOT); DMA_B1(SSTEP, bdst0 + B_SLOT + 8192);
  DMA_B0(2 * SSTEP, bdst0 + 2 * B_SLOT); DMA_B1(2 * SSTEP, bdst0 + 2 * B_SLOT + 8192);
  DMA_A0(128, adst0 + A_SLOT); DMA_A1(128, adst0 + A_SLOT + 8192);
  DMA_A2(128, a2base + a2mul * A_SLOT);
  DMA_B0(3 * SSTEP, bdst0 + 3 * B_SLOT); DMA_B1(3 * SSTEP, bdst0 + 3 * B_SLOT + 8192);
  DMA_B0(4 * SSTEP, bdst0 + 4 * B_SLOT); DMA_B1(4 * SSTEP, bdst0 + 4 * B_SLOT + 8192);
  DMA_A0(256, adst0 + 2 * A_SLOT); DMA_A1(256, adst0 + 2 * A_SLOT + 8192);
  if (w == 0 && lane < 48) {
    int rr = lane >> 3;
    *(LAS u32x4*)(sm + L_A + (rr >> 1) * A_SLOT + ((rr & 1) ? 175 : 95) * 128 + (lane & 7) * 16) =
        u32x4{0x3F803F80u, 0x3F803F80u, 0x3F803F80u, 0x3F803F80u};
  }
  const int bid = __builtin_amdgcn_workgroup_id_x();
  if (w == 1) {
    unsigned long long nn = __builtin_amdgcn_s_memrealtime();
    u32x2 nv = u32x2{(unsigned)nn, (unsigned)(nn >> 32)};
    if (lane == 0) {
      st64_sc1(ws + WS_REQ + bid * 64, nv);
      *(LAS u32x2*)(sm + L_NONCE) = nv;
    }
  }

  const int aoff0 = L_A + mh * 6 * 2048 + fr * 128 + ((g ^ ((fr >> 1) & 7)) * 16);
  const int aoff1 = aoff0 ^ 64;
  int boff[4];
  constexpr int TSTEP = GU ? 1024 : 3840;
  {
    const int q = fr >> 2, p = lane & 3;
    if (GU) {
      int f = q | ((g & 1) << 2);
#pragma unroll
      for (int j = 0; j < 4; ++j)
        boff[j] = L_B + (j >> 1) * 8192 + (8 * g + q) * 256 + (((2 * wn + (j & 1)) ^ f) * 32) + p * 8;
    } else {
      int rho = 16 * (g >> 1) + 4 * (g & 1) + q;
#pragma unroll
      for (int j = 0; j < 3; ++j) boff[j] = L_B + rho * 480 + (20 * j + 5 * wn + p) * 8;
      boff[3] = L_B + rho * 480 + (20 * (p == 3 ? 0 : p) + 5 * wn + 4) * 8;
    }
  }

  f32x4 acc[MT][4];
#pragma unroll
  for (int m = 0; m < MT; ++m)
#pragma unroll
    for (int j = 0; j < 4; ++j) acc[m][j] = f32x4{0.f, 0.f, 0.f, 0.f};
  bf16x8 fa0[MT], fa1[MT];
  s16x4 fb0[8], fb1[8];

  STAMPAT(1);
  WAITN_BAR(13);
  STAMPAT(2);
  unsigned wpk[8];
  if (!GU) {
    asm volatile("" : "+v"(wraw[0]), "+v"(wraw[1]), "+v"(wraw[2]), "+v"(wraw[3]), "+v"(wraw[4]), "+v"(wraw[5]), "+v"(wraw[6]), "+v"(wraw[7]),
                 "+v"(wraw[8]), "+v"(wraw[9]), "+v"(wraw[10]), "+v"(wraw[11]), "+v"(wraw[12]), "+v"(wraw[13]), "+v"(wraw[14]), "+v"(wraw[15]));
#pragma unroll
    for (int i = 0; i < 8; ++i) {
      wpk[i] = wraw[2 * i] | (wraw[2 * i + 1] << 16);
      if (i >= 4 && (fr & 3) != 0) wpk[i] = 0u;
    }
    asm volatile("" : "+v"(wpk[0]), "+v"(wpk[1]), "+v"(wpk[2]), "+v"(wpk[3]), "+v"(wpk[4]), "+v"(wpk[5]), "+v"(wpk[6]), "+v"(wpk[7]));
  }
#pragma unroll
  for (int m = 0; m < MT; ++m) fa0[m] = ld128(sm + aoff0 + m * 2048);
#pragma unroll
  for (int j = 0; j < 8; ++j) fb0[j] = tr1(sm + boff[j >> 1] + (j & 1) * TSTEP);
  MEMFENCE();

  constexpr int NM = MT * 4;
  constexpr int DS = (MT == 6) ? 6 : 5;
#define MMA(i, FA, FB) acc[(i) >> 2][(i) & 3] = mfma32(cat(FB[2 * ((i) & 3)], FB[2 * ((i) & 3) + 1]), FA[(i) >> 2], acc[(i) >> 2][(i) & 3])
#define BADDR(sl, i) (sm + (sl) * B_SLOT + boff[(i) >> 1] + ((i) & 1) * TSTEP)
  u32x4 ackv, stv[MT];
  const char* ackp = ws + WS_ACK + bid * 64;
  const char* stp = ws + WS_STAT + (size_t)(rg * 176 + mh * 96 + fr) * 16;
#pragma unroll
  for (int t = 0; t < 10; ++t) {
    SCHED0();
    WAITN_BAR(t < 8 ? 11 : (t == 8 ? 7 : 1 + MT));
    SCHED0();
#pragma unroll
    for (int i = 0; i < NM; ++i) {
      MMA(i, fa0, fb0);
      SCHED0();
      if (i < 8) fb1[i] = tr1(BADDR((2 * t + 1) % 5, i));
      else if (i < 8 + MT) fa1[i - 8] = ld128(sm + (t % 3) * A_SLOT + aoff1 + (i - 8) * 2048);
      if (t < 8) {
        if (i == 2) { if (MT == 6) DMA_A2((t + 2) * 128, adst0 + 16384 + ((t + 2) % 3) * A_SLOT); else DMA_A2((t + 2) * 128, a2base + a2mul * (((t + 2) % 3) * A_SLOT)); }
        if (i == 2 + DS) DMA_B0((2 * t + 5) * SSTEP, bdst0 + ((2 * t) % 5) * B_SLOT);
        if (i == 2 + 2 * DS) DMA_B1((2 * t + 5) * SSTEP, bdst0 + ((2 * t) % 5) * B_SLOT + 8192);
      }
      SCHED0();
    }
    if (t < 9) WAITN_BAR(t < 8 ? 9 : 2); else LGKM_BAR();
    SCHED0();
#pragma unroll
    for (int i = 0; i < NM; ++i) {
      MMA(i, fa1, fb1);
      SCHED0();
      if (t < 9) {
        if (i < 8) fb0[i] = tr1(BADDR((2 * t + 2) % 5, i));
        else if (i < 8 + MT) fa0[i - 8] = ld128(sm + ((t + 1) % 3) * A_SLOT + aoff0 + (i - 8) * 2048);
      }
      if (t < 7) {
        if (i == 1) DMA_B0((2 * t + 6) * SSTEP, bdst0 + ((2 * t + 1) % 5) * B_SLOT);
        if (i == 1 + DS) DMA_B1((2 * t + 6) * SSTEP, bdst0 + ((2 * t + 1) % 5) * B_SLOT + 8192);
        if (i == 1 + 2 * DS) DMA_A0((t + 3) * 128, adst0 + (t % 3) * A_SLOT);
        if (i == 1 + 3 * DS) DMA_A1((t + 3) * 128, adst0 + (t % 3) * A_SLOT + 8192);
      }
      if (i == 4 && t == 8) {
        asm volatile("global_load_dwordx4 %0, %1, off sc1" : "=&v"(ackv) : "v"(ackp) : "memory");
#pragma unroll
        for (int m = 0; m < MT; ++m) asm volatile("global_load_dwordx4 %0, %1, off offset:%2 sc1" : "=&v"(stv[m]) : "v"(stp), "n"(m * 256) : "memory");
      }
      SCHED0();
    }
  }
  SCHED0();
  STAMPAT(3);

  if (fr == 15) {
#pragma unroll
    for (int j = 0; j < 4; ++j) *(LAS f32x4*)(sm + L_CS + w * 256 + j * 64 + g * 16) = acc[MT - 1][j];
  }
  if (MT == 6) asm volatile("s_waitcnt vmcnt(0) lgkmcnt(0)" : "+v"(ackv), "+v"(stv[0]), "+v"(stv[1]), "+v"(stv[2]), "+v"(stv[3]), "+v"(stv[4]), "+v"(stv[MT - 1]) :: "memory");
  else asm volatile("s_waitcnt vmcnt(0) lgkmcnt(0)" : "+v"(ackv), "+v"(stv[0]), "+v"(stv[1]), "+v"(stv[2]), "+v"(stv[3]), "+v"(stv[4]) :: "memory");
  f32x4 cs[4];
#pragma unroll
  for (int j = 0; j < 4; ++j) cs[j] = *(const LAS f32x4*)(sm + L_CS + w * 256 + j * 64 + g * 16);
  {
    const u32x2 nv = *(const LAS u32x2*)(sm + L_NONCE);
    for (unsigned spins = 0; spins < SPIN_MAX; ++spins) {
      bool ok = (ackv[0] == nv[0]) && (ackv[1] == nv[1]);
#pragma unroll
      for (int m = 0; m < MT; ++m) ok = ok && (stv[m][2] == ackv[2]) && (stv[m][3] == ackv[3]);
      if (all_ok(ok)) break;
      __builtin_amdgcn_s_sleep(4);
      ackv = ld128_sc1(ackp);
#pragma unroll
      for (int m = 0; m < MT; ++m) stv[m] = ld128_sc1(stp + m * 256);
    }
  }
  const u32x2 ep = u32x2{ackv[2], ackv[3]};
  if (w == 0 && lane == 0) st64_sc1(ws + WS_DONE + bid * 64, ep);
  if (GU) {
    const unsigned vB = p2_vB(w, lane);
    p2_issueB_down(srd_wd, vB, c0p2, w, smb, 0, rot);
    p2_issueB_down(srd_wd, vB, c0p2, w, smb, 1, rot);
    p2_issueB_down(srd_wd, vB, c0p2, w, smb, 2, rot);
  }
  float rstd[MT];
#pragma unroll
  for (int m = 0; m < MT; ++m) {
    const f32x2 nm = sp2(opq(-u2f(stv[m][0])));
    rstd[m] = opq(u2f(stv[m][1]));
#pragma unroll
    for (int j = 0; j < 4; ++j) {
      f32x2 l = fma2(nm, lo2(cs[j]), lo2(acc[m][j])), h = fma2(nm, hi2(cs[j]), hi2(acc[m][j]));
      acc[m][j] = f32x4{l[0], l[1], h[0], h[1]};
    }
  }
  STAMPAT(4);

  if (GU) {
    const float K0 = -2.0f * 0.7978845608028654f * 1.4426950408889634f;
    const float K1c = K0 * 0.044715f;
    ldsp stg = sm + w * 7680;
    char* hg = ws + WS_H + ct * 256 + wn * 64 + (lane & 3) * 16;
#pragma unroll
    for (int m = 0; m <= MT; ++m) {
      if (m < MT) {
        const float r = rstd[m], r2 = r * r;
        const f32x2 c0 = sp2(opq(K0 * r)), c1 = sp2(opq(K1c * r2 * r)), ir2 = sp2(opq(__builtin_amdgcn_rcpf(r2)));
        u32x2 hp[2];
#pragma unroll
        for (int j = 0; j < 2; ++j) {
#pragma unroll
          for (int q = 0; q < 2; ++q) {
            const f32x2 a = q ? hi2(acc[m][j]) : lo2(acc[m][j]), b = q ? hi2(acc[m][j + 2]) : lo2(acc[m][j + 2]);
            const f32x2 z = a * fma2(a * a, c1, c0);
            const f32x2 e = f32x2{__builtin_amdgcn_exp2f(z[0]), __builtin_amdgcn_exp2f(z[1])};
            const f32x2 d = fma2(e, ir2, ir2);
            const f32x2 hv = (a * b) * f32x2{__builtin_amdgcn_rcpf(d[0]), __builtin_amdgcn_rcpf(d[1])};
            hp[j][q] = pk2(hv[0], hv[1]);
          }
        }
        *(LAS u32x2*)(stg + (m * 16 + fr) * 80 + g * 8) = hp[0];
        *(LAS u32x2*)(stg + (m * 16 + fr) * 80 + 32 + g * 8) = hp[1];
      }
      if (m >= 1) {
        const int rl = (m - 1) * 16 + (lane >> 2);
        u32x4 v = *(const LAS u32x4*)(stg + rl * 80 + (lane & 3) * 16);
        const int sl = mh * 96 + rl;
        const int ri = sl < 95 ? sl : sl - 1;
        if (sl != 95 && ri < nrow) st128_sc1(hg + (size_t)(r0 + ri) * 3072, v);
      }
    }
    STAMPAT(5);
    WAITN_BAR(0);
    if (w == 0 && lane == 0) st64_sc1(ws + WS_HF + rg * 128 + ct * 8, ep);
    STAMPAT(6);
  } else {
    const int head = ct * 4 + wn;
    bf16x8 wq[2], wk[2];
#pragma unroll
    for (int ft = 0; ft < 2; ++ft) {
      wq[ft] = asb(u32x4{wpk[4 * ft], wpk[4 * ft + 1], g == 0 ? wpk[4 * ft + 2] : 0u, g == 0 ? wpk[4 * ft + 3] : 0u});
      wk[ft] = asb(u32x4{wpk[4 * ft], wpk[4 * ft + 1], g == 1 ? wpk[4 * ft + 2] : 0u, g == 1 ? wpk[4 * ft + 3] : 0u});
    }
    const unsigned tag = ep[0] ^ (ep[1] * 0x9E3779B1u);
    const float L2E = 1.4426950408889634f;
    const f32x4 z4 = f32x4{0.f, 0.f, 0.f, 0.f};
    bool live[6];
    u32x2 rem[6];
#pragma unroll
    for (int m = 0; m < 6; ++m) {
      int s = mh * 96 + m * 16 + fr;
      int ri = s < 95 ? s : s - 1;
      live[m] = (m < MT) && (s != 95) && (ri < nrow);
      if (m < MT) rem[m] = u32x2{pk2(acc[m][3][0], acc[m][3][1]), pk2(acc[m][3][2], acc[m][3][3])};
    }
    ldsp pk_img = sm + w * 17408;
    ldsp v_img = pk_img + 96 * 104;
    {
      unsigned kf[6][5];
      u32x2 vv[6], ve[6];
#pragma unroll
      for (int mp = 0; mp < 3; ++mp) {
        f32x2 y01[2], y23[2], y4;
#pragma unroll
        for (int hh = 0; hh < 2; ++hh) {
          const int m = 2 * mp + hh;
          if (m < MT) {
            const f32x2 r = sp2(rstd[m]);
            const float c = opq(rstd[m] * L2E);
            u32x4 kb = u32x4{pk2(acc[m][1][0], acc[m][1][1]), pk2(acc[m][1][2], acc[m][1][3]), rem[m][0], rem[m][1]};
            f32x4 k0 = mfma32(wk[0], asb(kb), z4);
            f32x4 k1 = mfma32(wk[1], asb(kb), z4);
            y01[hh] = lo2(k0) * sp2(c); y23[hh] = hi2(k0) * sp2(c); y4[hh] = k1[0] * c;
            vv[m] = u32x2{pk2v(lo2(acc[m][2]) * r), pk2v(hi2(acc[m][2]) * r)};
            u32x2 vr = u32x2{pk2v(lo2(acc[m][3]) * r), pk2v(hi2(acc[m][3]) * r)};
            ve[m] = (g == 2) ? vr : u32x2{0x3F803F80u, 0x3F803F80u};
          } else {
            y01[hh] = sp2(0.f); y23[hh] = sp2(0.f); y4[hh] = 0.f;
            vv[m] = u32x2{0u, 0u}; ve[m] = vv[m];
          }
        }
        featmap2(y01[0], y23[0], y01[1], y23[1], y4, live[2 * mp], live[2 * mp + 1], g, kf[2 * mp], kf[2 * mp + 1]);
      }
      SCHED0();
#pragma unroll
      for (int m = 0; m < 6; ++m) {
        int tok = m * 16 + fr;
        *(LAS u32x2*)(pk_img + tok * 104 + g * 8) = u32x2{kf[m][0], kf[m][1]};
        *(LAS u32x2*)(pk_img + tok * 104 + 32 + g * 8) = u32x2{kf[m][2], kf[m][3]};
        *(LAS u32x2*)(pk_img + tok * 104 + 64 + g * 8) = u32x2{kf[m][4], 0u};
        *(LAS u32x2*)(v_img + tok * 72 + g * 8) = vv[m];
        *(LAS u32x2*)(v_img + tok * 72 + 32 + (g ^ 2) * 8) = ve[m];
      }
      LGKM0();
      SCHED0();
    }
    f32x4 kv[3][2];
#pragma unroll
    for (int ft = 0; ft < 3; ++ft)
#pragma unroll
      for (int dt = 0; dt < 2; ++dt) kv[ft][dt] = z4;
    {
      const int q = fr >> 2, p = lane & 3;
#pragma unroll
      for (int s = 0; s < 3; ++s) {
        int row = 32 * s + 8 * g + q;
        bf16x8 a[3], b[2];
#pragma unroll
        for (int ft = 0; ft < 3; ++ft) a[ft] = tr2(pk_img + row * 104 + ft * 32 + p * 8, pk_img + (row + 4) * 104 + ft * 32 + p * 8);
#pragma unroll
        for (int dt = 0; dt < 2; ++dt) b[dt] = tr2(v_img + row * 72 + dt * 32 + p * 8, v_img + (row + 4) * 72 + dt * 32 + p * 8);
#pragma unroll
        for (int ft = 0; ft < 3; ++ft)
#pragma unroll
          for (int dt = 0; dt < 2; ++dt) kv[ft][dt] = mfma32(a[ft], b[dt], kv[ft][dt]);
      }
    }
    {
      f32x4 o5[5] = {kv[0][0], kv[1][0], kv[0][1], kv[1][1], f32x4{kv[2][0][0], kv[2][0][1], kv[2][1][0], kv[2][1][1]}};
      ldsp ex = sm + (4 + wn) * 17408;
      if (mh == 1) {
        LGKM0();
#pragma unroll
        for (int i = 0; i < 5; ++i) *(LAS f32x4*)(ex + i * 1024 + lane * 16) = o5[i];
      }
      LGKM_BAR();
      if (mh == 0) {
        unsigned pp[10];
#pragma unroll
        for (int i = 0; i < 5; ++i) {
          f32x4 e = *(const LAS f32x4*)(ex + i * 1024 + lane * 16);
          pp[2 * i] = pk2(o5[i][0] + e[0], o5[i][1] + e[1]);
          pp[2 * i + 1] = pk2(o5[i][2] + e[2], o5[i][3] + e[3]);
        }
        char* kp = ws + WS_KV + (size_t)(rg * NH + head) * 4096 + lane * 16;
        st128_sc1(kp, u32x4{pp[0], pp[1], pp[2], tag});
        st128_sc1(kp + 1024, u32x4{pp[3], pp[4], pp[5], tag});
        st128_sc1(kp + 2048, u32x4{pp[6], pp[7], pp[8], tag});
        st128_sc1(kp + 3072, u32x4{pp[9], 0u, 0u, tag});
      }
    }
    STAMPAT(5);
    unsigned qf[6][5];
#pragma unroll
    for (int mp = 0; mp < 3; ++mp) {
      f32x2 y01[2], y23[2], y4;
#pragma unroll
      for (int hh = 0; hh < 2; ++hh) {
        const int m = 2 * mp + hh;
        if (m < MT) {
          const float c = opq(rstd[m] * L2E);
          u32x4 qb = u32x4{pk2(acc[m][0][0], acc[m][0][1]), pk2(acc[m][0][2], acc[m][0][3]), rem[m][0], rem[m][1]};
          f32x4 q0 = mfma32(wq[0], asb(qb), z4);
          f32x4 q1 = mfma32(wq[1], asb(qb), z4);
          y01[hh] = lo2(q0) * sp2(c); y23[hh] = hi2(q0) * sp2(c); y4[hh] = q1[0] * c;
        } else {
          y01[hh] = sp2(0.f); y23[hh] = sp2(0.f); y4[hh] = 0.f;
        }
      }
      featmap2(y01[0], y23[0], y01[1], y23[1], y4, true, true, g, qf[2 * mp], qf[2 * mp + 1]);
    }
    STAMPAT(6);
    float ks[20];
    {
      u32x4 pv[6][4];
      const char* kp = ws + WS_KV + (size_t)((mh * 6) * NH + head) * 4096 + lane * 16;
      for (unsigned spins = 0; spins < SPIN_MAX; ++spins) {
#pragma unroll
        for (int c = 0; c < 6; ++c) {
          const char* p = kp + (size_t)c * NH * 4096;
          asm volatile("global_load_dwordx4 %0, %4, off sc1\n\tglobal_load_dwordx4 %1, %4, off offset:1024 sc1\n\t"
                       "global_load_dwordx4 %2, %4, off offset:2048 sc1\n\tglobal_load_dwordx4 %3, %4, off offset:3072 sc1"
                       : "=&v"(pv[c][0]), "=&v"(pv[c][1]), "=&v"(pv[c][2]), "=&v"(pv[c][3]) : "v"(p) : "memory");
        }
        asm volatile("s_waitcnt vmcnt(0)" : "+v"(pv[0][0]), "+v"(pv[0][1]), "+v"(pv[0][2]), "+v"(pv[0][3]), "+v"(pv[1][0]), "+v"(pv[1][1]), "+v"(pv[1][2]), "+v"(pv[1][3]),
                     "+v"(pv[2][0]), "+v"(pv[2][1]), "+v"(pv[2][2]), "+v"(pv[2][3]), "+v"(pv[3][0]), "+v"(pv[3][1]), "+v"(pv[3][2]), "+v"(pv[3][3]),
                     "+v"(pv[4][0]), "+v"(pv[4][1]), "+v"(pv[4][2]), "+v"(pv[4][3]), "+v"(pv[5][0]), "+v"(pv[5][1]), "+v"(pv[5][2]), "+v"(pv[5][3]) :: "memory");
        bool ok = true;
#pragma unroll
        for (int c = 0; c < 6; ++c)
#pragma unroll
          for (int i = 0; i < 4; ++i) ok = ok && (pv[c][i][3] == tag);
        if (all_ok(ok)) break;
        __builtin_amdgcn_s_sleep(2);
      }
      STAMPAT(7);
#pragma unroll
      for (int i = 0; i < 10; ++i) {
        float lo = 0.f, hi = 0.f;
#pragma unroll
        for (int c = 0; c < 6; ++c) {
          unsigned u = pv[c][i / 3][i % 3];
          lo += u2f(u << 16); hi += u2f(u & 0xffff0000u);
        }
        ks[2 * i] = lo; ks[2 * i + 1] = hi;
      }
      ldsp mine = sm + w * 17408 + 12288, other = sm + (w ^ 4) * 17408 + 12288;
      SCHED0();
#pragma unroll
      for (int i = 0; i < 5; ++i) *(LAS f32x4*)(mine + i * 1024 + lane * 16) = f32x4{ks[4 * i], ks[4 * i + 1], ks[4 * i + 2], ks[4 * i + 3]};
      LGKM_BAR();
#pragma unroll
      for (int i = 0; i < 5; ++i) {
        f32x4 e = *(const LAS f32x4*)(other + i * 1024 + lane * 16);
#pragma unroll
        for (int q = 0; q < 4; ++q) ks[4 * i + q] = (mh == 0) ? ks[4 * i + q] + e[q] : e[q] + ks[4 * i + q];
      }
    }
    const bf16x8 ka0 = asb(u32x4{pk2(ks[0], ks[1]), pk2(ks[2], ks[3]), pk2(ks[4], ks[5]), pk2(ks[6], ks[7])});
    const bf16x8 ka1 = asb(u32x4{pk2(ks[8], ks[9]), pk2(ks[10], ks[11]), pk2(ks[12], ks[13]), pk2(ks[14], ks[15])});
    const bf16x8 kb0 = asb(u32x4{pk2(ks[16], ks[17]), 0u, 0u, 0u});
    const bf16x8 kb1 = asb(u32x4{pk2(ks[18], ks[19]), 0u, 0u, 0u});
    {
      u32x2 om_[MT], or_[MT];
#pragma unroll
      for (int m = 0; m < MT; ++m) {
        bf16x8 b0 = asb(u32x4{qf[m][0], qf[m][1], qf[m][2], qf[m][3]});
        bf16x8 b1 = asb(u32x4{qf[m][4], 0u, 0u, 0u});
        f32x4 n0 = mfma32(kb0, b1, mfma32(ka0, b0, z4));
        f32x4 n1 = mfma32(kb1, b1, mfma32(ka1, b0, z4));
        float da = n1[0], db = n1[0];
        swap16(da, db);
        const f32x2 inv = sp2(__builtin_amdgcn_rcpf(db + 1e-6f));
        om_[m] = u32x2{pk2v(lo2(n0) * inv), pk2v(hi2(n0) * inv)};
        or_[m] = u32x2{pk2v(lo2(n1) * inv), pk2v(hi2(n1) * inv)};
      }
      SCHED0();
      ldsp stg = sm + w * 17408 + 8192;
#pragma unroll
      for (int m = 0; m < MT; ++m) {
        *(LAS u32x2*)(stg + (m * 16 + fr) * 32 + g * 8) = om_[m];
        if (g == 0) *(LAS u32x2*)(stg + 3072 + (m * 16 + fr) * 8) = or_[m];
      }
      LGKM0();
      const int tok0 = r0 + mh * 95;
      const int ntok = mh == 0 ? 95 : nrow - 95;
      char* om = ws + WS_OM + ((size_t)head * S_ + tok0) * 32;
      char* orr = ws + WS_OR + ((size_t)head * S_ + tok0) * 8;
      u32x4 tv[3];
      u32x2 tr[2];
#pragma unroll
      for (int i = 0; i < 3; ++i) tv[i] = *(const LAS u32x4*)(stg + i * 1024 + lane * 16);
#pragma unroll
      for (int i = 0; i < 2; ++i) tr[i] = *(const LAS u32x2*)(stg + 3072 + ((i * 64 + lane) < 96 ? (i * 64 + lane) : 0) * 8);
#pragma unroll
      for (int i = 0; i < 3; ++i)
        if (i * 32 + (lane >> 1) < ntok) st128_sc1(om + i * 1024 + lane * 16, tv[i]);
#pragma unroll
      for (int i = 0; i < 2; ++i)
        if (i * 64 + lane < ntok) st64_sc1(orr + (i * 64 + lane) * 8, tr[i]);
    }
    WAITN_BAR(0);
    if (w == 0 && lane == 0) st64_sc1(ws + WS_OF + rg * 128 + ct * 8, ep);
    STAMPAT(8);
  }
  return ep;
}

DI u32x2 service(ldsp sm, const char* x, char* ws, int sid, int w, int lane) {
  const int bid = __builtin_amdgcn_workgroup_id_x();
  const int g = lane >> 4, fr = lane & 15;
  STAMPAT(0);
  if (w == 0) {
    unsigned long long nn = __builtin_amdgcn_s_memrealtime();
    u32x2 nv = u32x2{(unsigned)nn, (unsigned)(nn >> 32)};
    if (lane == 0) {
      if (sid != 15) st64_sc1(ws + WS_REQ + bid * 64, nv);
      *(LAS u32x2*)(sm + L_NONCE) = nv;
    }
  }
  float mean[6], rstd[6];
  if (sid < 12) {
    const int r0 = rs_of(sid), nrow = rs_of(sid + 1) - r0;
    u32x4 v[6][5];
#pragma unroll
    for (int i = 0; i < 6; ++i) {
      int slot = (i * 8 + w) * 4 + g;
      int ri = slot < 95 ? slot : slot - 1;
      ri = ri < nrow ? ri : nrow - 1;
      const char* p = x + (size_t)(r0 + ri) * 1280 + fr * 16;
#pragma unroll
      for (int c = 0; c < 5; ++c) v[i][c] = *(const u32x4*)(p + c * 256);
    }
#pragma unroll
    for (int i = 0; i < 6; ++i) {
      float a = 0.f;
#pragma unroll
      for (int c = 0; c < 5; ++c)
#pragma unroll
        for (int e = 0; e < 4; ++e) a += u2f(v[i][c][e] << 16) + u2f(v[i][c][e] & 0xffff0000u);
      a += dpp<0xB1>(a); a += dpp<0x4E>(a); a += dpp<0x141>(a); a += dpp<0x140>(a);
      mean[i] = a * (1.0f / 640.0f);
      float b = 0.f;
#pragma unroll
      for (int c = 0; c < 5; ++c)
#pragma unroll
        for (int e = 0; e < 4; ++e) {
          float lo = u2f(v[i][c][e] << 16) - mean[i], hi = u2f(v[i][c][e] & 0xffff0000u) - mean[i];
          b = __builtin_fmaf(lo, lo, b);
          b = __builtin_fmaf(hi, hi, b);
        }
      b += dpp<0xB1>(b); b += dpp<0x4E>(b); b += dpp<0x141>(b); b += dpp<0x140>(b);
      rstd[i] = __builtin_amdgcn_rsqf(b * (1.0f / 640.0f) + 1e-5f);
    }
  }
  LGKM_BAR();
  const u32x2 nv = *(const LAS u32x2*)(sm + L_NONCE);
  u32x2 ep;
  if (sid == 15) {
    ep = u32x2{nv[0] ^ 0x5bd1e995u, nv[1] ^ 0x80000000u};
    if (w < 4) {
      const int cb = w * 64 + lane;
      const bool me = (cb == bid);
      for (unsigned spins = 0; spins < SPIN_MAX; ++spins) {
        u32x2 rq = ld64_sc1(ws + WS_REQ + cb * 64);
        st128_sc1(ws + WS_ACK + cb * 64, u32x4{rq[0], rq[1], ep[0], ep[1]});
        u32x2 dn = ld64_sc1(ws + WS_DONE + cb * 64);
        if (all_ok(me || (dn[0] == ep[0] && dn[1] == ep[1]))) break;
        __builtin_amdgcn_s_sleep(16);
      }
    }
  } else {
    u32x4 ack;
    for (unsigned spins = 0; spins < SPIN_MAX; ++spins) {
      ack = ld128_sc1(ws + WS_ACK + bid * 64);
      if (all_ok(ack[0] == nv[0] && ack[1] == nv[1])) break;
      __builtin_amdgcn_s_sleep(4);
    }
    ep = u32x2{ack[2], ack[3]};
    if (w == 0 && lane == 0) st64_sc1(ws + WS_DONE + bid * 64, ep);
    if (sid < 12) {
#pragma unroll
      for (int i = 0; i < 6; ++i) {
        int slot = (i * 8 + w) * 4 + g;
        if (fr == 0 && slot < 176) st128_sc1(ws + WS_STAT + (size_t)(sid * 176 + slot) * 16, u32x4{f2u(mean[i]), f2u(rstd[i]), ep[0], ep[1]});
      }
    }
  }
  STAMPAT(1);
  return ep;
}

__global__ __launch_bounds__(512, 2) void fused(const char* __restrict__ x, const char* __restrict__ wqkv,
                                                const char* __restrict__ wphi, const char* __restrict__ wo,
                                                const char* __restrict__ wg, const char* __restrict__ wu,
                                                const char* __restrict__ wd, char* __restrict__ ws, char* __restrict__ out) {
  extern __shared__ __attribute__((aligned(16))) char smem[];
  ldsp sm = (ldsp)smem;
  const int tid = __builtin_amdgcn_workitem_id_x();
  const int b = __builtin_amdgcn_workgroup_id_x(), xcd = b & 7, idx = b >> 3;
  const int w = __builtin_amdgcn_readfirstlane(tid >> 6), lane = tid & 63;
  const u32x4 srd_wd = make_srd(wd, DF_ * D_ * 2);
  const int rgl = idx / 5, c5 = idx % 5;
  const int rg = (xcd >> 2) * 6 + rgl, cq = xcd & 3;
  if (idx < 30 && c5 >= 3) {
    const int ct = cq * 2 + (c5 - 3);
    if (w < 4) phase1<6, false>(sm, x, wqkv, wphi, wg, wu, srd_wd, ws, rg, ct, 0, 0, w, lane);
    else phase1<5, false>(sm, x, wqkv, wphi, wg, wu, srd_wd, ws, rg, ct, 0, 0, w, lane);
    return;
  }
  const int p2i = idx < 30 ? rgl * 3 + c5 : 18 + (idx - 30);
  const int rt = (xcd >> 1) * 4 + p2i / 5, ct2 = (xcd & 1) * 5 + p2i % 5;
#define ROTF ((((p2i % 5) * 12) / 5 + (p2i / 5) * 3) % 12)
  const int rot = ROTF;
  u32x2 ep;
  if (idx < 30) {
    const int ct = cq * 3 + c5;
    if (w < 4) ep = phase1<6, true>(sm, x, wqkv, wphi, wg, wu, srd_wd, ws, rg, ct, ct2 * 64, rot, w, lane);
    else ep = phase1<5, true>(sm, x, wqkv, wphi, wg, wu, srd_wd, ws, rg, ct, ct2 * 64, rot, w, lane);
  } else {
    ep = service(sm, x, ws, b - 240, w, lane);
    {
      const u32x4 srd_wo = make_srd(wo, D_ * D_ * 2);
      const unsigned vp = (lane >> 3) * 1280 + (lane & 7) * 16 + (xcd & 1) * 640;
      const unsigned dst = (unsigned)(size_t)sm + w * 1024;
      const int w16 = (idx - 30) * 8 + w;
      if (!(b == 255 && w < 4)) {
#pragma unroll 1
        for (int p = w16; p < 192; p += 16) {
#pragma unroll
          for (int c = 0; c < 5; ++c) dma_b(srd_wd, vp, p * 8 * 1280 + c * 128, dst);
          asm volatile("s_waitcnt vmcnt(30)" ::: "memory");
        }
#pragma unroll 1
        for (int p = w16; p < 80; p += 16) {
#pragma unroll
          for (int c = 0; c < 5; ++c) dma_b(srd_wo, vp, p * 8 * 1280 + c * 128, dst);
          asm volatile("s_waitcnt vmcnt(30)" ::: "memory");
        }
      }
      WAITN_BAR(0);
    }
    const unsigned vB = p2_vB(w, lane);
    const unsigned smb = (unsigned)(size_t)sm;
    p2_issueB_down(srd_wd, vB, ct2 * 64, w, smb, 0, rot);
    p2_issueB_down(srd_wd, vB, ct2 * 64, w, smb, 1, rot);
    p2_issueB_down(srd_wd, vB, ct2 * 64, w, smb, 2, rot);
  }
  phase2(sm, x, wo, srd_wd, ws, out, rt, ct2, rot, w, lane, ep);
}

#if defined(__HIP_DEVICE_COMPILE__)

#endif

extern "C" void kernel_launch(void* const* d_in, const int* in_sizes, int n_in, void* d_out, int out_size, void* d_ws,
                              size_t ws_size, hipStream_t stream) {
  (void)hipFuncSetAttribute((const void*)fused, hipFuncAttributeMaxDynamicSharedMemorySize, K_LDS);
  fused<<<256, 512, K_LDS, stream>>>((const char*)d_in[0], (const char*)d_in[1], (const char*)d_in[2], (const char*)d_in[3],
                                     (const char*)d_in[5], (const char*)d_in[6], (const char*)d_in[7], (char*)d_ws, (char*)d_out);
}
```

```cpp
#define POLLS 6
#include <hip/hip_runtime.h>
#include <hip/hip_bf16.h>
#include <stdint.h>

typedef __attribute__((ext_vector_type(8))) __bf16 bf16x8;
typedef __attribute__((ext_vector_type(2))) __bf16 bf16x2;
typedef __attribute__((ext_vector_type(4))) short s16x4;
typedef __attribute__((ext_vector_type(8))) short s16x8;
typedef __attribute__((ext_vector_type(4))) float f32x4;
typedef __attribute__((ext_vector_type(2))) float f32x2;
typedef __attribute__((ext_vector_type(4))) unsigned u32x4;
typedef __attribute__((ext_vector_type(2))) unsigned u32x2;
#define LAS __attribute__((address_space(3)))
#define DI __device__ __forceinline__
typedef LAS char* ldsp;

constexpr int S_ = 2048, D_ = 640, DF_ = 1536, NH = 32;
constexpr int NRG = 12;
constexpr size_t WS_H = 0;
constexpr size_t WS_KV = WS_H + (size_t)S_ * DF_ * 2;
constexpr size_t WS_OM = WS_KV + (size_t)NRG * NH * 4096;
constexpr size_t WS_OR = WS_OM + (size_t)NH * S_ * 32;
constexpr size_t WS_REQ = WS_OR + (size_t)NH * S_ * 8;
constexpr size_t WS_ACK = WS_REQ + 256 * 64;
constexpr size_t WS_DONE = WS_ACK + 256 * 64;
constexpr size_t WS_STAT = WS_DONE + 256 * 64;
constexpr size_t WS_HF = WS_STAT + 12 * 176 * 16;
constexpr size_t WS_KF = WS_HF + 12 * 128;
constexpr size_t WS_OF = WS_KF + 8 * 128;
constexpr size_t WS_DBG = 64u << 20;

constexpr int A_SLOT = 22528, B_SLOT = 16384;
constexpr int L_A = 0;
constexpr int L_B = 3 * A_SLOT;
constexpr int L_MISC = L_B + 5 * B_SLOT;
constexpr int L_DUMMY = L_MISC;
constexpr int L_NONCE = L_MISC + 1024;
constexpr int L_CS = L_NONCE + 64;
constexpr int K_LDS = L_CS + 2048;
constexpr int P2_A = 0, P2_B = 98304;
constexpr unsigned SPIN_MAX = 1u << 20;

#if defined(__HIP_DEVICE_COMPILE__)

#endif

#define STAMPAT(i)

DI float u2f(unsigned u) { return __builtin_bit_cast(float, u); }
DI unsigned f2u(float f) { return __builtin_bit_cast(unsigned, f); }
DI u32x4 make_srd(const void* p, unsigned bytes) {
  uint64_t a = (uint64_t)p;
  u32x4 r;
  r[0] = (unsigned)a;
  r[1] = (unsigned)(a >> 32) & 0xffffu;
  r[2] = bytes;
  r[3] = 0x00020000u;
  return r;
}
DI void dma_b(u32x4 srd, unsigned voff, unsigned soff, unsigned m0v) {
  asm volatile("s_mov_b32 m0, %3\n\ts_nop 0\n\tbuffer_load_dwordx4 %0, %1, %2 offen lds" ::"v"(voff), "s"(srd), "s"(soff), "s"(m0v) : "memory");
}
DI void dma_b_mask(u32x4 srd, unsigned voff, unsigned soff, unsigned m0v, unsigned long long mask) {
  asm volatile("s_mov_b32 m0, %3\n\ts_mov_b64 exec, %4\n\tbuffer_load_dwordx4 %0, %1, %2 offen lds\n\ts_mov_b64 exec, -1" ::"v"(voff), "s"(srd), "s"(soff), "s"(m0v), "s"(mask) : "memory");
}
DI void dma_b_sc1(u32x4 srd, unsigned voff, unsigned soff, unsigned m0v) {
  asm volatile("s_mov_b32 m0, %3\n\ts_nop 0\n\tbuffer_load_dwordx4 %0, %1, %2 offen sc1 lds" ::"v"(voff), "s"(srd), "s"(soff), "s"(m0v) : "memory");
}
DI u32x4 ld128_sc1(const char* p) { u32x4 v; asm volatile("global_load_dwordx4 %0, %1, off sc1\n\ts_waitcnt vmcnt(0)" : "=&v"(v) : "v"(p) : "memory"); return v; }
DI u32x2 ld64_sc1(const char* p) { u32x2 v; asm volatile("global_load_dwordx2 %0, %1, off sc1\n\ts_waitcnt vmcnt(0)" : "=&v"(v) : "v"(p) : "memory"); return v; }
DI void st128_sc1(char* p, u32x4 v) { asm volatile("global_store_dwordx4 %0, %1, off sc1\n\ts_nop 1" ::"v"(p), "v"(v) : "memory"); }
DI void st64_sc1(char* p, u32x2 v) { asm volatile("global_store_dwordx2 %0, %1, off sc1" ::"v"(p), "v"(v) : "memory"); }
#define WAITN_BAR(N) asm volatile("s_waitcnt vmcnt(%0) lgkmcnt(0)\n\ts_barrier" ::"n"(N) : "memory")
#define LGKM_BAR() asm volatile("s_waitcnt lgkmcnt(0)\n\ts_barrier" ::: "memory")
#define LGKM0() asm volatile("s_waitcnt lgkmcnt(0)" ::: "memory")
#define MEMFENCE() asm volatile("" ::: "memory")
#define SCHED0() __builtin_amdgcn_sched_barrier(0)

DI bf16x8 ld128(ldsp p) { return *(const LAS bf16x8*)p; }
DI s16x4 tr1(ldsp p) { return __builtin_amdgcn_ds_read_tr16_b64_v4i16((LAS s16x4*)p); }
DI bf16x8 cat(s16x4 a, s16x4 b) { return __builtin_bit_cast(bf16x8, __builtin_shufflevector(a, b, 0, 1, 2, 3, 4, 5, 6, 7)); }
DI bf16x8 tr2(ldsp p0, ldsp p1) { return cat(tr1(p0), tr1(p1)); }
DI f32x4 mfma32(bf16x8 a, bf16x8 b, f32x4 c) { return __builtin_amdgcn_mfma_f32_16x16x32_bf16(a, b, c, 0, 0, 0); }
DI bf16x8 asb(u32x4 v) { return __builtin_bit_cast(bf16x8, v); }
DI unsigned pk2(float a, float b) {
  bf16x2 v;
  v[0] = (__bf16)a;
  v[1] = (__bf16)b;
  return __builtin_bit_cast(unsigned, v);
}
DI void swap16(float& a, float& b) {
  auto r = __builtin_amdgcn_permlane16_swap(f2u(a), f2u(b), false, false);
  a = u2f(r[0]); b = u2f(r[1]);
}
DI void swap32(float& a, float& b) {
  auto r = __builtin_amdgcn_permlane32_swap(f2u(a), f2u(b), false, false);
  a = u2f(r[0]); b = u2f(r[1]);
}
template <int CTRL> DI float dpp(float x) { return u2f(__builtin_amdgcn_update_dpp(0u, f2u(x), CTRL, 0xf, 0xf, false)); }
DI float fmx(float a, float b) { return __builtin_fmaxf(a, b); }
DI float fmn(float a, float b) { return __builtin_fminf(a, b); }
DI float rawmax(float a, float b) { float r; asm("v_max_f32 %0, %1, %2" : "=v"(r) : "v"(a), "v"(b)); return r; }
DI float opq(float x) { asm("" : "+v"(x)); return x; }
DI f32x2 lo2(f32x4 v) { return __builtin_shufflevector(v, v, 0, 1); }
DI f32x2 hi2(f32x4 v) { return __builtin_shufflevector(v, v, 2, 3); }
DI f32x2 sp2(float x) { return f32x2{x, x}; }
DI f32x2 fma2(f32x2 a, f32x2 b, f32x2 c) { return __builtin_elementwise_fma(a, b, c); }
DI int rs_of(int rg) { return (512 * rg) / 3; }
DI int rg_of(int row) { return (3 * row + 2) >> 9; }
DI bool all_ok(bool ok) { return __builtin_amdgcn_ballot_w64(ok) == ~0ull; }

DI void wait_flags(const char* p, bool need, u32x2 ep) {
  for (unsigned spins = 0; spins < SPIN_MAX; ++spins) {
    u32x2 v = ld64_sc1(p);
    if (all_ok(!need || (v[0] == ep[0] && v[1] == ep[1]))) break;
    __builtin_amdgcn_s_sleep(POLLS);
  }
}

DI f32x2 ex2(f32x2 v) { return f32x2{__builtin_amdgcn_exp2f(v[0]), __builtin_amdgcn_exp2f(v[1])}; }
DI unsigned pk2v(f32x2 v) { return pk2(v[0], v[1]); }
DI void featmap2(f32x2 a01, f32x2 a23, f32x2 b01, f32x2 b23, f32x2 e4, bool livea, bool liveb, int g, unsigned (&oa)[5], unsigned (&ob)[5]) {
  float a0 = fmx(fmx(fmx(a01[0], a01[1]), a23[0]), fmx(a23[1], e4[0]));
  float a1 = -fmn(fmn(fmn(a01[0], a01[1]), a23[0]), fmn(a23[1], e4[0]));
  float b0 = fmx(fmx(fmx(b01[0], b01[1]), b23[0]), fmx(b23[1], e4[1]));
  float b1 = -fmn(fmn(fmn(b01[0], b01[1]), b23[0]), fmn(b23[1], e4[1]));
  swap16(a0, a1); swap16(b0, b1);
  float ca = rawmax(a0, a1), cb = rawmax(b0, b1);
  swap32(ca, cb);
  float d0 = rawmax(ca, cb), d1 = d0;
  swap32(d0, d1);
  float Ma = d0, Na = d0, Mb = d1, Nb = d1;
  swap16(Ma, Na); swap16(Mb, Nb);
  const f32x2 pa01 = ex2(a01 - sp2(Ma)), pa23 = ex2(a23 - sp2(Ma)), na01 = ex2(-a01 - sp2(Na)), na23 = ex2(-a23 - sp2(Na));
  const f32x2 pb01 = ex2(b01 - sp2(Mb)), pb23 = ex2(b23 - sp2(Mb)), nb01 = ex2(-b01 - sp2(Nb)), nb23 = ex2(-b23 - sp2(Nb));
  const float pa4 = __builtin_amdgcn_exp2f(e4[0] - Ma), na4 = __builtin_amdgcn_exp2f(-e4[0] - Na);
  const float pb4 = __builtin_amdgcn_exp2f(e4[1] - Mb), nb4 = __builtin_amdgcn_exp2f(-e4[1] - Nb);
  f32x2 t;
  t = pa01 + pa23; a0 = (t[0] + t[1]) + pa4;
  t = na01 + na23; a1 = (t[0] + t[1]) + na4;
  t = pb01 + pb23; b0 = (t[0] + t[1]) + pb4;
  t = nb01 + nb23; b1 = (t[0] + t[1]) + nb4;
  swap16(a0, a1); swap16(b0, b1);
  ca = a0 + a1; cb = b0 + b1;
  swap32(ca, cb);
  d0 = __builtin_amdgcn_rcpf(ca + cb);
  if (!(g < 2 ? livea : liveb)) d0 = 0.f;
  d1 = d0;
  swap32(d0, d1);
  Ma = d0; Na = d0; Mb = d1; Nb = d1;
  swap16(Ma, Na); swap16(Mb, Nb);
  oa[0] = pk2v(pa01 * sp2(Ma)); oa[1] = pk2v(pa23 * sp2(Ma));
  oa[2] = pk2v(na01 * sp2(Na)); oa[3] = pk2v(na23 * sp2(Na));
  oa[4] = pk2(pa4 * Ma, na4 * Na);
  ob[0] = pk2v(pb01 * sp2(Mb)); ob[1] = pk2v(pb23 * sp2(Mb));
  ob[2] = pk2v(nb01 * sp2(Nb)); ob[3] = pk2v(nb23 * sp2(Nb));
  ob[4] = pk2(pb4 * Mb, nb4 * Nb);
}

struct P2Ctx { unsigned vB, vR; int c0; };
DI unsigned p2_vB(int w, int lane) {
  const int l3 = lane >> 3, l7 = lane & 7;
  int fb = ((l3 >> 1) & 1) | ((w & 1) << 1);
  int ch = (((l7 >> 1) ^ fb) << 1) | (l7 & 1);
  return l3 * 1280 + ch * 16;
}
DI int p2_rot(int s, int rot) { int r = s + rot; return r >= 12 ? r - 12 : r; }
DI void p2_issueB_down(u32x4 srd_wd, unsigned vB, int c0, int w, unsigned smb, int s, int rot) {
  const int sr = p2_rot(s, rot);
  dma_b(srd_wd, vB, (128 * sr + 8 * w) * 1280 + c0 * 2, smb + P2_B + (s % 3) * 16384 + w * 1024);
  dma_b(srd_wd, vB, (128 * sr + 8 * w + 64) * 1280 + c0 * 2, smb + P2_B + (s % 3) * 16384 + w * 1024 + 8192);
}

DI void phase2(ldsp sm, const char* x, const char* wo, const u32x4 srd_wd, char* ws, char* out, int rt, int ct, int rot, int w, int lane, u32x2 ep) {
  const int r0 = rt * 128, c0 = ct * 64;
  const int g = lane >> 4, fr = lane & 15, l3 = lane >> 3, l7 = lane & 7;
  const int kh = w & 1, nh = (w >> 1) & 1, mh = w >> 2;
  const unsigned smb = (unsigned)(size_t)sm;
  const u32x4 srd_h = make_srd(ws + WS_H, S_ * DF_ * 2);
  const u32x4 srd_wo = make_srd(wo, D_ * D_ * 2);
  const u32x4 srd_om = make_srd(ws + WS_OM, NH * S_ * 32);
  const u32x4 srd_or = make_srd(ws + WS_OR, NH * S_ * 8);
  const unsigned vB = p2_vB(w, lane);
  const unsigned vA = l3 * 3072 + ((l7 ^ ((4 * (w & 1) + (l3 >> 1)) & 7)) * 16);
  const unsigned vR = vB - l3 * 1280 + (20 * (lane >> 5) + 16 + (l3 & 3)) * 1280;
  const unsigned vL = lane * 16;
  const int rgl = rg_of(r0), rgh = rg_of(r0 + 127);

#define P2_PIECE(s, p) do { \
    constexpr int i_ = (p) < 2 ? (p) : (p) - 2; \
    if ((s) < 12) { \
      if ((p) < 2) dma_b(srd_wd, vB, (128 * p2_rot((s), rot) + 8 * w + 64 * i_) * 1280 + c0 * 2, smb + P2_B + ((s) % 3) * 16384 + i_ * 8192 + w * 1024); \
      else dma_b_sc1(srd_h, vA, (r0 + 8 * w + 64 * (i_ & 1)) * 3072 + p2_rot((s), rot) * 256 + (i_ >> 1) * 128, smb + P2_A + ((s) % 3) * 32768 + (i_ >> 1) * 16384 + (i_ & 1) * 8192 + w * 1024); \
    } else if ((s) < 16) { \
      if ((p) < 2) dma_b(srd_wo, vB, (160 * ((s) - 12) + 20 * ((w + 8 * i_) >> 1) + 8 * (w & 1)) * 1280 + c0 * 2, smb + P2_B + ((s) % 3) * 16384 + i_ * 8192 + w * 1024); \
      else dma_b_sc1(srd_om, vL, ((8 * ((s) - 12) + 2 * i_ + (w >> 2)) * S_ + r0) * 32 + (w & 3) * 1024, smb + P2_A + ((s) % 3) * 32768 + i_ * 8192 + w * 1024); \
    } else { \
      if ((p) < 2) dma_b(srd_wo, vR, (40 * (w + 8 * i_)) * 1280 + c0 * 2, smb + P2_B + ((s) % 3) * 16384 + i_ * 8192 + w * 1024); \
      else dma_b_sc1(srd_or, vL, ((8 * i_ + w) * S_ + r0) * 8, smb + P2_A + ((s) % 3) * 32768 + i_ * 8192 + w * 1024); \
    } } while (0)

  const int aD = P2_A + kh * 16384 + (64 * mh + fr) * 128 + ((g ^ ((fr >> 1) & 7)) * 16);
  const int aM = P2_A + (4 * kh + (g >> 1)) * 4096 + (64 * mh + fr) * 32 + (g & 1) * 16;
  const int aR = P2_A + (16 * kh + 2 * g) * 1024 + (64 * mh + fr) * 8;
  int bO[2];
  {
    const int q = fr >> 2, p = lane & 3;
    const int f = ((q >> 1) & 1) | ((g & 1) << 1);
#pragma unroll
    for (int j = 0; j < 2; ++j) bO[j] = P2_B + (64 * kh + 8 * g + q) * 128 + (((2 * nh + j) ^ f) * 32) + p * 8;
  }
  f32x4 acc[4][2];
#pragma unroll
  for (int m = 0; m < 4; ++m)
#pragma unroll
    for (int j = 0; j < 2; ++j) acc[m][j] = f32x4{0.f, 0.f, 0.f, 0.f};

  STAMPAT(8);
  if (w == 0) wait_flags(ws + WS_HF + (size_t)(lane < 16 ? rgl : rgh) * 128 + (lane & 15) * 8, lane < 32 && (lane & 15) < 12, ep);
  WAITN_BAR(0);
  STAMPAT(9);
#define P2_A3(s) do { P2_PIECE(s, 2); P2_PIECE(s, 3); P2_PIECE(s, 4); P2_PIECE(s, 5); } while (0)
  P2_A3(0); P2_A3(1); P2_A3(2);
  u32x4 xr[2];
  bf16x8 fa[2][8];
  s16x4 fb[2][8];
#define P2_RD(s, i) do { \
    if ((i) < 8) fb[(s) & 1][i] = tr1(sm + ((s) % 3) * 16384 + bO[((i) >> 1) & 1] + ((i) >> 2) * 4096 + ((i) & 1) * 512); \
    else if ((i) < 16) { \
      constexpr int sl_ = ((i) - 8) >> 2, m_ = ((i) - 8) & 3; \
      if ((s) < 12) fa[(s) & 1][((i) - 8) & 7] = ld128(sm + ((s) % 3) * 32768 + (sl_ ? (aD ^ 64) : aD) + m_ * 2048); \
      else if ((s) < 16) fa[(s) & 1][((i) - 8) & 7] = ld128(sm + ((s) % 3) * 32768 + aM + sl_ * 8192 + m_ * 512); \
      else fa[(s) & 1][((i) - 8) & 7] = cat(*(const LAS s16x4*)(sm + ((s) % 3) * 32768 + aR + sl_ * 8192 + m_ * 128), *(const LAS s16x4*)(sm + ((s) % 3) * 32768 + aR + sl_ * 8192 + 1024 + m_ * 128)); } } while (0)
#define P2_MM(s, i) acc[((i) >> 1) & 3][(i) & 1] = mfma32(cat(fb[(s) & 1][((i) >> 3) * 4 + 2 * ((i) & 1)], fb[(s) & 1][((i) >> 3) * 4 + 2 * ((i) & 1) + 1]), fa[(s) & 1][((i) >> 3) * 4 + (((i) >> 1) & 3)], acc[((i) >> 1) & 3][(i) & 1])
#define P2_GROUP(s, q) do { \
    if ((s) >= 1 && (s) + 2 < 17) { \
      if ((s) + 2 == 12 && (q) == 2) { \
        STAMPAT(10); \
        if (w == 0) wait_flags(ws + WS_OF + (size_t)(lane < 16 ? rgl : rgh) * 128 + (lane & 15) * 8, lane < 32 && (lane & 15) < 8, ep); \
        asm volatile("s_barrier" ::: "memory"); \
        STAMPAT(11); \
      } \
      P2_PIECE((s) + 2, q); } \
    SCHED0(); \
    if ((s) < 17) { P2_RD(s, 3 * (q)); P2_RD(s, 3 * (q) + 1); P2_RD(s, 3 * (q) + 2); } \
    SCHED0(); \
    if ((s) >= 1) { P2_MM((s) - 1, ((q) * 8) / 3); P2_MM((s) - 1, ((q) * 8) / 3 + 1); if (((q) * 8) / 3 + 2 < (((q) + 1) * 8) / 3) P2_MM((s) - 1, ((q) * 8) / 3 + 2); } \
    SCHED0(); } while (0)
#define P2_STAGE(s) do { \
    SCHED0(); \
    if ((s) < 17) WAITN_BAR((s) == 0 ? 8 : ((s) == 1 ? 4 : ((s) < 16 ? 6 : 0))); \
    SCHED0(); \
    P2_GROUP(s, 0); P2_GROUP(s, 1); P2_GROUP(s, 2); P2_GROUP(s, 3); P2_GROUP(s, 4); P2_GROUP(s, 5); } while (0)
  P2_STAGE(0); P2_STAGE(1); P2_STAGE(2); P2_STAGE(3); P2_STAGE(4); P2_STAGE(5); P2_STAGE(6); P2_STAGE(7); P2_STAGE(8);
  P2_STAGE(9); P2_STAGE(10); P2_STAGE(11); P2_STAGE(12); P2_STAGE(13); P2_STAGE(14); P2_STAGE(15);
  const int orow = 64 * mh + 32 * kh + (lane >> 2), ocol = c0 * 2 + nh * 64 + (lane & 3) * 16;
#pragma unroll
  for (int i = 0; i < 2; ++i)
    asm volatile("global_load_dwordx4 %0, %1, off" : "=&v"(xr[i]) : "v"(x + (size_t)(r0 + orow + 16 * i) * 1280 + ocol) : "memory");
  P2_STAGE(16); P2_STAGE(17);
  SCHED0();
  STAMPAT(12);
  f32x4 keep[2][2];
  {
    ldsp ex = sm + w * 4096 + lane * 16;
#pragma unroll
    for (int i = 0; i < 2; ++i)
#pragma unroll
      for (int j = 0; j < 2; ++j) {
        f32x4 snd;
#pragma unroll
        for (int e = 0; e < 4; ++e) {
          const float lo_ = acc[i][j][e], hi_ = acc[2 + i][j][e];
          snd[e] = kh ? lo_ : hi_;
          keep[i][j][e] = kh ? hi_ : lo_;
        }
        *(LAS f32x4*)(ex + (i * 2 + j) * 1024) = snd;
      }
  }
  LGKM_BAR();
  {
    ldsp ex = sm + (w ^ 1) * 4096 + lane * 16;
    ldsp tp = sm + 65536 + w * 4608;
#pragma unroll
    for (int i = 0; i < 2; ++i)
#pragma unroll
      for (int j = 0; j < 2; ++j) {
        const f32x4 e = *(const LAS f32x4*)(ex + (i * 2 + j) * 1024);
        *(LAS f32x4*)(tp + (i * 16 + fr) * 144 + j * 64 + g * 16) = keep[i][j] + e;
      }
    asm volatile("" : "+v"(xr[0]), "+v"(xr[1]));
#pragma unroll
    for (int i = 0; i < 2; ++i) {
      const f32x4 s0 = *(const LAS f32x4*)(tp + (i * 16 + (lane >> 2)) * 144 + (lane & 3) * 32);
      const f32x4 s1 = *(const LAS f32x4*)(tp + (i * 16 + (lane >> 2)) * 144 + (lane & 3) * 32 + 16);
      u32x4 o;
#pragma unroll
      for (int e = 0; e < 2; ++e) {
        o[e] = pk2(s0[2 * e] + u2f(xr[i][e] << 16), s0[2 * e + 1] + u2f(xr[i][e] & 0xffff0000u));
        o[2 + e] = pk2(s1[2 * e] + u2f(xr[i][2 + e] << 16), s1[2 * e + 1] + u2f(xr[i][2 + e] & 0xffff0000u));
      }
      *(u32x4*)(out + (size_t)(r0 + orow + 16 * i) * 1280 + ocol) = o;
    }
  }
}

template <int MT, bool GU>
DI u32x2 phase1(ldsp sm, const char* x, const char* wqkv, const char* wphi, const char* wg, const char* wu, const u32x4 srd_wd,
                char* ws, int rg, int ct, int c0p2, int rot, int w, int lane) {
  STAMPAT(0);
  constexpr int mh = (MT == 6) ? 0 : 1;
  const int wn = w & 3;
  const int r0 = rs_of(rg), nrow = rs_of(rg + 1) - r0;
  const unsigned smb = (unsigned)(size_t)sm;
  const int g = lane >> 4, fr = lane & 15;

  const u32x4 srdx = make_srd(x, S_ * D_ * 2);
  unsigned voffA[3];
#pragma unroll
  for (int c = 0; c < 3; ++c) {
    int slot = 8 * (w + 8 * c) + (lane >> 3);
    int ch = (lane & 7) ^ ((slot >> 1) & 7);
    int ri = slot < 95 ? slot : slot - 1;
    ri = ri < nrow ? ri : nrow - 1;
    voffA[c] = (unsigned)((r0 + ri) * 1280 + ch * 16);
  }
  if (w >= 6) voffA[2] = 0x40000000u;
  const unsigned long long FULL = ~0ull, PART = 0x00FFFFFFFFFFFFFFull;
  const unsigned long long mask1 = (w == 3) ? PART : FULL, mask2 = (w == 5) ? PART : FULL;
  const unsigned adst0 = smb + L_A + w * 1024;
  u32x4 srd0, srd1;
  unsigned voff0, voff1;
  constexpr unsigned SSTEP = GU ? 32 * 3072 : 32 * 3840;
  if (GU) {
    srd0 = make_srd(wg, D_ * DF_ * 2);
    srd1 = make_srd(wu, D_ * DF_ * 2);
    int krow = 4 * w + (lane >> 4);
    int chp = lane & 15;
    int f = (krow & 3) | (((krow >> 3) & 1) << 2);
    int ch = (((chp >> 1) ^ f) << 1) | (chp & 1);
    voff0 = voff1 = krow * 3072 + ct * 256 + ch * 16;
  } else {
    srd0 = srd1 = make_srd(wqkv, D_ * 3 * D_ * 2);
    unsigned vo[2];
#pragma unroll
    for (int c = 0; c < 2; ++c) {
      int cidx = (w + 8 * c) * 64 + lane;
      int rho = cidx / 30, cc = cidx % 30;
      int seg = cc / 10, ch = cc % 10;
      int k = (rho & 16) | ((rho & 4) << 1) | ((rho & 8) >> 1) | (rho & 3);
      vo[c] = (cidx < 960) ? (unsigned)(k * 3840 + seg * 1280 + ct * 160 + ch * 16) : 0x40000000u;
    }
    voff0 = vo[0];
    voff1 = vo[1];
  }
  const unsigned bdst0 = smb + L_B + w * 1024;
#define KROT (((rg%6)*5/3+ct*2)%10)
  const unsigned krot = KROT;
#define RA(so) ((so) + krot * 128u >= 1280u ? (so) + krot * 128u - 1280u : (so) + krot * 128u)
#define RB(so) ((so) + krot * 2u * SSTEP >= 20u * SSTEP ? (so) + krot * 2u * SSTEP - 20u * SSTEP : (so) + krot * 2u * SSTEP)
#define DMA_B0(soff, dst) dma_b(srd0, voff0, RB(soff), (dst))
#define DMA_B1(soff, dst) dma_b(srd1, voff1, RB(soff), (dst))
#define DMA_A0(soff, dst) dma_b(srdx, voffA[0], RA(soff), (dst))
#define DMA_A1(soff, dst) dma_b_mask(srdx, voffA[1], RA(soff), (dst), mask1)
#define DMA_A2(soff, dst) dma_b_mask(srdx, voffA[2], RA(soff), (dst), mask2)
  const unsigned a2base = (w >= 6) ? smb + L_DUMMY : adst0 + 16384;
  const unsigned a2mul = (w >= 6) ? 0u : 1u;
  unsigned wraw[16];
  if (!GU) {
    const char* wp = wphi + (ct * 4 + wn) * 800;
    const char* p0 = wp + (5 * (fr >> 2) + (fr & 3)) * 2;
    const char* p1 = wp + (5 * (fr >> 2) + 4) * 2;
    const char* q0 = p0 + g * 160;
    const char* q1 = p1 + g * 160;
#pragma unroll
    for (int j = 0; j < 4; ++j) {
      asm volatile("global_load_ushort %0, %1, off offset:%2" : "=&v"(wraw[j]) : "v"(q0), "n"(j * 40) : "memory");
      asm volatile("global_load_ushort %0, %1, off offset:%2" : "=&v"(wraw[4 + j]) : "v"(p0), "n"(640 + j * 40) : "memory");
      asm volatile("global_load_ushort %0, %1, off offset:%2" : "=&v"(wraw[8 + j]) : "v"(q1), "n"(j * 40) : "memory");
      asm volatile("global_load_ushort %0, %1, off offset:%2" : "=&v"(wraw[12 + j]) : "v"(p1), "n"(640 + j * 40) : "memory");
    }
  }
  DMA_B0(0, bdst0); DMA_B1(0, bdst0 + 8192);
  DMA_A0(0, adst0); DMA_A1(0, adst0 + 8192);
  DMA_A2(0, a2base);
  DMA_B0(SSTEP, bdst0 + B_SLOT); DMA_B1(SSTEP, bdst0 + B_SLOT + 8192);
  DMA_B0(2 * SSTEP, bdst0 + 2 * B_SLOT); DMA_B1(2 * SSTEP, bdst0 + 2 * B_SLOT + 8192);
  DMA_A0(128, adst0 + A_SLOT); DMA_A1(128, adst0 + A_SLOT + 8192);
  DMA_A2(128, a2base + a2mul * A_SLOT);
  DMA_B0(3 * SSTEP, bdst0 + 3 * B_SLOT); DMA_B1(3 * SSTEP, bdst0 + 3 * B_SLOT + 8192);
  DMA_B0(4 * SSTEP, bdst0 + 4 * B_SLOT); DMA_B1(4 * SSTEP, bdst0 + 4 * B_SLOT + 8192);
  DMA_A0(256, adst0 + 2 * A_SLOT); DMA_A1(256, adst0 + 2 * A_SLOT + 8192);
  if (w == 0 && lane < 48) {
    int rr = lane >> 3;
    *(LAS u32x4*)(sm + L_A + (rr >> 1) * A_SLOT + ((rr & 1) ? 175 : 95) * 128 + (lane & 7) * 16) =
        u32x4{0x3F803F80u, 0x3F803F80u, 0x3F803F80u, 0x3F803F80u};
  }
  const int bid = __builtin_amdgcn_workgroup_id_x();
  if (w == 1) {
    unsigned long long nn = __builtin_amdgcn_s_memrealtime();
    u32x2 nv = u32x2{(unsigned)nn, (unsigned)(nn >> 32)};
    if (lane == 0) {
      st64_sc1(ws + WS_REQ + bid * 64, nv);
      *(LAS u32x2*)(sm + L_NONCE) = nv;
    }
  }

  const int aoff0 = L_A + mh * 6 * 2048 + fr * 128 + ((g ^ ((fr >> 1) & 7)) * 16);
  const int aoff1 = aoff0 ^ 64;
  int boff[4];
  constexpr int TSTEP = GU ? 1024 : 3840;
  {
    const int q = fr >> 2, p = lane & 3;
    if (GU) {
      int f = q | ((g & 1) << 2);
#pragma unroll
      for (int j = 0; j < 4; ++j)
        boff[j] = L_B + (j >> 1) * 8192 + (8 * g + q) * 256 + (((2 * wn + (j & 1)) ^ f) * 32) + p * 8;
    } else {
      int rho = 16 * (g >> 1) + 4 * (g & 1) + q;
#pragma unroll
      for (int j = 0; j < 3; ++j) boff[j] = L_B + rho * 480 + (20 * j + 5 * wn + p) * 8;
      boff[3] = L_B + rho * 480 + (20 * (p == 3 ? 0 : p) + 5 * wn + 4) * 8;
    }
  }

  f32x4 acc[MT][4];
#pragma unroll
  for (int m = 0; m < MT; ++m)
#pragma unroll
    for (int j = 0; j < 4; ++j) acc[m][j] = f32x4{0.f, 0.f, 0.f, 0.f};
  bf16x8 fa0[MT], fa1[MT];
  s16x4 fb0[8], fb1[8];

  STAMPAT(1);
  WAITN_BAR(13);
  STAMPAT(2);
  unsigned wpk[8];
  if (!GU) {
    asm volatile("" : "+v"(wraw[0]), "+v"(wraw[1]), "+v"(wraw[2]), "+v"(wraw[3]), "+v"(wraw[4]), "+v"(wraw[5]), "+v"(wraw[6]), "+v"(wraw[7]),
                 "+v"(wraw[8]), "+v"(wraw[9]), "+v"(wraw[10]), "+v"(wraw[11]), "+v"(wraw[12]), "+v"(wraw[13]), "+v"(wraw[14]), "+v"(wraw[15]));
#pragma unroll
    for (int i = 0; i < 8; ++i) {
      wpk[i] = wraw[2 * i] | (wraw[2 * i + 1] << 16);
      if (i >= 4 && (fr & 3) != 0) wpk[i] = 0u;
    }
    asm volatile("" : "+v"(wpk[0]), "+v"(wpk[1]), "+v"(wpk[2]), "+v"(wpk[3]), "+v"(wpk[4]), "+v"(wpk[5]), "+v"(wpk[6]), "+v"(wpk[7]));
  }
#pragma unroll
  for (int m = 0; m < MT; ++m) fa0[m] = ld128(sm + aoff0 + m * 2048);
#pragma unroll
  for (int j = 0; j < 8; ++j) fb0[j] = tr1(sm + boff[j >> 1] + (j & 1) * TSTEP);
  MEMFENCE();

  constexpr int NM = MT * 4;
  constexpr int DS = (MT == 6) ? 6 : 5;
#define MMA(i, FA, FB) acc[(i) >> 2][(i) & 3] = mfma32(cat(FB[2 * ((i) & 3)], FB[2 * ((i) & 3) + 1]), FA[(i) >> 2], acc[(i) >> 2][(i) & 3])
#define BADDR(sl, i) (sm + (sl) * B_SLOT + boff[(i) >> 1] + ((i) & 1) * TSTEP)
  u32x4 ackv, stv[MT];
  const char* ackp = ws + WS_ACK + bid * 64;
  const char* stp = ws + WS_STAT + (size_t)(rg * 176 + mh * 96 + fr) * 16;
#pragma unroll
  for (int t = 0; t < 10; ++t) {
    SCHED0();
    WAITN_BAR(t < 8 ? 11 : (t == 8 ? 7 : 1 + MT));
    SCHED0();
#pragma unroll
    for (int i = 0; i < NM; ++i) {
      MMA(i, fa0, fb0);
      SCHED0();
      if (i < 8) fb1[i] = tr1(BADDR((2 * t + 1) % 5, i));
      else if (i < 8 + MT) fa1[i - 8] = ld128(sm + (t % 3) * A_SLOT + aoff1 + (i - 8) * 2048);
      if (t < 8) {
        if (i == 2) { if (MT == 6) DMA_A2((t + 2) * 128, adst0 + 16384 + ((t + 2) % 3) * A_SLOT); else DMA_A2((t + 2) * 128, a2base + a2mul * (((t + 2) % 3) * A_SLOT)); }
        if (i == 2 + DS) DMA_B0((2 * t + 5) * SSTEP, bdst0 + ((2 * t) % 5) * B_SLOT);
        if (i == 2 + 2 * DS) DMA_B1((2 * t + 5) * SSTEP, bdst0 + ((2 * t) % 5) * B_SLOT + 8192);
      }
      SCHED0();
    }
    if (t < 9) WAITN_BAR(t < 8 ? 9 : 2); else LGKM_BAR();
    SCHED0();
#pragma unroll
    for (int i = 0; i < NM; ++i) {
      MMA(i, fa1, fb1);
      SCHED0();
      if (t < 9) {
        if (i < 8) fb0[i] = tr1(BADDR((2 * t + 2) % 5, i));
        else if (i < 8 + MT) fa0[i - 8] = ld128(sm + ((t + 1) % 3) * A_SLOT + aoff0 + (i - 8) * 2048);
      }
      if (t < 7) {
        if (i == 1) DMA_B0((2 * t + 6) * SSTEP, bdst0 + ((2 * t + 1) % 5) * B_SLOT);
        if (i == 1 + DS) DMA_B1((2 * t + 6) * SSTEP, bdst0 + ((2 * t + 1) % 5) * B_SLOT + 8192);
        if (i == 1 + 2 * DS) DMA_A0((t + 3) * 128, adst0 + (t % 3) * A_SLOT);
        if (i == 1 + 3 * DS) DMA_A1((t + 3) * 128, adst0 + (t % 3) * A_SLOT + 8192);
      }
      if (i == 4 && t == 8) {
        asm volatile("global_load_dwordx4 %0, %1, off sc1" : "=&v"(ackv) : "v"(ackp) : "memory");
#pragma unroll
        for (int m = 0; m < MT; ++m) asm volatile("global_load_dwordx4 %0, %1, off offset:%2 sc1" : "=&v"(stv[m]) : "v"(stp), "n"(m * 256) : "memory");
      }
      SCHED0();
    }
  }
  SCHED0();
  STAMPAT(3);

  if (fr == 15) {
#pragma unroll
    for (int j = 0; j < 4; ++j) *(LAS f32x4*)(sm + L_CS + w * 256 + j * 64 + g * 16) = acc[MT - 1][j];
  }
  if (MT == 6) asm volatile("s_waitcnt vmcnt(0) lgkmcnt(0)" : "+v"(ackv), "+v"(stv[0]), "+v"(stv[1]), "+v"(stv[2]), "+v"(stv[3]), "+v"(stv[4]), "+v"(stv[MT - 1]) :: "memory");
  else asm volatile("s_waitcnt vmcnt(0) lgkmcnt(0)" : "+v"(ackv), "+v"(stv[0]), "+v"(stv[1]), "+v"(stv[2]), "+v"(stv[3]), "+v"(stv[4]) :: "memory");
  f32x4 cs[4];
#pragma unroll
  for (int j = 0; j < 4; ++j) cs[j] = *(const LAS f32x4*)(sm + L_CS + w * 256 + j * 64 + g * 16);
  {
    const u32x2 nv = *(const LAS u32x2*)(sm + L_NONCE);
    for (unsigned spins = 0; spins < SPIN_MAX; ++spins) {
      bool ok = (ackv[0] == nv[0]) && (ackv[1] == nv[1]);
#pragma unroll
      for (int m = 0; m < MT; ++m) ok = ok && (stv[m][2] == ackv[2]) && (stv[m][3] == ackv[3]);
      if (all_ok(ok)) break;
      __builtin_amdgcn_s_sleep(4);
      ackv = ld128_sc1(ackp);
#pragma unroll
      for (int m = 0; m < MT; ++m) stv[m] = ld128_sc1(stp + m * 256);
    }
  }
  const u32x2 ep = u32x2{ackv[2], ackv[3]};
  if (w == 0 && lane == 0) st64_sc1(ws + WS_DONE + bid * 64, ep);
  if (GU) {
    const unsigned vB = p2_vB(w, lane);
    p2_issueB_down(srd_wd, vB, c0p2, w, smb, 0, rot);
    p2_issueB_down(srd_wd, vB, c0p2, w, smb, 1, rot);
    p2_issueB_down(srd_wd, vB, c0p2, w, smb, 2, rot);
  }
  float rstd[MT];
#pragma unroll
  for (int m = 0; m < MT; ++m) {
    const f32x2 nm = sp2(opq(-u2f(stv[m][0])));
    rstd[m] = opq(u2f(stv[m][1]));
#pragma unroll
    for (int j = 0; j < 4; ++j) {
      f32x2 l = fma2(nm, lo2(cs[j]), lo2(acc[m][j])), h = fma2(nm, hi2(cs[j]), hi2(acc[m][j]));
      acc[m][j] = f32x4{l[0], l[1], h[0], h[1]};
    }
  }
  STAMPAT(4);

  if (GU) {
    const float K0 = -2.0f * 0.7978845608028654f * 1.4426950408889634f;
    const float K1c = K0 * 0.044715f;
    ldsp stg = sm + w * 7680;
    char* hg = ws + WS_H + ct * 256 + wn * 64 + (lane & 3) * 16;
#pragma unroll
    for (int m = 0; m <= MT; ++m) {
      if (m < MT) {
        const float r = rstd[m], r2 = r * r;
        const f32x2 c0 = sp2(opq(K0 * r)), c1 = sp2(opq(K1c * r2 * r)), ir2 = sp2(opq(__builtin_amdgcn_rcpf(r2)));
        u32x2 hp[2];
#pragma unroll
        for (int j = 0; j < 2; ++j) {
#pragma unroll
          for (int q = 0; q < 2; ++q) {
            const f32x2 a = q ? hi2(acc[m][j]) : lo2(acc[m][j]), b = q ? hi2(acc[m][j + 2]) : lo2(acc[m][j + 2]);
            const f32x2 z = a * fma2(a * a, c1, c0);
            const f32x2 e = f32x2{__builtin_amdgcn_exp2f(z[0]), __builtin_amdgcn_exp2f(z[1])};
            const f32x2 d = fma2(e, ir2, ir2);
            const f32x2 hv = (a * b) * f32x2{__builtin_amdgcn_rcpf(d[0]), __builtin_amdgcn_rcpf(d[1])};
            hp[j][q] = pk2(hv[0], hv[1]);
          }
        }
        *(LAS u32x2*)(stg + (m * 16 + fr) * 80 + g * 8) = hp[0];
        *(LAS u32x2*)(stg + (m * 16 + fr) * 80 + 32 + g * 8) = hp[1];
      }
      if (m >= 1) {
        const int rl = (m - 1) * 16 + (lane >> 2);
        u32x4 v = *(const LAS u32x4*)(stg + rl * 80 + (lane & 3) * 16);
        const int sl = mh * 96 + rl;
        const int ri = sl < 95 ? sl : sl - 1;
        if (sl != 95 && ri < nrow) st128_sc1(hg + (size_t)(r0 + ri) * 3072, v);
      }
    }
    STAMPAT(5);
    WAITN_BAR(0);
    if (w == 0 && lane == 0) st64_sc1(ws + WS_HF + rg * 128 + ct * 8, ep);
    STAMPAT(6);
  } else {
    const int head = ct * 4 + wn;
    bf16x8 wq[2], wk[2];
#pragma unroll
    for (int ft = 0; ft < 2; ++ft) {
      wq[ft] = asb(u32x4{wpk[4 * ft], wpk[4 * ft + 1], g == 0 ? wpk[4 * ft + 2] : 0u, g == 0 ? wpk[4 * ft + 3] : 0u});
      wk[ft] = asb(u32x4{wpk[4 * ft], wpk[4 * ft + 1], g == 1 ? wpk[4 * ft + 2] : 0u, g == 1 ? wpk[4 * ft + 3] : 0u});
    }
    const unsigned tag = ep[0] ^ (ep[1] * 0x9E3779B1u);
    const float L2E = 1.4426950408889634f;
    const f32x4 z4 = f32x4{0.f, 0.f, 0.f, 0.f};
    bool live[6];
    u32x2 rem[6];
#pragma unroll
    for (int m = 0; m < 6; ++m) {
      int s = mh * 96 + m * 16 + fr;
      int ri = s < 95 ? s : s - 1;
      live[m] = (m < MT) && (s != 95) && (ri < nrow);
      if (m < MT) rem[m] = u32x2{pk2(acc[m][3][0], acc[m][3][1]), pk2(acc[m][3][2], acc[m][3][3])};
    }
    ldsp pk_img = sm + w * 17408;
    ldsp v_img = pk_img + 96 * 104;
    {
      unsigned kf[6][5];
      u32x2 vv[6], ve[6];
#pragma unroll
      for (int mp = 0; mp < 3; ++mp) {
        f32x2 y01[2], y23[2], y4;
#pragma unroll
        for (int hh = 0; hh < 2; ++hh) {
          const int m = 2 * mp + hh;
          if (m < MT) {
            const f32x2 r = sp2(rstd[m]);
            const float c = opq(rstd[m] * L2E);
            u32x4 kb = u32x4{pk2(acc[m][1][0], acc[m][1][1]), pk2(acc[m][1][2], acc[m][1][3]), rem[m][0], rem[m][1]};
            f32x4 k0 = mfma32(wk[0], asb(kb), z4);
            f32x4 k1 = mfma32(wk[1], asb(kb), z4);
            y01[hh] = lo2(k0) * sp2(c); y23[hh] = hi2(k0) * sp2(c); y4[hh] = k1[0] * c;
            vv[m] = u32x2{pk2v(lo2(acc[m][2]) * r), pk2v(hi2(acc[m][2]) * r)};
            u32x2 vr = u32x2{pk2v(lo2(acc[m][3]) * r), pk2v(hi2(acc[m][3]) * r)};
            ve[m] = (g == 2) ? vr : u32x2{0x3F803F80u, 0x3F803F80u};
          } else {
            y01[hh] = sp2(0.f); y23[hh] = sp2(0.f); y4[hh] = 0.f;
            vv[m] = u32x2{0u, 0u}; ve[m] = vv[m];
          }
        }
        featmap2(y01[0], y23[0], y01[1], y23[1], y4, live[2 * mp], live[2 * mp + 1], g, kf[2 * mp], kf[2 * mp + 1]);
      }
      SCHED0();
#pragma unroll
      for (int m = 0; m < 6; ++m) {
        int tok = m * 16 + fr;
        *(LAS u32x2*)(pk_img + tok * 104 + g * 8) = u32x2{kf[m][0], kf[m][1]};
        *(LAS u32x2*)(pk_img + tok * 104 + 32 + g * 8) = u32x2{kf[m][2], kf[m][3]};
        *(LAS u32x2*)(pk_img + tok * 104 + 64 + g * 8) = u32x2{kf[m][4], 0u};
        *(LAS u32x2*)(v_img + tok * 72 + g * 8) = vv[m];
        *(LAS u32x2*)(v_img + tok * 72 + 32 + (g ^ 2) * 8) = ve[m];
      }
      LGKM0();
      SCHED0();
    }
    f32x4 kv[3][2];
#pragma unroll
    for (int ft = 0; ft < 3; ++ft)
#pragma unroll
      for (int dt = 0; dt < 2; ++dt) kv[ft][dt] = z4;
    {
      const int q = fr >> 2, p = lane & 3;
#pragma unroll
      for (int s = 0; s < 3; ++s) {
        int row = 32 * s + 8 * g + q;
        bf16x8 a[3], b[2];
#pragma unroll
        for (int ft = 0; ft < 3; ++ft) a[ft] = tr2(pk_img + row * 104 + ft * 32 + p * 8, pk_img + (row + 4) * 104 + ft * 32 + p * 8);
#pragma unroll
        for (int dt = 0; dt < 2; ++dt) b[dt] = tr2(v_img + row * 72 + dt * 32 + p * 8, v_img + (row + 4) * 72 + dt * 32 + p * 8);
#pragma unroll
        for (int ft = 0; ft < 3; ++ft)
#pragma unroll
          for (int dt = 0; dt < 2; ++dt) kv[ft][dt] = mfma32(a[ft], b[dt], kv[ft][dt]);
      }
    }
    {
      f32x4 o5[5] = {kv[0][0], kv[1][0], kv[0][1], kv[1][1], f32x4{kv[2][0][0], kv[2][0][1], kv[2][1][0], kv[2][1][1]}};
      ldsp ex = sm + (4 + wn) * 17408;
      if (mh == 1) {
        LGKM0();
#pragma unroll
        for (int i = 0; i < 5; ++i) *(LAS f32x4*)(ex + i * 1024 + lane * 16) = o5[i];
      }
      LGKM_BAR();
      if (mh == 0) {
        unsigned pp[10];
#pragma unroll
        for (int i = 0; i < 5; ++i) {
          f32x4 e = *(const LAS f32x4*)(ex + i * 1024 + lane * 16);
          pp[2 * i] = pk2(o5[i][0] + e[0], o5[i][1] + e[1]);
          pp[2 * i + 1] = pk2(o5[i][2] + e[2], o5[i][3] + e[3]);
        }
        char* kp = ws + WS_KV + (size_t)(rg * NH + head) * 4096 + lane * 16;
        st128_sc1(kp, u32x4{pp[0], pp[1], pp[2], tag});
        st128_sc1(kp + 1024, u32x4{pp[3], pp[4], pp[5], tag});
        st128_sc1(kp + 2048, u32x4{pp[6], pp[7], pp[8], tag});
        st128_sc1(kp + 3072, u32x4{pp[9], 0u, 0u, tag});
      }
    }
    STAMPAT(5);
    unsigned qf[6][5];
#pragma unroll
    for (int mp = 0; mp < 3; ++mp) {
      f32x2 y01[2], y23[2], y4;
#pragma unroll
      for (int hh = 0; hh < 2; ++hh) {
        const int m = 2 * mp + hh;
        if (m < MT) {
          const float c = opq(rstd[m] * L2E);
          u32x4 qb = u32x4{pk2(acc[m][0][0], acc[m][0][1]), pk2(acc[m][0][2], acc[m][0][3]), rem[m][0], rem[m][1]};
          f32x4 q0 = mfma32(wq[0], asb(qb), z4);
          f32x4 q1 = mfma32(wq[1], asb(qb), z4);
          y01[hh] = lo2(q0) * sp2(c); y23[hh] = hi2(q0) * sp2(c); y4[hh] = q1[0] * c;
        } else {
          y01[hh] = sp2(0.f); y23[hh] = sp2(0.f); y4[hh] = 0.f;
        }
      }
      featmap2(y01[0], y23[0], y01[1], y23[1], y4, true, true, g, qf[2 * mp], qf[2 * mp + 1]);
    }
    STAMPAT(6);
    float ks[20];
    {
      {
        const char* p1 = ws + WS_KV + ((size_t)(mh * 6 + (lane < 6 ? lane : 0)) * NH + head) * 4096 + 3072 + 1008;
        for (unsigned spins = 0; spins < SPIN_MAX; ++spins) {
          u32x4 v = ld128_sc1(p1);
          if (all_ok(v[3] == tag)) break;
          __builtin_amdgcn_s_sleep(POLLS);
        }
      }
      u32x4 pv[6][4];
      const char* kp = ws + WS_KV + (size_t)((mh * 6) * NH + head) * 4096 + lane * 16;
      for (unsigned spins = 0; spins < SPIN_MAX; ++spins) {
#pragma unroll
        for (int c = 0; c < 6; ++c) {
          const char* p = kp + (size_t)c * NH * 4096;
          asm volatile("global_load_dwordx4 %0, %4, off sc1\n\tglobal_load_dwordx4 %1, %4, off offset:1024 sc1\n\t"
                       "global_load_dwordx4 %2, %4, off offset:2048 sc1\n\tglobal_load_dwordx4 %3, %4, off offset:3072 sc1"
                       : "=&v"(pv[c][0]), "=&v"(pv[c][1]), "=&v"(pv[c][2]), "=&v"(pv[c][3]) : "v"(p) : "memory");
        }
        asm volatile("s_waitcnt vmcnt(0)" : "+v"(pv[0][0]), "+v"(pv[0][1]), "+v"(pv[0][2]), "+v"(pv[0][3]), "+v"(pv[1][0]), "+v"(pv[1][1]), "+v"(pv[1][2]), "+v"(pv[1][3]),
                     "+v"(pv[2][0]), "+v"(pv[2][1]), "+v"(pv[2][2]), "+v"(pv[2][3]), "+v"(pv[3][0]), "+v"(pv[3][1]), "+v"(pv[3][2]), "+v"(pv[3][3]),
                     "+v"(pv[4][0]), "+v"(pv[4][1]), "+v"(pv[4][2]), "+v"(pv[4][3]), "+v"(pv[5][0]), "+v"(pv[5][1]), "+v"(pv[5][2]), "+v"(pv[5][3]) :: "memory");
        bool ok = true;
#pragma unroll
        for (int c = 0; c < 6; ++c)
#pragma unroll
          for (int i = 0; i < 4; ++i) ok = ok && (pv[c][i][3] == tag);
        if (all_ok(ok)) break;
        __builtin_amdgcn_s_sleep(2);
      }
      STAMPAT(7);
#pragma unroll
      for (int i = 0; i < 10; ++i) {
        float lo = 0.f, hi = 0.f;
#pragma unroll
        for (int c = 0; c < 6; ++c) {
          unsigned u = pv[c][i / 3][i % 3];
          lo += u2f(u << 16); hi += u2f(u & 0xffff0000u);
        }
        ks[2 * i] = lo; ks[2 * i + 1] = hi;
      }
      ldsp mine = sm + w * 17408 + 12288, other = sm + (w ^ 4) * 17408 + 12288;
      SCHED0();
#pragma unroll
      for (int i = 0; i < 5; ++i) *(LAS f32x4*)(mine + i * 1024 + lane * 16) = f32x4{ks[4 * i], ks[4 * i + 1], ks[4 * i + 2], ks[4 * i + 3]};
      LGKM_BAR();
#pragma unroll
      for (int i = 0; i < 5; ++i) {
        f32x4 e = *(const LAS f32x4*)(other + i * 1024 + lane * 16);
#pragma unroll
        for (int q = 0; q < 4; ++q) ks[4 * i + q] = (mh == 0) ? ks[4 * i + q] + e[q] : e[q] + ks[4 * i + q];
      }
    }
    const bf16x8 ka0 = asb(u32x4{pk2(ks[0], ks[1]), pk2(ks[2], ks[3]), pk2(ks[4], ks[5]), pk2(ks[6], ks[7])});
    const bf16x8 ka1 = asb(u32x4{pk2(ks[8], ks[9]), pk2(ks[10], ks[11]), pk2(ks[12], ks[13]), pk2(ks[14], ks[15])});
    const bf16x8 kb0 = asb(u32x4{pk2(ks[16], ks[17]), 0u, 0u, 0u});
    const bf16x8 kb1 = asb(u32x4{pk2(ks[18], ks[19]), 0u, 0u, 0u});
    {
      u32x2 om_[MT], or_[MT];
#pragma unroll
      for (int m = 0; m < MT; ++m) {
        bf16x8 b0 = asb(u32x4{qf[m][0], qf[m][1], qf[m][2], qf[m][3]});
        bf16x8 b1 = asb(u32x4{qf[m][4], 0u, 0u, 0u});
        f32x4 n0 = mfma32(kb0, b1, mfma32(ka0, b0, z4));
        f32x4 n1 = mfma32(kb1, b1, mfma32(ka1, b0, z4));
        float da = n1[0], db = n1[0];
        swap16(da, db);
        const f32x2 inv = sp2(__builtin_amdgcn_rcpf(db + 1e-6f));
        om_[m] = u32x2{pk2v(lo2(n0) * inv), pk2v(hi2(n0) * inv)};
        or_[m] = u32x2{pk2v(lo2(n1) * inv), pk2v(hi2(n1) * inv)};
      }
      SCHED0();
      ldsp stg = sm + w * 17408 + 8192;
#pragma unroll
      for (int m = 0; m < MT; ++m) {
        *(LAS u32x2*)(stg + (m * 16 + fr) * 32 + g * 8) = om_[m];
        if (g == 0) *(LAS u32x2*)(stg + 3072 + (m * 16 + fr) * 8) = or_[m];
      }
      LGKM0();
      const int tok0 = r0 + mh * 95;
      const int ntok = mh == 0 ? 95 : nrow - 95;
      char* om = ws + WS_OM + ((size_t)head * S_ + tok0) * 32;
      char* orr = ws + WS_OR + ((size_t)head * S_ + tok0) * 8;
      u32x4 tv[3];
      u32x2 tr[2];
#pragma unroll
      for (int i = 0; i < 3; ++i) tv[i] = *(const LAS u32x4*)(stg + i * 1024 + lane * 16);
#pragma unroll
      for (int i = 0; i < 2; ++i) tr[i] = *(const LAS u32x2*)(stg + 3072 + ((i * 64 + lane) < 96 ? (i * 64 + lane) : 0) * 8);
#pragma unroll
      for (int i = 0; i < 3; ++i)
        if (i * 32 + (lane >> 1) < ntok) st128_sc1(om + i * 1024 + lane * 16, tv[i]);
#pragma unroll
      for (int i = 0; i < 2; ++i)
        if (i * 64 + lane < ntok) st64_sc1(orr + (i * 64 + lane) * 8, tr[i]);
    }
    WAITN_BAR(0);
    if (w == 0 && lane == 0) st64_sc1(ws + WS_OF + rg * 128 + ct * 8, ep);
    STAMPAT(8);
  }
  return ep;
}

DI u32x2 service(ldsp sm, const char* x, char* ws, int sid, int w, int lane) {
  const int bid = __builtin_amdgcn_workgroup_id_x();
  const int g = lane >> 4, fr = lane & 15;
  STAMPAT(0);
  if (w == 0) {
    unsigned long long nn = __builtin_amdgcn_s_memrealtime();
    u32x2 nv = u32x2{(unsigned)nn, (unsigned)(nn >> 32)};
    if (lane == 0) {
      if (sid != 15) st64_sc1(ws + WS_REQ + bid * 64, nv);
      *(LAS u32x2*)(sm + L_NONCE) = nv;
    }
  }
  float mean[6], rstd[6];
  if (sid < 12) {
    const int r0 = rs_of(sid), nrow = rs_of(sid + 1) - r0;
    u32x4 v[6][5];
#pragma unroll
    for (int i = 0; i < 6; ++i) {
      int slot = (i * 8 + w) * 4 + g;
      int ri = slot < 95 ? slot : slot - 1;
      ri = ri < nrow ? ri : nrow - 1;
      const char* p = x + (size_t)(r0 + ri) * 1280 + fr * 16;
#pragma unroll
      for (int c = 0; c < 5; ++c) v[i][c] = *(const u32x4*)(p + c * 256);
    }
#pragma unroll
    for (int i = 0; i < 6; ++i) {
      float a = 0.f;
#pragma unroll
      for (int c = 0; c < 5; ++c)
#pragma unroll
        for (int e = 0; e < 4; ++e) a += u2f(v[i][c][e] << 16) + u2f(v[i][c][e] & 0xffff0000u);
      a += dpp<0xB1>(a); a += dpp<0x4E>(a); a += dpp<0x141>(a); a += dpp<0x140>(a);
      mean[i] = a * (1.0f / 640.0f);
      float b = 0.f;
#pragma unroll
      for (int c = 0; c < 5; ++c)
#pragma unroll
        for (int e = 0; e < 4; ++e) {
          float lo = u2f(v[i][c][e] << 16) - mean[i], hi = u2f(v[i][c][e] & 0xffff0000u) - mean[i];
          b = __builtin_fmaf(lo, lo, b);
          b = __builtin_fmaf(hi, hi, b);
        }
      b += dpp<0xB1>(b); b += dpp<0x4E>(b); b += dpp<0x141>(b); b += dpp<0x140>(b);
      rstd[i] = __builtin_amdgcn_rsqf(b * (1.0f / 640.0f) + 1e-5f);
    }
  }
  LGKM_BAR();
  const u32x2 nv = *(const LAS u32x2*)(sm + L_NONCE);
  u32x2 ep;
  if (sid == 15) {
    ep = u32x2{nv[0] ^ 0x5bd1e995u, nv[1] ^ 0x80000000u};
    if (w < 4) {
      const int cb = w * 64 + lane;
      const bool me = (cb == bid);
      for (unsigned spins = 0; spins < SPIN_MAX; ++spins) {
        u32x2 rq = ld64_sc1(ws + WS_REQ + cb * 64);
        st128_sc1(ws + WS_ACK + cb * 64, u32x4{rq[0], rq[1], ep[0], ep[1]});
        u32x2 dn = ld64_sc1(ws + WS_DONE + cb * 64);
        if (all_ok(me || (dn[0] == ep[0] && dn[1] == ep[1]))) break;
        __builtin_amdgcn_s_sleep(16);
      }
    }
  } else {
    u32x4 ack;
    for (unsigned spins = 0; spins < SPIN_MAX; ++spins) {
      ack = ld128_sc1(ws + WS_ACK + bid * 64);
      if (all_ok(ack[0] == nv[0] && ack[1] == nv[1])) break;
      __builtin_amdgcn_s_sleep(4);
    }
    ep = u32x2{ack[2], ack[3]};
    if (w == 0 && lane == 0) st64_sc1(ws + WS_DONE + bid * 64, ep);
    if (sid < 12) {
#pragma unroll
      for (int i = 0; i < 6; ++i) {
        int slot = (i * 8 + w) * 4 + g;
        if (fr == 0 && slot < 176) st128_sc1(ws + WS_STAT + (size_t)(sid * 176 + slot) * 16, u32x4{f2u(mean[i]), f2u(rstd[i]), ep[0], ep[1]});
      }
    }
  }
  STAMPAT(1);
  return ep;
}

__global__ __launch_bounds__(512, 2) void fused(const char* __restrict__ x, const char* __restrict__ wqkv,
                                                const char* __restrict__ wphi, const char* __restrict__ wo,
                                                const char* __restrict__ wg, const char* __restrict__ wu,
                                                const char* __restrict__ wd, char* __restrict__ ws, char* __restrict__ out) {
  extern __shared__ __attribute__((aligned(16))) char smem[];
  ldsp sm = (ldsp)smem;
  const int tid = __builtin_amdgcn_workitem_id_x();
  const int b = __builtin_amdgcn_workgroup_id_x(), xcd = b & 7, idx = b >> 3;
  const int w = __builtin_amdgcn_readfirstlane(tid >> 6), lane = tid & 63;
  const u32x4 srd_wd = make_srd(wd, DF_ * D_ * 2);
  const int rgl = idx / 5, c5 = idx % 5;
  const int rg = (xcd >> 2) * 6 + rgl, cq = xcd & 3;
  if (idx < 30 && c5 >= 3) {
    const int ct = cq * 2 + (c5 - 3);
    if (w < 4) phase1<6, false>(sm, x, wqkv, wphi, wg, wu, srd_wd, ws, rg, ct, 0, 0, w, lane);
    else phase1<5, false>(sm, x, wqkv, wphi, wg, wu, srd_wd, ws, rg, ct, 0, 0, w, lane);
    return;
  }
  const int p2i = idx < 30 ? rgl * 3 + c5 : 18 + (idx - 30);
  const int rt = (xcd >> 1) * 4 + p2i / 5, ct2 = (xcd & 1) * 5 + p2i % 5;
#define ROTF ((((p2i % 5) * 12) / 5 + (p2i / 5) * 3) % 12)
  const int rot = ROTF;
  u32x2 ep;
  if (idx < 30) {
    const int ct = cq * 3 + c5;
    if (w < 4) ep = phase1<6, true>(sm, x, wqkv, wphi, wg, wu, srd_wd, ws, rg, ct, ct2 * 64, rot, w, lane);
    else ep = phase1<5, true>(sm, x, wqkv, wphi, wg, wu, srd_wd, ws, rg, ct, ct2 * 64, rot, w, lane);
  } else {
    ep = service(sm, x, ws, b - 240, w, lane);
    {
      const u32x4 srd_wo = make_srd(wo, D_ * D_ * 2);
      const unsigned vp = (lane >> 3) * 1280 + (lane & 7) * 16 + (xcd & 1) * 640;
      const unsigned dst = (unsigned)(size_t)sm + w * 1024;
      const int w16 = (idx - 30) * 8 + w;
      if (!(b == 255 && w < 4)) {
#pragma unroll 1
        for (int p = w16; p < 192; p += 16) {
#pragma unroll
          for (int c = 0; c < 5; ++c) dma_b(srd_wd, vp, p * 8 * 1280 + c * 128, dst);
          asm volatile("s_waitcnt vmcnt(30)" ::: "memory");
        }
#pragma unroll 1
        for (int p = w16; p < 80; p += 16) {
#pragma unroll
          for (int c = 0; c < 5; ++c) dma_b(srd_wo, vp, p * 8 * 1280 + c * 128, dst);
          asm volatile("s_waitcnt vmcnt(30)" ::: "memory");
        }
      }
      WAITN_BAR(0);
    }
    const unsigned vB = p2_vB(w, lane);
    const unsigned smb = (unsigned)(size_t)sm;
    p2_issueB_down(srd_wd, vB, ct2 * 64, w, smb, 0, rot);
    p2_issueB_down(srd_wd, vB, ct2 * 64, w, smb, 1, rot);
    p2_issueB_down(srd_wd, vB, ct2 * 64, w, smb, 2, rot);
  }
  phase2(sm, x, wo, srd_wd, ws, out, rt, ct2, rot, w, lane, ep);
}

#if defined(__HIP_DEVICE_COMPILE__)

#endif

extern "C" void kernel_launch(void* const* d_in, const int* in_sizes, int n_in, void* d_out, int out_size, void* d_ws,
                              size_t ws_size, hipStream_t stream) {
  (void)hipFuncSetAttribute((const void*)fused, hipFuncAttributeMaxDynamicSharedMemorySize, K_LDS);
  fused<<<256, 512, K_LDS, stream>>>((const char*)d_in[0], (const char*)d_in[1], (const char*)d_in[2], (const char*)d_in[3],
                                     (const char*)d_in[5], (const char*)d_in[6], (const char*)d_in[7], (char*)d_ws, (char*)d_out);
}
```

```cpp
#define POLLS 6
#include <hip/hip_runtime.h>
#include <hip/hip_bf16.h>
#include <stdint.h>

typedef __attribute__((ext_vector_type(8))) __bf16 bf16x8;
typedef __attribute__((ext_vector_type(2))) __bf16 bf16x2;
typedef __attribute__((ext_vector_type(4))) short s16x4;
typedef __attribute__((ext_vector_type(8))) short s16x8;
typedef __attribute__((ext_vector_type(4))) float f32x4;
typedef __attribute__((ext_vector_type(2))) float f32x2;
typedef __attribute__((ext_vector_type(4))) unsigned u32x4;
typedef __attribute__((ext_vector_type(2))) unsigned u32x2;
#define LAS __attribute__((address_space(3)))
#define DI __device__ __forceinline__
typedef LAS char* ldsp;

constexpr int S_ = 2048, D_ = 640, DF_ = 1536, NH = 32;
constexpr int NRG = 12;
constexpr size_t WS_H = 0;
constexpr size_t WS_KV = WS_H + (size_t)S_ * DF_ * 2;
constexpr size_t WS_OM = WS_KV + (size_t)NRG * NH * 4096;
constexpr size_t WS_OR = WS_OM + (size_t)NH * S_ * 32;
constexpr size_t WS_REQ = WS_OR + (size_t)NH * S_ * 8;
constexpr size_t WS_ACK = WS_REQ + 256 * 64;
constexpr size_t WS_DONE = WS_ACK + 256 * 64;
constexpr size_t WS_STAT = WS_DONE + 256 * 64;
constexpr size_t WS_HF = WS_STAT + 12 * 176 * 16;
constexpr size_t WS_KF = WS_HF + 12 * 128;
constexpr size_t WS_OF = WS_KF + 8 * 128;
constexpr size_t WS_DBG = 64u << 20;

constexpr int A_SLOT = 22528, B_SLOT = 16384;
constexpr int L_A = 0;
constexpr int L_B = 3 * A_SLOT;
constexpr int L_MISC = L_B + 5 * B_SLOT;
constexpr int L_DUMMY = L_MISC;
constexpr int L_NONCE = L_MISC + 1024;
constexpr int L_CS = L_NONCE + 64;
constexpr int K_LDS = L_CS + 2048;
constexpr int P2_A = 0, P2_B = 98304;
constexpr unsigned SPIN_MAX = 1u << 20;
constexpr int CODE_KB = 102;

#if defined(__HIP_DEVICE_COMPILE__)

#endif

#define STAMPAT(i)

DI float u2f(unsigned u) { return __builtin_bit_cast(float, u); }
DI unsigned f2u(float f) { return __builtin_bit_cast(unsigned, f); }
DI u32x4 make_srd(const void* p, unsigned bytes) {
  uint64_t a = (uint64_t)p;
  u32x4 r;
  r[0] = (unsigned)a;
  r[1] = (unsigned)(a >> 32) & 0xffffu;
  r[2] = bytes;
  r[3] = 0x00020000u;
  return r;
}
DI void dma_b(u32x4 srd, unsigned voff, unsigned soff, unsigned m0v) {
  asm volatile("s_mov_b32 m0, %3\n\ts_nop 0\n\tbuffer_load_dwordx4 %0, %1, %2 offen lds" ::"v"(voff), "s"(srd), "s"(soff), "s"(m0v) : "memory");
}
DI void dma_b_mask(u32x4 srd, unsigned voff, unsigned soff, unsigned m0v, unsigned long long mask) {
  asm volatile("s_mov_b32 m0, %3\n\ts_mov_b64 exec, %4\n\tbuffer_load_dwordx4 %0, %1, %2 offen lds\n\ts_mov_b64 exec, -1" ::"v"(voff), "s"(srd), "s"(soff), "s"(m0v), "s"(mask) : "memory");
}
DI void dma_b_sc1(u32x4 srd, unsigned voff, unsigned soff, unsigned m0v) {
  asm volatile("s_mov_b32 m0, %3\n\ts_nop 0\n\tbuffer_load_dwordx4 %0, %1, %2 offen sc1 lds" ::"v"(voff), "s"(srd), "s"(soff), "s"(m0v) : "memory");
}
DI u32x4 ld128_sc1(const char* p) { u32x4 v; asm volatile("global_load_dwordx4 %0, %1, off sc1\n\ts_waitcnt vmcnt(0)" : "=&v"(v) : "v"(p) : "memory"); return v; }
DI u32x2 ld64_sc1(const char* p) { u32x2 v; asm volatile("global_load_dwordx2 %0, %1, off sc1\n\ts_waitcnt vmcnt(0)" : "=&v"(v) : "v"(p) : "memory"); return v; }
DI void st128_sc1(char* p, u32x4 v) { asm volatile("global_store_dwordx4 %0, %1, off sc1\n\ts_nop 1" ::"v"(p), "v"(v) : "memory"); }
DI void st64_sc1(char* p, u32x2 v) { asm volatile("global_store_dwordx2 %0, %1, off sc1" ::"v"(p), "v"(v) : "memory"); }
#define WAITN_BAR(N) asm volatile("s_waitcnt vmcnt(%0) lgkmcnt(0)\n\ts_barrier" ::"n"(N) : "memory")
#define LGKM_BAR() asm volatile("s_waitcnt lgkmcnt(0)\n\ts_barrier" ::: "memory")
#define LGKM0() asm volatile("s_waitcnt lgkmcnt(0)" ::: "memory")
#define MEMFENCE() asm volatile("" ::: "memory")
#define SCHED0() __builtin_amdgcn_sched_barrier(0)

DI bf16x8 ld128(ldsp p) { return *(const LAS bf16x8*)p; }
DI s16x4 tr1(ldsp p) { return __builtin_amdgcn_ds_read_tr16_b64_v4i16((LAS s16x4*)p); }
DI bf16x8 cat(s16x4 a, s16x4 b) { return __builtin_bit_cast(bf16x8, __builtin_shufflevector(a, b, 0, 1, 2, 3, 4, 5, 6, 7)); }
DI bf16x8 tr2(ldsp p0, ldsp p1) { return cat(tr1(p0), tr1(p1)); }
DI f32x4 mfma32(bf16x8 a, bf16x8 b, f32x4 c) { return __builtin_amdgcn_mfma_f32_16x16x32_bf16(a, b, c, 0, 0, 0); }
DI bf16x8 asb(u32x4 v) { return __builtin_bit_cast(bf16x8, v); }
DI unsigned pk2(float a, float b) {
  bf16x2 v;
  v[0] = (__bf16)a;
  v[1] = (__bf16)b;
  return __builtin_bit_cast(unsigned, v);
}
DI void swap16(float& a, float& b) {
  auto r = __builtin_amdgcn_permlane16_swap(f2u(a), f2u(b), false, false);
  a = u2f(r[0]); b = u2f(r[1]);
}
DI void swap32(float& a, float& b) {
  auto r = __builtin_amdgcn_permlane32_swap(f2u(a), f2u(b), false, false);
  a = u2f(r[0]); b = u2f(r[1]);
}
template <int CTRL> DI float dpp(float x) { return u2f(__builtin_amdgcn_update_dpp(0u, f2u(x), CTRL, 0xf, 0xf, false)); }
DI float fmx(float a, float b) { return __builtin_fmaxf(a, b); }
DI float fmn(float a, float b) { return __builtin_fminf(a, b); }
DI float rawmax(float a, float b) { float r; asm("v_max_f32 %0, %1, %2" : "=v"(r) : "v"(a), "v"(b)); return r; }
DI float opq(float x) { asm("" : "+v"(x)); return x; }
DI f32x2 lo2(f32x4 v) { return __builtin_shufflevector(v, v, 0, 1); }
DI f32x2 hi2(f32x4 v) { return __builtin_shufflevector(v, v, 2, 3); }
DI f32x2 sp2(float x) { return f32x2{x, x}; }
DI f32x2 fma2(f32x2 a, f32x2 b, f32x2 c) { return __builtin_elementwise_fma(a, b, c); }
DI int rs_of(int rg) { return (512 * rg) / 3; }
DI int rg_of(int row) { return (3 * row + 2) >> 9; }
DI bool all_ok(bool ok) { return __builtin_amdgcn_ballot_w64(ok) == ~0ull; }

DI void wait_flags(const char* p, bool need, u32x2 ep) {
  for (unsigned spins = 0; spins < SPIN_MAX; ++spins) {
    u32x2 v = ld64_sc1(p);
    if (all_ok(!need || (v[0] == ep[0] && v[1] == ep[1]))) break;
    __builtin_amdgcn_s_sleep(POLLS);
  }
}

DI f32x2 ex2(f32x2 v) { return f32x2{__builtin_amdgcn_exp2f(v[0]), __builtin_amdgcn_exp2f(v[1])}; }
DI unsigned pk2v(f32x2 v) { return pk2(v[0], v[1]); }
DI void featmap2(f32x2 a01, f32x2 a23, f32x2 b01, f32x2 b23, f32x2 e4, bool livea, bool liveb, int g, unsigned (&oa)[5], unsigned (&ob)[5]) {
  float a0 = fmx(fmx(fmx(a01[0], a01[1]), a23[0]), fmx(a23[1], e4[0]));
  float a1 = -fmn(fmn(fmn(a01[0], a01[1]), a23[0]), fmn(a23[1], e4[0]));
  float b0 = fmx(fmx(fmx(b01[0], b01[1]), b23[0]), fmx(b23[1], e4[1]));
  float b1 = -fmn(fmn(fmn(b01[0], b01[1]), b23[0]), fmn(b23[1], e4[1]));
  swap16(a0, a1); swap16(b0, b1);
  float ca = rawmax(a0, a1), cb = rawmax(b0, b1);
  swap32(ca, cb);
  float d0 = rawmax(ca, cb), d1 = d0;
  swap32(d0, d1);
  float Ma = d0, Na = d0, Mb = d1, Nb = d1;
  swap16(Ma, Na); swap16(Mb, Nb);
  const f32x2 pa01 = ex2(a01 - sp2(Ma)), pa23 = ex2(a23 - sp2(Ma)), na01 = ex2(-a01 - sp2(Na)), na23 = ex2(-a23 - sp2(Na));
  const f32x2 pb01 = ex2(b01 - sp2(Mb)), pb23 = ex2(b23 - sp2(Mb)), nb01 = ex2(-b01 - sp2(Nb)), nb23 = ex2(-b23 - sp2(Nb));
  const float pa4 = __builtin_amdgcn_exp2f(e4[0] - Ma), na4 = __builtin_amdgcn_exp2f(-e4[0] - Na);
  const float pb4 = __builtin_amdgcn_exp2f(e4[1] - Mb), nb4 = __builtin_amdgcn_exp2f(-e4[1] - Nb);
  f32x2 t;
  t = pa01 + pa23; a0 = (t[0] + t[1]) + pa4;
  t = na01 + na23; a1 = (t[0] + t[1]) + na4;
  t = pb01 + pb23; b0 = (t[0] + t[1]) + pb4;
  t = nb01 + nb23; b1 = (t[0] + t[1]) + nb4;
  swap16(a0, a1); swap16(b0, b1);
  ca = a0 + a1; cb = b0 + b1;
  swap32(ca, cb);
  d0 = __builtin_amdgcn_rcpf(ca + cb);
  if (!(g < 2 ? livea : liveb)) d0 = 0.f;
  d1 = d0;
  swap32(d0, d1);
  Ma = d0; Na = d0; Mb = d1; Nb = d1;
  swap16(Ma, Na); swap16(Mb, Nb);
  oa[0] = pk2v(pa01 * sp2(Ma)); oa[1] = pk2v(pa23 * sp2(Ma));
  oa[2] = pk2v(na01 * sp2(Na)); oa[3] = pk2v(na23 * sp2(Na));
  oa[4] = pk2(pa4 * Ma, na4 * Na);
  ob[0] = pk2v(pb01 * sp2(Mb)); ob[1] = pk2v(pb23 * sp2(Mb));
  ob[2] = pk2v(nb01 * sp2(Nb)); ob[3] = pk2v(nb23 * sp2(Nb));
  ob[4] = pk2(pb4 * Mb, nb4 * Nb);
}

struct P2Ctx { unsigned vB, vR; int c0; };
DI unsigned p2_vB(int w, int lane) {
  const int l3 = lane >> 3, l7 = lane & 7;
  int fb = ((l3 >> 1) & 1) | ((w & 1) << 1);
  int ch = (((l7 >> 1) ^ fb) << 1) | (l7 & 1);
  return l3 * 1280 + ch * 16;
}
DI int p2_rot(int s, int rot) { int r = s + rot; return r >= 12 ? r - 12 : r; }
DI void p2_issueB_down(u32x4 srd_wd, unsigned vB, int c0, int w, unsigned smb, int s, int rot) {
  const int sr = p2_rot(s, rot);
  dma_b(srd_wd, vB, (128 * sr + 8 * w) * 1280 + c0 * 2, smb + P2_B + (s % 3) * 16384 + w * 1024);
  dma_b(srd_wd, vB, (128 * sr + 8 * w + 64) * 1280 + c0 * 2, smb + P2_B + (s % 3) * 16384 + w * 1024 + 8192);
}

DI void phase2(ldsp sm, const char* x, const char* wo, const u32x4 srd_wd, char* ws, char* out, int rt, int ct, int rot, int w, int lane, u32x2 ep) {
  const int r0 = rt * 128, c0 = ct * 64;
  const int g = lane >> 4, fr = lane & 15, l3 = lane >> 3, l7 = lane & 7;
  const int kh = w & 1, nh = (w >> 1) & 1, mh = w >> 2;
  const unsigned smb = (unsigned)(size_t)sm;
  const u32x4 srd_h = make_srd(ws + WS_H, S_ * DF_ * 2);
  const u32x4 srd_wo = make_srd(wo, D_ * D_ * 2);
  const u32x4 srd_om = make_srd(ws + WS_OM, NH * S_ * 32);
  const u32x4 srd_or = make_srd(ws + WS_OR, NH * S_ * 8);
  const unsigned vB = p2_vB(w, lane);
  const unsigned vA = l3 * 3072 + ((l7 ^ ((4 * (w & 1) + (l3 >> 1)) & 7)) * 16);
  const unsigned vR = vB - l3 * 1280 + (20 * (lane >> 5) + 16 + (l3 & 3)) * 1280;
  const unsigned vL = lane * 16;
  const int rgl = rg_of(r0), rgh = rg_of(r0 + 127);

#define P2_PIECE(s, p) do { \
    constexpr int i_ = (p) < 2 ? (p) : (p) - 2; \
    if ((s) < 12) { \
      if ((p) < 2) dma_b(srd_wd, vB, (128 * p2_rot((s), rot) + 8 * w + 64 * i_) * 1280 + c0 * 2, smb + P2_B + ((s) % 3) * 16384 + i_ * 8192 + w * 1024); \
      else dma_b_sc1(srd_h, vA, (r0 + 8 * w + 64 * (i_ & 1)) * 3072 + p2_rot((s), rot) * 256 + (i_ >> 1) * 128, smb + P2_A + ((s) % 3) * 32768 + (i_ >> 1) * 16384 + (i_ & 1) * 8192 + w * 1024); \
    } else if ((s) < 16) { \
      if ((p) < 2) dma_b(srd_wo, vB, (160 * ((s) - 12) + 20 * ((w + 8 * i_) >> 1) + 8 * (w & 1)) * 1280 + c0 * 2, smb + P2_B + ((s) % 3) * 16384 + i_ * 8192 + w * 1024); \
      else dma_b_sc1(srd_om, vL, ((8 * ((s) - 12) + 2 * i_ + (w >> 2)) * S_ + r0) * 32 + (w & 3) * 1024, smb + P2_A + ((s) % 3) * 32768 + i_ * 8192 + w * 1024); \
    } else { \
      if ((p) < 2) dma_b(srd_wo, vR, (40 * (w + 8 * i_)) * 1280 + c0 * 2, smb + P2_B + ((s) % 3) * 16384 + i_ * 8192 + w * 1024); \
      else dma_b_sc1(srd_or, vL, ((8 * i_ + w) * S_ + r0) * 8, smb + P2_A + ((s) % 3) * 32768 + i_ * 8192 + w * 1024); \
    } } while (0)

  const int aD = P2_A + kh * 16384 + (64 * mh + fr) * 128 + ((g ^ ((fr >> 1) & 7)) * 16);
  const int aM = P2_A + (4 * kh + (g >> 1)) * 4096 + (64 * mh + fr) * 32 + (g & 1) * 16;
  const int aR = P2_A + (16 * kh + 2 * g) * 1024 + (64 * mh + fr) * 8;
  int bO[2];
  {
    const int q = fr >> 2, p = lane & 3;
    const int f = ((q >> 1) & 1) | ((g & 1) << 1);
#pragma unroll
    for (int j = 0; j < 2; ++j) bO[j] = P2_B + (64 * kh + 8 * g + q) * 128 + (((2 * nh + j) ^ f) * 32) + p * 8;
  }
  f32x4 acc[4][2];
#pragma unroll
  for (int m = 0; m < 4; ++m)
#pragma unroll
    for (int j = 0; j < 2; ++j) acc[m][j] = f32x4{0.f, 0.f, 0.f, 0.f};

  STAMPAT(8);
  if (w == 0) wait_flags(ws + WS_HF + (size_t)(lane < 16 ? rgl : rgh) * 128 + (lane & 15) * 8, lane < 32 && (lane & 15) < 12, ep);
  WAITN_BAR(0);
  STAMPAT(9);
#define P2_A3(s) do { P2_PIECE(s, 2); P2_PIECE(s, 3); P2_PIECE(s, 4); P2_PIECE(s, 5); } while (0)
  P2_A3(0); P2_A3(1); P2_A3(2);
  u32x4 xr[2];
  bf16x8 fa[2][8];
  s16x4 fb[2][8];
#define P2_RD(s, i) do { \
    if ((i) < 8) fb[(s) & 1][i] = tr1(sm + ((s) % 3) * 16384 + bO[((i) >> 1) & 1] + ((i) >> 2) * 4096 + ((i) & 1) * 512); \
    else if ((i) < 16) { \
      constexpr int sl_ = ((i) - 8) >> 2, m_ = ((i) - 8) & 3; \
      if ((s) < 12) fa[(s) & 1][((i) - 8) & 7] = ld128(sm + ((s) % 3) * 32768 + (sl_ ? (aD ^ 64) : aD) + m_ * 2048); \
      else if ((s) < 16) fa[(s) & 1][((i) - 8) & 7] = ld128(sm + ((s) % 3) * 32768 + aM + sl_ * 8192 + m_ * 512); \
      else fa[(s) & 1][((i) - 8) & 7] = cat(*(const LAS s16x4*)(sm + ((s) % 3) * 32768 + aR + sl_ * 8192 + m_ * 128), *(const LAS s16x4*)(sm + ((s) % 3) * 32768 + aR + sl_ * 8192 + 1024 + m_ * 128)); } } while (0)
#define P2_MM(s, i) acc[((i) >> 1) & 3][(i) & 1] = mfma32(cat(fb[(s) & 1][((i) >> 3) * 4 + 2 * ((i) & 1)], fb[(s) & 1][((i) >> 3) * 4 + 2 * ((i) & 1) + 1]), fa[(s) & 1][((i) >> 3) * 4 + (((i) >> 1) & 3)], acc[((i) >> 1) & 3][(i) & 1])
#define P2_GROUP(s, q) do { \
    if ((s) >= 1 && (s) + 2 < 17) { \
      if ((s) + 2 == 12 && (q) == 2) { \
        STAMPAT(10); \
        if (w == 0) wait_flags(ws + WS_OF + (size_t)(lane < 16 ? rgl : rgh) * 128 + (lane & 15) * 8, lane < 32 && (lane & 15) < 8, ep); \
        asm volatile("s_barrier" ::: "memory"); \
        STAMPAT(11); \
      } \
      P2_PIECE((s) + 2, q); } \
    SCHED0(); \
    if ((s) < 17) { P2_RD(s, 3 * (q)); P2_RD(s, 3 * (q) + 1); P2_RD(s, 3 * (q) + 2); } \
    SCHED0(); \
    if ((s) >= 1) { P2_MM((s) - 1, ((q) * 8) / 3); P2_MM((s) - 1, ((q) * 8) / 3 + 1); if (((q) * 8) / 3 + 2 < (((q) + 1) * 8) / 3) P2_MM((s) - 1, ((q) * 8) / 3 + 2); } \
    SCHED0(); } while (0)
#define P2_STAGE(s) do { \
    SCHED0(); \
    if ((s) < 17) WAITN_BAR((s) == 0 ? 8 : ((s) == 1 ? 4 : ((s) < 16 ? 6 : 0))); \
    SCHED0(); \
    P2_GROUP(s, 0); P2_GROUP(s, 1); P2_GROUP(s, 2); P2_GROUP(s, 3); P2_GROUP(s, 4); P2_GROUP(s, 5); } while (0)
  P2_STAGE(0); P2_STAGE(1); P2_STAGE(2); P2_STAGE(3); P2_STAGE(4); P2_STAGE(5); P2_STAGE(6); P2_STAGE(7); P2_STAGE(8);
  P2_STAGE(9); P2_STAGE(10); P2_STAGE(11); P2_STAGE(12); P2_STAGE(13); P2_STAGE(14); P2_STAGE(15);
  const int orow = 64 * mh + 32 * kh + (lane >> 2), ocol = c0 * 2 + nh * 64 + (lane & 3) * 16;
#pragma unroll
  for (int i = 0; i < 2; ++i)
    asm volatile("global_load_dwordx4 %0, %1, off" : "=&v"(xr[i]) : "v"(x + (size_t)(r0 + orow + 16 * i) * 1280 + ocol) : "memory");
  P2_STAGE(16); P2_STAGE(17);
  SCHED0();
  STAMPAT(12);
  f32x4 keep[2][2];
  {
    ldsp ex = sm + w * 4096 + lane * 16;
#pragma unroll
    for (int i = 0; i < 2; ++i)
#pragma unroll
      for (int j = 0; j < 2; ++j) {
        f32x4 snd;
#pragma unroll
        for (int e = 0; e < 4; ++e) {
          const float lo_ = acc[i][j][e], hi_ = acc[2 + i][j][e];
          snd[e] = kh ? lo_ : hi_;
          keep[i][j][e] = kh ? hi_ : lo_;
        }
        *(LAS f32x4*)(ex + (i * 2 + j) * 1024) = snd;
      }
  }
  LGKM_BAR();
  {
    ldsp ex = sm + (w ^ 1) * 4096 + lane * 16;
    ldsp tp = sm + 65536 + w * 4608;
#pragma unroll
    for (int i = 0; i < 2; ++i)
#pragma unroll
      for (int j = 0; j < 2; ++j) {
        const f32x4 e = *(const LAS f32x4*)(ex + (i * 2 + j) * 1024);
        *(LAS f32x4*)(tp + (i * 16 + fr) * 144 + j * 64 + g * 16) = keep[i][j] + e;
      }
    asm volatile("" : "+v"(xr[0]), "+v"(xr[1]));
#pragma unroll
    for (int i = 0; i < 2; ++i) {
      const f32x4 s0 = *(const LAS f32x4*)(tp + (i * 16 + (lane >> 2)) * 144 + (lane & 3) * 32);
      const f32x4 s1 = *(const LAS f32x4*)(tp + (i * 16 + (lane >> 2)) * 144 + (lane & 3) * 32 + 16);
      u32x4 o;
#pragma unroll
      for (int e = 0; e < 2; ++e) {
        o[e] = pk2(s0[2 * e] + u2f(xr[i][e] << 16), s0[2 * e + 1] + u2f(xr[i][e] & 0xffff0000u));
        o[2 + e] = pk2(s1[2 * e] + u2f(xr[i][2 + e] << 16), s1[2 * e + 1] + u2f(xr[i][2 + e] & 0xffff0000u));
      }
      *(u32x4*)(out + (size_t)(r0 + orow + 16 * i) * 1280 + ocol) = o;
    }
  }
}

template <int MT, bool GU>
DI u32x2 phase1(ldsp sm, const char* x, const char* wqkv, const char* wphi, const char* wg, const char* wu, const u32x4 srd_wd,
                char* ws, int rg, int ct, int c0p2, int rot, int w, int lane) {
  STAMPAT(0);
  constexpr int mh = (MT == 6) ? 0 : 1;
  const int wn = w & 3;
  const int r0 = rs_of(rg), nrow = rs_of(rg + 1) - r0;
  const unsigned smb = (unsigned)(size_t)sm;
  const int g = lane >> 4, fr = lane & 15;

  const u32x4 srdx = make_srd(x, S_ * D_ * 2);
  unsigned voffA[3];
#pragma unroll
  for (int c = 0; c < 3; ++c) {
    int slot = 8 * (w + 8 * c) + (lane >> 3);
    int ch = (lane & 7) ^ ((slot >> 1) & 7);
    int ri = slot < 95 ? slot : slot - 1;
    ri = ri < nrow ? ri : nrow - 1;
    voffA[c] = (unsigned)((r0 + ri) * 1280 + ch * 16);
  }
  if (w >= 6) voffA[2] = 0x40000000u;
  const unsigned long long FULL = ~0ull, PART = 0x00FFFFFFFFFFFFFFull;
  const unsigned long long mask1 = (w == 3) ? PART : FULL, mask2 = (w == 5) ? PART : FULL;
  const unsigned adst0 = smb + L_A + w * 1024;
  u32x4 srd0, srd1;
  unsigned voff0, voff1;
  constexpr unsigned SSTEP = GU ? 32 * 3072 : 32 * 3840;
  if (GU) {
    srd0 = make_srd(wg, D_ * DF_ * 2);
    srd1 = make_srd(wu, D_ * DF_ * 2);
    int krow = 4 * w + (lane >> 4);
    int chp = lane & 15;
    int f = (krow & 3) | (((krow >> 3) & 1) << 2);
    int ch = (((chp >> 1) ^ f) << 1) | (chp & 1);
    voff0 = voff1 = krow * 3072 + ct * 256 + ch * 16;
  } else {
    srd0 = srd1 = make_srd(wqkv, D_ * 3 * D_ * 2);
    unsigned vo[2];
#pragma unroll
    for (int c = 0; c < 2; ++c) {
      int cidx = (w + 8 * c) * 64 + lane;
      int rho = cidx / 30, cc = cidx % 30;
      int seg = cc / 10, ch = cc % 10;
      int k = (rho & 16) | ((rho & 4) << 1) | ((rho & 8) >> 1) | (rho & 3);
      vo[c] = (cidx < 960) ? (unsigned)(k * 3840 + seg * 1280 + ct * 160 + ch * 16) : 0x40000000u;
    }
    voff0 = vo[0];
    voff1 = vo[1];
  }
  const unsigned bdst0 = smb + L_B + w * 1024;
#define KROT (((rg%6)*5/3+ct*2)%10)
  const unsigned krot = KROT;
#define RA(so) ((so) + krot * 128u >= 1280u ? (so) + krot * 128u - 1280u : (so) + krot * 128u)
#define RB(so) ((so) + krot * 2u * SSTEP >= 20u * SSTEP ? (so) + krot * 2u * SSTEP - 20u * SSTEP : (so) + krot * 2u * SSTEP)
#define DMA_B0(soff, dst) dma_b(srd0, voff0, RB(soff), (dst))
#define DMA_B1(soff, dst) dma_b(srd1, voff1, RB(soff), (dst))
#define DMA_A0(soff, dst) dma_b(srdx, voffA[0], RA(soff), (dst))
#define DMA_A1(soff, dst) dma_b_mask(srdx, voffA[1], RA(soff), (dst), mask1)
#define DMA_A2(soff, dst) dma_b_mask(srdx, voffA[2], RA(soff), (dst), mask2)
  const unsigned a2base = (w >= 6) ? smb + L_DUMMY : adst0 + 16384;
  const unsigned a2mul = (w >= 6) ? 0u : 1u;
  unsigned wraw[16];
  if (!GU) {
    const char* wp = wphi + (ct * 4 + wn) * 800;
    const char* p0 = wp + (5 * (fr >> 2) + (fr & 3)) * 2;
    const char* p1 = wp + (5 * (fr >> 2) + 4) * 2;
    const char* q0 = p0 + g * 160;
    const char* q1 = p1 + g * 160;
#pragma unroll
    for (int j = 0; j < 4; ++j) {
      asm volatile("global_load_ushort %0, %1, off offset:%2" : "=&v"(wraw[j]) : "v"(q0), "n"(j * 40) : "memory");
      asm volatile("global_load_ushort %0, %1, off offset:%2" : "=&v"(wraw[4 + j]) : "v"(p0), "n"(640 + j * 40) : "memory");
      asm volatile("global_load_ushort %0, %1, off offset:%2" : "=&v"(wraw[8 + j]) : "v"(q1), "n"(j * 40) : "memory");
      asm volatile("global_load_ushort %0, %1, off offset:%2" : "=&v"(wraw[12 + j]) : "v"(p1), "n"(640 + j * 40) : "memory");
    }
  }
  DMA_B0(0, bdst0); DMA_B1(0, bdst0 + 8192);
  DMA_A0(0, adst0); DMA_A1(0, adst0 + 8192);
  DMA_A2(0, a2base);
  DMA_B0(SSTEP, bdst0 + B_SLOT); DMA_B1(SSTEP, bdst0 + B_SLOT + 8192);
  DMA_B0(2 * SSTEP, bdst0 + 2 * B_SLOT); DMA_B1(2 * SSTEP, bdst0 + 2 * B_SLOT + 8192);
  DMA_A0(128, adst0 + A_SLOT); DMA_A1(128, adst0 + A_SLOT + 8192);
  DMA_A2(128, a2base + a2mul * A_SLOT);
  DMA_B0(3 * SSTEP, bdst0 + 3 * B_SLOT); DMA_B1(3 * SSTEP, bdst0 + 3 * B_SLOT + 8192);
  DMA_B0(4 * SSTEP, bdst0 + 4 * B_SLOT); DMA_B1(4 * SSTEP, bdst0 + 4 * B_SLOT + 8192);
  DMA_A0(256, adst0 + 2 * A_SLOT); DMA_A1(256, adst0 + 2 * A_SLOT + 8192);
  if (w == 0 && lane < 48) {
    int rr = lane >> 3;
    *(LAS u32x4*)(sm + L_A + (rr >> 1) * A_SLOT + ((rr & 1) ? 175 : 95) * 128 + (lane & 7) * 16) =
        u32x4{0x3F803F80u, 0x3F803F80u, 0x3F803F80u, 0x3F803F80u};
  }
  const int bid = __builtin_amdgcn_workgroup_id_x();
  if (w == 1) {
    unsigned long long nn = __builtin_amdgcn_s_memrealtime();
    u32x2 nv = u32x2{(unsigned)nn, (unsigned)(nn >> 32)};
    if (lane == 0) {
      st64_sc1(ws + WS_REQ + bid * 64, nv);
      *(LAS u32x2*)(sm + L_NONCE) = nv;
    }
  }

  const int aoff0 = L_A + mh * 6 * 2048 + fr * 128 + ((g ^ ((fr >> 1) & 7)) * 16);
  const int aoff1 = aoff0 ^ 64;
  int boff[4];
  constexpr int TSTEP = GU ? 1024 : 3840;
  {
    const int q = fr >> 2, p = lane & 3;
    if (GU) {
      int f = q | ((g & 1) << 2);
#pragma unroll
      for (int j = 0; j < 4; ++j)
        boff[j] = L_B + (j >> 1) * 8192 + (8 * g + q) * 256 + (((2 * wn + (j & 1)) ^ f) * 32) + p * 8;
    } else {
      int rho = 16 * (g >> 1) + 4 * (g & 1) + q;
#pragma unroll
      for (int j = 0; j < 3; ++j) boff[j] = L_B + rho * 480 + (20 * j + 5 * wn + p) * 8;
      boff[3] = L_B + rho * 480 + (20 * (p == 3 ? 0 : p) + 5 * wn + 4) * 8;
    }
  }

  f32x4 acc[MT][4];
#pragma unroll
  for (int m = 0; m < MT; ++m)
#pragma unroll
    for (int j = 0; j < 4; ++j) acc[m][j] = f32x4{0.f, 0.f, 0.f, 0.f};
  bf16x8 fa0[MT], fa1[MT];
  s16x4 fb0[8], fb1[8];

  STAMPAT(1);
  WAITN_BAR(13);
  STAMPAT(2);
  unsigned wpk[8];
  if (!GU) {
    asm volatile("" : "+v"(wraw[0]), "+v"(wraw[1]), "+v"(wraw[2]), "+v"(wraw[3]), "+v"(wraw[4]), "+v"(wraw[5]), "+v"(wraw[6]), "+v"(wraw[7]),
                 "+v"(wraw[8]), "+v"(wraw[9]), "+v"(wraw[10]), "+v"(wraw[11]), "+v"(wraw[12]), "+v"(wraw[13]), "+v"(wraw[14]), "+v"(wraw[15]));
#pragma unroll
    for (int i = 0; i < 8; ++i) {
      wpk[i] = wraw[2 * i] | (wraw[2 * i + 1] << 16);
      if (i >= 4 && (fr & 3) != 0) wpk[i] = 0u;
    }
    asm volatile("" : "+v"(wpk[0]), "+v"(wpk[1]), "+v"(wpk[2]), "+v"(wpk[3]), "+v"(wpk[4]), "+v"(wpk[5]), "+v"(wpk[6]), "+v"(wpk[7]));
  }
#pragma unroll
  for (int m = 0; m < MT; ++m) fa0[m] = ld128(sm + aoff0 + m * 2048);
#pragma unroll
  for (int j = 0; j < 8; ++j) fb0[j] = tr1(sm + boff[j >> 1] + (j & 1) * TSTEP);
  MEMFENCE();

  constexpr int NM = MT * 4;
  constexpr int DS = (MT == 6) ? 6 : 5;
#define MMA(i, FA, FB) acc[(i) >> 2][(i) & 3] = mfma32(cat(FB[2 * ((i) & 3)], FB[2 * ((i) & 3) + 1]), FA[(i) >> 2], acc[(i) >> 2][(i) & 3])
#define BADDR(sl, i) (sm + (sl) * B_SLOT + boff[(i) >> 1] + ((i) & 1) * TSTEP)
  u32x4 ackv, stv[MT];
  const char* ackp = ws + WS_ACK + bid * 64;
  const char* stp = ws + WS_STAT + (size_t)(rg * 176 + mh * 96 + fr) * 16;
#pragma unroll
  for (int t = 0; t < 10; ++t) {
    SCHED0();
    WAITN_BAR(t < 8 ? 11 : (t == 8 ? 7 : 1 + MT));
    SCHED0();
#pragma unroll
    for (int i = 0; i < NM; ++i) {
      MMA(i, fa0, fb0);
      SCHED0();
      if (i < 8) fb1[i] = tr1(BADDR((2 * t + 1) % 5, i));
      else if (i < 8 + MT) fa1[i - 8] = ld128(sm + (t % 3) * A_SLOT + aoff1 + (i - 8) * 2048);
      if (t < 8) {
        if (i == 2) { if (MT == 6) DMA_A2((t + 2) * 128, adst0 + 16384 + ((t + 2) % 3) * A_SLOT); else DMA_A2((t + 2) * 128, a2base + a2mul * (((t + 2) % 3) * A_SLOT)); }
        if (i == 2 + DS) DMA_B0((2 * t + 5) * SSTEP, bdst0 + ((2 * t) % 5) * B_SLOT);
        if (i == 2 + 2 * DS) DMA_B1((2 * t + 5) * SSTEP, bdst0 + ((2 * t) % 5) * B_SLOT + 8192);
      }
      SCHED0();
    }
    if (t < 9) WAITN_BAR(t < 8 ? 9 : 2); else LGKM_BAR();
    SCHED0();
#pragma unroll
    for (int i = 0; i < NM; ++i) {
      MMA(i, fa1, fb1);
      SCHED0();
      if (t < 9) {
        if (i < 8) fb0[i] = tr1(BADDR((2 * t + 2) % 5, i));
        else if (i < 8 + MT) fa0[i - 8] = ld128(sm + ((t + 1) % 3) * A_SLOT + aoff0 + (i - 8) * 2048);
      }
      if (t < 7) {
        if (i == 1) DMA_B0((2 * t + 6) * SSTEP, bdst0 + ((2 * t + 1) % 5) * B_SLOT);
        if (i == 1 + DS) DMA_B1((2 * t + 6) * SSTEP, bdst0 + ((2 * t + 1) % 5) * B_SLOT + 8192);
        if (i == 1 + 2 * DS) DMA_A0((t + 3) * 128, adst0 + (t % 3) * A_SLOT);
        if (i == 1 + 3 * DS) DMA_A1((t + 3) * 128, adst0 + (t % 3) * A_SLOT + 8192);
      }
      if (i == 4 && t == 8) {
        asm volatile("global_load_dwordx4 %0, %1, off sc1" : "=&v"(ackv) : "v"(ackp) : "memory");
#pragma unroll
        for (int m = 0; m < MT; ++m) asm volatile("global_load_dwordx4 %0, %1, off offset:%2 sc1" : "=&v"(stv[m]) : "v"(stp), "n"(m * 256) : "memory");
      }
      SCHED0();
    }
  }
  SCHED0();
  STAMPAT(3);

  if (fr == 15) {
#pragma unroll
    for (int j = 0; j < 4; ++j) *(LAS f32x4*)(sm + L_CS + w * 256 + j * 64 + g * 16) = acc[MT - 1][j];
  }
  if (MT == 6) asm volatile("s_waitcnt vmcnt(0) lgkmcnt(0)" : "+v"(ackv), "+v"(stv[0]), "+v"(stv[1]), "+v"(stv[2]), "+v"(stv[3]), "+v"(stv[4]), "+v"(stv[MT - 1]) :: "memory");
  else asm volatile("s_waitcnt vmcnt(0) lgkmcnt(0)" : "+v"(ackv), "+v"(stv[0]), "+v"(stv[1]), "+v"(stv[2]), "+v"(stv[3]), "+v"(stv[4]) :: "memory");
  f32x4 cs[4];
#pragma unroll
  for (int j = 0; j < 4; ++j) cs[j] = *(const LAS f32x4*)(sm + L_CS + w * 256 + j * 64 + g * 16);
  {
    const u32x2 nv = *(const LAS u32x2*)(sm + L_NONCE);
    for (unsigned spins = 0; spins < SPIN_MAX; ++spins) {
      bool ok = (ackv[0] == nv[0]) && (ackv[1] == nv[1]);
#pragma unroll
      for (int m = 0; m < MT; ++m) ok = ok && (stv[m][2] == ackv[2]) && (stv[m][3] == ackv[3]);
      if (all_ok(ok)) break;
      __builtin_amdgcn_s_sleep(4);
      ackv = ld128_sc1(ackp);
#pragma unroll
      for (int m = 0; m < MT; ++m) stv[m] = ld128_sc1(stp + m * 256);
    }
  }
  const u32x2 ep = u32x2{ackv[2], ackv[3]};
  if (w == 0 && lane == 0) st64_sc1(ws + WS_DONE + bid * 64, ep);
  if (GU) {
    const unsigned vB = p2_vB(w, lane);
    p2_issueB_down(srd_wd, vB, c0p2, w, smb, 0, rot);
    p2_issueB_down(srd_wd, vB, c0p2, w, smb, 1, rot);
    p2_issueB_down(srd_wd, vB, c0p2, w, smb, 2, rot);
  }
  float rstd[MT];
#pragma unroll
  for (int m = 0; m < MT; ++m) {
    const f32x2 nm = sp2(opq(-u2f(stv[m][0])));
    rstd[m] = opq(u2f(stv[m][1]));
#pragma unroll
    for (int j = 0; j < 4; ++j) {
      f32x2 l = fma2(nm, lo2(cs[j]), lo2(acc[m][j])), h = fma2(nm, hi2(cs[j]), hi2(acc[m][j]));
      acc[m][j] = f32x4{l[0], l[1], h[0], h[1]};
    }
  }
  STAMPAT(4);

  if (GU) {
    const float K0 = -2.0f * 0.7978845608028654f * 1.4426950408889634f;
    const float K1c = K0 * 0.044715f;
    ldsp stg = sm + w * 7680;
    char* hg = ws + WS_H + ct * 256 + wn * 64 + (lane & 3) * 16;
#pragma unroll
    for (int m = 0; m <= MT; ++m) {
      if (m < MT) {
        const float r = rstd[m], r2 = r * r;
        const f32x2 c0 = sp2(opq(K0 * r)), c1 = sp2(opq(K1c * r2 * r)), ir2 = sp2(opq(__builtin_amdgcn_rcpf(r2)));
        u32x2 hp[2];
#pragma unroll
        for (int j = 0; j < 2; ++j) {
#pragma unroll
          for (int q = 0; q < 2; ++q) {
            const f32x2 a = q ? hi2(acc[m][j]) : lo2(acc[m][j]), b = q ? hi2(acc[m][j + 2]) : lo2(acc[m][j + 2]);
            const f32x2 z = a * fma2(a * a, c1, c0);
            const f32x2 e = f32x2{__builtin_amdgcn_exp2f(z[0]), __builtin_amdgcn_exp2f(z[1])};
            const f32x2 d = fma2(e, ir2, ir2);
            const f32x2 hv = (a * b) * f32x2{__builtin_amdgcn_rcpf(d[0]), __builtin_amdgcn_rcpf(d[1])};
            hp[j][q] = pk2(hv[0], hv[1]);
          }
        }
        *(LAS u32x2*)(stg + (m * 16 + fr) * 80 + g * 8) = hp[0];
        *(LAS u32x2*)(stg + (m * 16 + fr) * 80 + 32 + g * 8) = hp[1];
      }
      if (m >= 1) {
        const int rl = (m - 1) * 16 + (lane >> 2);
        u32x4 v = *(const LAS u32x4*)(stg + rl * 80 + (lane & 3) * 16);
        const int sl = mh * 96 + rl;
        const int ri = sl < 95 ? sl : sl - 1;
        if (sl != 95 && ri < nrow) st128_sc1(hg + (size_t)(r0 + ri) * 3072, v);
      }
    }
    STAMPAT(5);
    WAITN_BAR(0);
    if (w == 0 && lane == 0) st64_sc1(ws + WS_HF + rg * 128 + ct * 8, ep);
    STAMPAT(6);
  } else {
    const int head = ct * 4 + wn;
    bf16x8 wq[2], wk[2];
#pragma unroll
    for (int ft = 0; ft < 2; ++ft) {
      wq[ft] = asb(u32x4{wpk[4 * ft], wpk[4 * ft + 1], g == 0 ? wpk[4 * ft + 2] : 0u, g == 0 ? wpk[4 * ft + 3] : 0u});
      wk[ft] = asb(u32x4{wpk[4 * ft], wpk[4 * ft + 1], g == 1 ? wpk[4 * ft + 2] : 0u, g == 1 ? wpk[4 * ft + 3] : 0u});
    }
    const unsigned tag = ep[0] ^ (ep[1] * 0x9E3779B1u);
    const float L2E = 1.4426950408889634f;
    const f32x4 z4 = f32x4{0.f, 0.f, 0.f, 0.f};
    bool live[6];
    u32x2 rem[6];
#pragma unroll
    for (int m = 0; m < 6; ++m) {
      int s = mh * 96 + m * 16 + fr;
      int ri = s < 95 ? s : s - 1;
      live[m] = (m < MT) && (s != 95) && (ri < nrow);
      if (m < MT) rem[m] = u32x2{pk2(acc[m][3][0], acc[m][3][1]), pk2(acc[m][3][2], acc[m][3][3])};
    }
    ldsp pk_img = sm + w * 17408;
    ldsp v_img = pk_img + 96 * 104;
    {
      unsigned kf[6][5];
      u32x2 vv[6], ve[6];
#pragma unroll
      for (int mp = 0; mp < 3; ++mp) {
        f32x2 y01[2], y23[2], y4;
#pragma unroll
        for (int hh = 0; hh < 2; ++hh) {
          const int m = 2 * mp + hh;
          if (m < MT) {
            const f32x2 r = sp2(rstd[m]);
            const float c = opq(rstd[m] * L2E);
            u32x4 kb = u32x4{pk2(acc[m][1][0], acc[m][1][1]), pk2(acc[m][1][2], acc[m][1][3]), rem[m][0], rem[m][1]};
            f32x4 k0 = mfma32(wk[0], asb(kb), z4);
            f32x4 k1 = mfma32(wk[1], asb(kb), z4);
            y01[hh] = lo2(k0) * sp2(c); y23[hh] = hi2(k0) * sp2(c); y4[hh] = k1[0] * c;
            vv[m] = u32x2{pk2v(lo2(acc[m][2]) * r), pk2v(hi2(acc[m][2]) * r)};
            u32x2 vr = u32x2{pk2v(lo2(acc[m][3]) * r), pk2v(hi2(acc[m][3]) * r)};
            ve[m] = (g == 2) ? vr : u32x2{0x3F803F80u, 0x3F803F80u};
          } else {
            y01[hh] = sp2(0.f); y23[hh] = sp2(0.f); y4[hh] = 0.f;
            vv[m] = u32x2{0u, 0u}; ve[m] = vv[m];
          }
        }
        featmap2(y01[0], y23[0], y01[1], y23[1], y4, live[2 * mp], live[2 * mp + 1], g, kf[2 * mp], kf[2 * mp + 1]);
      }
      SCHED0();
#pragma unroll
      for (int m = 0; m < 6; ++m) {
        int tok = m * 16 + fr;
        *(LAS u32x2*)(pk_img + tok * 104 + g * 8) = u32x2{kf[m][0], kf[m][1]};
        *(LAS u32x2*)(pk_img + tok * 104 + 32 + g * 8) = u32x2{kf[m][2], kf[m][3]};
        *(LAS u32x2*)(pk_img + tok * 104 + 64 + g * 8) = u32x2{kf[m][4], 0u};
        *(LAS u32x2*)(v_img + tok * 72 + g * 8) = vv[m];
        *(LAS u32x2*)(v_img + tok * 72 + 32 + (g ^ 2) * 8) = ve[m];
      }
      LGKM0();
      SCHED0();
    }
    f32x4 kv[3][2];
#pragma unroll
    for (int ft = 0; ft < 3; ++ft)
#pragma unroll
      for (int dt = 0; dt < 2; ++dt) kv[ft][dt] = z4;
    {
      const int q = fr >> 2, p = lane & 3;
#pragma unroll
      for (int s = 0; s < 3; ++s) {
        int row = 32 * s + 8 * g + q;
        bf16x8 a[3], b[2];
#pragma unroll
        for (int ft = 0; ft < 3; ++ft) a[ft] = tr2(pk_img + row * 104 + ft * 32 + p * 8, pk_img + (row + 4) * 104 + ft * 32 + p * 8);
#pragma unroll
        for (int dt = 0; dt < 2; ++dt) b[dt] = tr2(v_img + row * 72 + dt * 32 + p * 8, v_img + (row + 4) * 72 + dt * 32 + p * 8);
#pragma unroll
        for (int ft = 0; ft < 3; ++ft)
#pragma unroll
          for (int dt = 0; dt < 2; ++dt) kv[ft][dt] = mfma32(a[ft], b[dt], kv[ft][dt]);
      }
    }
    {
      f32x4 o5[5] = {kv[0][0], kv[1][0], kv[0][1], kv[1][1], f32x4{kv[2][0][0], kv[2][0][1], kv[2][1][0], kv[2][1][1]}};
      ldsp ex = sm + (4 + wn) * 17408;
      if (mh == 1) {
        LGKM0();
#pragma unroll
        for (int i = 0; i < 5; ++i) *(LAS f32x4*)(ex + i * 1024 + lane * 16) = o5[i];
      }
      LGKM_BAR();
      if (mh == 0) {
        unsigned pp[10];
#pragma unroll
        for (int i = 0; i < 5; ++i) {
          f32x4 e = *(const LAS f32x4*)(ex + i * 1024 + lane * 16);
          pp[2 * i] = pk2(o5[i][0] + e[0], o5[i][1] + e[1]);
          pp[2 * i + 1] = pk2(o5[i][2] + e[2], o5[i][3] + e[3]);
        }
        char* kp = ws + WS_KV + (size_t)(rg * NH + head) * 4096 + lane * 16;
        st128_sc1(kp, u32x4{pp[0], pp[1], pp[2], tag});
        st128_sc1(kp + 1024, u32x4{pp[3], pp[4], pp[5], tag});
        st128_sc1(kp + 2048, u32x4{pp[6], pp[7], pp[8], tag});
        st128_sc1(kp + 3072, u32x4{pp[9], 0u, 0u, tag});
      }
    }
    STAMPAT(5);
    unsigned qf[6][5];
#pragma unroll
    for (int mp = 0; mp < 3; ++mp) {
      f32x2 y01[2], y23[2], y4;
#pragma unroll
      for (int hh = 0; hh < 2; ++hh) {
        const int m = 2 * mp + hh;
        if (m < MT) {
          const float c = opq(rstd[m] * L2E);
          u32x4 qb = u32x4{pk2(acc[m][0][0], acc[m][0][1]), pk2(acc[m][0][2], acc[m][0][3]), rem[m][0], rem[m][1]};
          f32x4 q0 = mfma32(wq[0], asb(qb), z4);
          f32x4 q1 = mfma32(wq[1], asb(qb), z4);
          y01[hh] = lo2(q0) * sp2(c); y23[hh] = hi2(q0) * sp2(c); y4[hh] = q1[0] * c;
        } else {
          y01[hh] = sp2(0.f); y23[hh] = sp2(0.f); y4[hh] = 0.f;
        }
      }
      featmap2(y01[0], y23[0], y01[1], y23[1], y4, true, true, g, qf[2 * mp], qf[2 * mp + 1]);
    }
    STAMPAT(6);
    float ks[20];
    {
      {
        const char* p1 = ws + WS_KV + ((size_t)(mh * 6 + (lane < 6 ? lane : 0)) * NH + head) * 4096 + 3072 + 1008;
        for (unsigned spins = 0; spins < SPIN_MAX; ++spins) {
          u32x4 v = ld128_sc1(p1);
          if (all_ok(v[3] == tag)) break;
          __builtin_amdgcn_s_sleep(POLLS);
        }
      }
      u32x4 pv[6][4];
      const char* kp = ws + WS_KV + (size_t)((mh * 6) * NH + head) * 4096 + lane * 16;
      for (unsigned spins = 0; spins < SPIN_MAX; ++spins) {
#pragma unroll
        for (int c = 0; c < 6; ++c) {
          const char* p = kp + (size_t)c * NH * 4096;
          asm volatile("global_load_dwordx4 %0, %4, off sc1\n\tglobal_load_dwordx4 %1, %4, off offset:1024 sc1\n\t"
                       "global_load_dwordx4 %2, %4, off offset:2048 sc1\n\tglobal_load_dwordx4 %3, %4, off offset:3072 sc1"
                       : "=&v"(pv[c][0]), "=&v"(pv[c][1]), "=&v"(pv[c][2]), "=&v"(pv[c][3]) : "v"(p) : "memory");
        }
        asm volatile("s_waitcnt vmcnt(0)" : "+v"(pv[0][0]), "+v"(pv[0][1]), "+v"(pv[0][2]), "+v"(pv[0][3]), "+v"(pv[1][0]), "+v"(pv[1][1]), "+v"(pv[1][2]), "+v"(pv[1][3]),
                     "+v"(pv[2][0]), "+v"(pv[2][1]), "+v"(pv[2][2]), "+v"(pv[2][3]), "+v"(pv[3][0]), "+v"(pv[3][1]), "+v"(pv[3][2]), "+v"(pv[3][3]),
                     "+v"(pv[4][0]), "+v"(pv[4][1]), "+v"(pv[4][2]), "+v"(pv[4][3]), "+v"(pv[5][0]), "+v"(pv[5][1]), "+v"(pv[5][2]), "+v"(pv[5][3]) :: "memory");
        bool ok = true;
#pragma unroll
        for (int c = 0; c < 6; ++c)
#pragma unroll
          for (int i = 0; i < 4; ++i) ok = ok && (pv[c][i][3] == tag);
        if (all_ok(ok)) break;
        __builtin_amdgcn_s_sleep(2);
      }
      STAMPAT(7);
#pragma unroll
      for (int i = 0; i < 10; ++i) {
        float lo = 0.f, hi = 0.f;
#pragma unroll
        for (int c = 0; c < 6; ++c) {
          unsigned u = pv[c][i / 3][i % 3];
          lo += u2f(u << 16); hi += u2f(u & 0xffff0000u);
        }
        ks[2 * i] = lo; ks[2 * i + 1] = hi;
      }
      ldsp mine = sm + w * 17408 + 12288, other = sm + (w ^ 4) * 17408 + 12288;
      SCHED0();
#pragma unroll
      for (int i = 0; i < 5; ++i) *(LAS f32x4*)(mine + i * 1024 + lane * 16) = f32x4{ks[4 * i], ks[4 * i + 1], ks[4 * i + 2], ks[4 * i + 3]};
      LGKM_BAR();
#pragma unroll
      for (int i = 0; i < 5; ++i) {
        f32x4 e = *(const LAS f32x4*)(other + i * 1024 + lane * 16);
#pragma unroll
        for (int q = 0; q < 4; ++q) ks[4 * i + q] = (mh == 0) ? ks[4 * i + q] + e[q] : e[q] + ks[4 * i + q];
      }
    }
    const bf16x8 ka0 = asb(u32x4{pk2(ks[0], ks[1]), pk2(ks[2], ks[3]), pk2(ks[4], ks[5]), pk2(ks[6], ks[7])});
    const bf16x8 ka1 = asb(u32x4{pk2(ks[8], ks[9]), pk2(ks[10], ks[11]), pk2(ks[12], ks[13]), pk2(ks[14], ks[15])});
    const bf16x8 kb0 = asb(u32x4{pk2(ks[16], ks[17]), 0u, 0u, 0u});
    const bf16x8 kb1 = asb(u32x4{pk2(ks[18], ks[19]), 0u, 0u, 0u});
    {
      u32x2 om_[MT], or_[MT];
#pragma unroll
      for (int m = 0; m < MT; ++m) {
        bf16x8 b0 = asb(u32x4{qf[m][0], qf[m][1], qf[m][2], qf[m][3]});
        bf16x8 b1 = asb(u32x4{qf[m][4], 0u, 0u, 0u});
        f32x4 n0 = mfma32(kb0, b1, mfma32(ka0, b0, z4));
        f32x4 n1 = mfma32(kb1, b1, mfma32(ka1, b0, z4));
        float da = n1[0], db = n1[0];
        swap16(da, db);
        const f32x2 inv = sp2(__builtin_amdgcn_rcpf(db + 1e-6f));
        om_[m] = u32x2{pk2v(lo2(n0) * inv), pk2v(hi2(n0) * inv)};
        or_[m] = u32x2{pk2v(lo2(n1) * inv), pk2v(hi2(n1) * inv)};
      }
      SCHED0();
      ldsp stg = sm + w * 17408 + 8192;
#pragma unroll
      for (int m = 0; m < MT; ++m) {
        *(LAS u32x2*)(stg + (m * 16 + fr) * 32 + g * 8) = om_[m];
        if (g == 0) *(LAS u32x2*)(stg + 3072 + (m * 16 + fr) * 8) = or_[m];
      }
      LGKM0();
      const int tok0 = r0 + mh * 95;
      const int ntok = mh == 0 ? 95 : nrow - 95;
      char* om = ws + WS_OM + ((size_t)head * S_ + tok0) * 32;
      char* orr = ws + WS_OR + ((size_t)head * S_ + tok0) * 8;
      u32x4 tv[3];
      u32x2 tr[2];
#pragma unroll
      for (int i = 0; i < 3; ++i) tv[i] = *(const LAS u32x4*)(stg + i * 1024 + lane * 16);
#pragma unroll
      for (int i = 0; i < 2; ++i) tr[i] = *(const LAS u32x2*)(stg + 3072 + ((i * 64 + lane) < 96 ? (i * 64 + lane) : 0) * 8);
#pragma unroll
      for (int i = 0; i < 3; ++i)
        if (i * 32 + (lane >> 1) < ntok) st128_sc1(om + i * 1024 + lane * 16, tv[i]);
#pragma unroll
      for (int i = 0; i < 2; ++i)
        if (i * 64 + lane < ntok) st64_sc1(orr + (i * 64 + lane) * 8, tr[i]);
    }
    WAITN_BAR(0);
    if (w == 0 && lane == 0) st64_sc1(ws + WS_OF + rg * 128 + ct * 8, ep);
    STAMPAT(8);
  }
  return ep;
}

DI u32x2 service(ldsp sm, const char* x, char* ws, int sid, int w, int lane) {
  const int bid = __builtin_amdgcn_workgroup_id_x();
  const int g = lane >> 4, fr = lane & 15;
  STAMPAT(0);
  if (w == 0) {
    unsigned long long nn = __builtin_amdgcn_s_memrealtime();
    u32x2 nv = u32x2{(unsigned)nn, (unsigned)(nn >> 32)};
    if (lane == 0) {
      if (sid != 15) st64_sc1(ws + WS_REQ + bid * 64, nv);
      *(LAS u32x2*)(sm + L_NONCE) = nv;
    }
  }
  float mean[6], rstd[6];
  if (sid < 12) {
    const int r0 = rs_of(sid), nrow = rs_of(sid + 1) - r0;
    u32x4 v[6][5];
#pragma unroll
    for (int i = 0; i < 6; ++i) {
      int slot = (i * 8 + w) * 4 + g;
      int ri = slot < 95 ? slot : slot - 1;
      ri = ri < nrow ? ri : nrow - 1;
      const char* p = x + (size_t)(r0 + ri) * 1280 + fr * 16;
#pragma unroll
      for (int c = 0; c < 5; ++c) v[i][c] = *(const u32x4*)(p + c * 256);
    }
#pragma unroll
    for (int i = 0; i < 6; ++i) {
      float a = 0.f;
#pragma unroll
      for (int c = 0; c < 5; ++c)
#pragma unroll
        for (int e = 0; e < 4; ++e) a += u2f(v[i][c][e] << 16) + u2f(v[i][c][e] & 0xffff0000u);
      a += dpp<0xB1>(a); a += dpp<0x4E>(a); a += dpp<0x141>(a); a += dpp<0x140>(a);
      mean[i] = a * (1.0f / 640.0f);
      float b = 0.f;
#pragma unroll
      for (int c = 0; c < 5; ++c)
#pragma unroll
        for (int e = 0; e < 4; ++e) {
          float lo = u2f(v[i][c][e] << 16) - mean[i], hi = u2f(v[i][c][e] & 0xffff0000u) - mean[i];
          b = __builtin_fmaf(lo, lo, b);
          b = __builtin_fmaf(hi, hi, b);
        }
      b += dpp<0xB1>(b); b += dpp<0x4E>(b); b += dpp<0x141>(b); b += dpp<0x140>(b);
      rstd[i] = __builtin_amdgcn_rsqf(b * (1.0f / 640.0f) + 1e-5f);
    }
  }
  LGKM_BAR();
  const u32x2 nv = *(const LAS u32x2*)(sm + L_NONCE);
  u32x2 ep;
  if (sid == 15) {
    ep = u32x2{nv[0] ^ 0x5bd1e995u, nv[1] ^ 0x80000000u};
    if (w < 4) {
      const int cb = w * 64 + lane;
      const bool me = (cb == bid);
      for (unsigned spins = 0; spins < SPIN_MAX; ++spins) {
        u32x2 rq = ld64_sc1(ws + WS_REQ + cb * 64);
        st128_sc1(ws + WS_ACK + cb * 64, u32x4{rq[0], rq[1], ep[0], ep[1]});
        u32x2 dn = ld64_sc1(ws + WS_DONE + cb * 64);
        if (all_ok(me || (dn[0] == ep[0] && dn[1] == ep[1]))) break;
        __builtin_amdgcn_s_sleep(16);
      }
    }
  } else {
    u32x4 ack;
    for (unsigned spins = 0; spins < SPIN_MAX; ++spins) {
      ack = ld128_sc1(ws + WS_ACK + bid * 64);
      if (all_ok(ack[0] == nv[0] && ack[1] == nv[1])) break;
      __builtin_amdgcn_s_sleep(4);
    }
    ep = u32x2{ack[2], ack[3]};
    if (w == 0 && lane == 0) st64_sc1(ws + WS_DONE + bid * 64, ep);
    if (sid < 12) {
#pragma unroll
      for (int i = 0; i < 6; ++i) {
        int slot = (i * 8 + w) * 4 + g;
        if (fr == 0 && slot < 176) st128_sc1(ws + WS_STAT + (size_t)(sid * 176 + slot) * 16, u32x4{f2u(mean[i]), f2u(rstd[i]), ep[0], ep[1]});
      }
    }
  }
  STAMPAT(1);
  return ep;
}

__global__ __launch_bounds__(512, 2) void fused(const char* __restrict__ x, const char* __restrict__ wqkv,
                                                const char* __restrict__ wphi, const char* __restrict__ wo,
                                                const char* __restrict__ wg, const char* __restrict__ wu,
                                                const char* __restrict__ wd, char* __restrict__ ws, char* __restrict__ out) {
  extern __shared__ __attribute__((aligned(16))) char smem[];
  ldsp sm = (ldsp)smem;
  const int tid = __builtin_amdgcn_workitem_id_x();
  const int b = __builtin_amdgcn_workgroup_id_x(), xcd = b & 7, idx = b >> 3;
  const int w = __builtin_amdgcn_readfirstlane(tid >> 6), lane = tid & 63;
  {
    unsigned long long pc;
    asm volatile("s_getpc_b64 %0" : "=s"(pc));
    const int sl = idx * 4 + (w - 4);
    if (w >= 4 && sl < CODE_KB)
      asm volatile("s_mov_b32 m0, %1\n\ts_nop 0\n\tglobal_load_lds_dwordx4 %0, off" ::"v"((const char*)(pc & ~255ull) + sl * 1024 + lane * 16), "s"((unsigned)(size_t)sm + L_DUMMY) : "memory");
  }
  const u32x4 srd_wd = make_srd(wd, DF_ * D_ * 2);
  const int rgl = idx / 5, c5 = idx % 5;
  const int rg = (xcd >> 2) * 6 + rgl, cq = xcd & 3;
  if (idx < 30 && c5 >= 3) {
    const int ct = cq * 2 + (c5 - 3);
    if (w < 4) phase1<6, false>(sm, x, wqkv, wphi, wg, wu, srd_wd, ws, rg, ct, 0, 0, w, lane);
    else phase1<5, false>(sm, x, wqkv, wphi, wg, wu, srd_wd, ws, rg, ct, 0, 0, w, lane);
    return;
  }
  const int p2i = idx < 30 ? rgl * 3 + c5 : 18 + (idx - 30);
  const int rt = (xcd >> 1) * 4 + p2i / 5, ct2 = (xcd & 1) * 5 + p2i % 5;
#define ROTF ((((p2i % 5) * 12) / 5 + (p2i / 5) * 3) % 12)
  const int rot = ROTF;
  u32x2 ep;
  if (idx < 30) {
    const int ct = cq * 3 + c5;
    if (w < 4) ep = phase1<6, true>(sm, x, wqkv, wphi, wg, wu, srd_wd, ws, rg, ct, ct2 * 64, rot, w, lane);
    else ep = phase1<5, true>(sm, x, wqkv, wphi, wg, wu, srd_wd, ws, rg, ct, ct2 * 64, rot, w, lane);
  } else {
    ep = service(sm, x, ws, b - 240, w, lane);
    {
      const u32x4 srd_wo = make_srd(wo, D_ * D_ * 2);
      const unsigned vp = (lane >> 3) * 1280 + (lane & 7) * 16 + (xcd & 1) * 640;
      const unsigned dst = (unsigned)(size_t)sm + w * 1024;
      const int w16 = (idx - 30) * 8 + w;
      if (!(b == 255 && w < 4)) {
#pragma unroll 1
        for (int p = w16; p < 192; p += 16) {
#pragma unroll
          for (int c = 0; c < 5; ++c) dma_b(srd_wd, vp, p * 8 * 1280 + c * 128, dst);
          asm volatile("s_waitcnt vmcnt(30)" ::: "memory");
        }
#pragma unroll 1
        for (int p = w16; p < 80; p += 16) {
#pragma unroll
          for (int c = 0; c < 5; ++c) dma_b(srd_wo, vp, p * 8 * 1280 + c * 128, dst);
          asm volatile("s_waitcnt vmcnt(30)" ::: "memory");
        }
      }
      WAITN_BAR(0);
    }
    const unsigned vB = p2_vB(w, lane);
    const unsigned smb = (unsigned)(size_t)sm;
    p2_issueB_down(srd_wd, vB, ct2 * 64, w, smb, 0, rot);
    p2_issueB_down(srd_wd, vB, ct2 * 64, w, smb, 1, rot);
    p2_issueB_down(srd_wd, vB, ct2 * 64, w, smb, 2, rot);
  }
  phase2(sm, x, wo, srd_wd, ws, out, rt, ct2, rot, w, lane, ep);
}

#if defined(__HIP_DEVICE_COMPILE__)

#endif

extern "C" void kernel_launch(void* const* d_in, const int* in_sizes, int n_in, void* d_out, int out_size, void* d_ws,
                              size_t ws_size, hipStream_t stream) {
  (void)hipFuncSetAttribute((const void*)fused, hipFuncAttributeMaxDynamicSharedMemorySize, K_LDS);
  fused<<<256, 512, K_LDS, stream>>>((const char*)d_in[0], (const char*)d_in[1], (const char*)d_in[2], (const char*)d_in[3],
                                     (const char*)d_in[5], (const char*)d_in[6], (const char*)d_in[7], (char*)d_ws, (char*)d_out);
}
```

```cpp
#define ORDER 1
#define NOPF
#define POLLS 6
#include <hip/hip_runtime.h>
#include <hip/hip_bf16.h>
#include <stdint.h>

typedef __attribute__((ext_vector_type(8))) __bf16 bf16x8;
typedef __attribute__((ext_vector_type(2))) __bf16 bf16x2;
typedef __attribute__((ext_vector_type(4))) short s16x4;
typedef __attribute__((ext_vector_type(8))) short s16x8;
typedef __attribute__((ext_vector_type(4))) float f32x4;
typedef __attribute__((ext_vector_type(2))) float f32x2;
typedef __attribute__((ext_vector_type(4))) unsigned u32x4;
typedef __attribute__((ext_vector_type(2))) unsigned u32x2;
#define LAS __attribute__((address_space(3)))
#define DI __device__ __forceinline__
typedef LAS char* ldsp;

constexpr int S_ = 2048, D_ = 640, DF_ = 1536, NH = 32;
constexpr int NRG = 12;
constexpr size_t WS_H = 0;
constexpr size_t WS_KV = WS_H + (size_t)S_ * DF_ * 2;
constexpr size_t WS_OM = WS_KV + (size_t)NRG * NH * 4096;
constexpr size_t WS_OR = WS_OM + (size_t)NH * S_ * 32;
constexpr size_t WS_REQ = WS_OR + (size_t)NH * S_ * 8;
constexpr size_t WS_ACK = WS_REQ + 256 * 64;
constexpr size_t WS_DONE = WS_ACK + 256 * 64;
constexpr size_t WS_STAT = WS_DONE + 256 * 64;
constexpr size_t WS_HF = WS_STAT + 12 * 176 * 16;
constexpr size_t WS_KF = WS_HF + 12 * 128;
constexpr size_t WS_OF = WS_KF + 8 * 128;
constexpr size_t WS_DBG = 64u << 20;

constexpr int A_SLOT = 22528, B_SLOT = 16384;
constexpr int L_A = 0;
constexpr int L_B = 3 * A_SLOT;
constexpr int L_MISC = L_B + 5 * B_SLOT;
constexpr int L_DUMMY = L_MISC;
constexpr int L_NONCE = L_MISC + 1024;
constexpr int L_CS = L_NONCE + 64;
constexpr int K_LDS = L_CS + 2048;
constexpr int P2_A = 0, P2_B = 98304;
constexpr unsigned SPIN_MAX = 1u << 20;

#if defined(__HIP_DEVICE_COMPILE__)

#endif

#define STAMPAT(i)

DI float u2f(unsigned u) { return __builtin_bit_cast(float, u); }
DI unsigned f2u(float f) { return __builtin_bit_cast(unsigned, f); }
DI u32x4 make_srd(const void* p, unsigned bytes) {
  uint64_t a = (uint64_t)p;
  u32x4 r;
  r[0] = (unsigned)a;
  r[1] = (unsigned)(a >> 32) & 0xffffu;
  r[2] = bytes;
  r[3] = 0x00020000u;
  return r;
}
DI void dma_b(u32x4 srd, unsigned voff, unsigned soff, unsigned m0v) {
  asm volatile("s_mov_b32 m0, %3\n\ts_nop 0\n\tbuffer_load_dwordx4 %0, %1, %2 offen lds" ::"v"(voff), "s"(srd), "s"(soff), "s"(m0v) : "memory");
}
DI void dma_b_mask(u32x4 srd, unsigned voff, unsigned soff, unsigned m0v, unsigned long long mask) {
  asm volatile("s_mov_b32 m0, %3\n\ts_mov_b64 exec, %4\n\tbuffer_load_dwordx4 %0, %1, %2 offen lds\n\ts_mov_b64 exec, -1" ::"v"(voff), "s"(srd), "s"(soff), "s"(m0v), "s"(mask) : "memory");
}
DI void dma_b_sc1(u32x4 srd, unsigned voff, unsigned soff, unsigned m0v) {
  asm volatile("s_mov_b32 m0, %3\n\ts_nop 0\n\tbuffer_load_dwordx4 %0, %1, %2 offen sc1 lds" ::"v"(voff), "s"(srd), "s"(soff), "s"(m0v) : "memory");
}
DI u32x4 ld128_sc1(const char* p) { u32x4 v; asm volatile("global_load_dwordx4 %0, %1, off sc1\n\ts_waitcnt vmcnt(0)" : "=&v"(v) : "v"(p) : "memory"); return v; }
DI u32x2 ld64_sc1(const char* p) { u32x2 v; asm volatile("global_load_dwordx2 %0, %1, off sc1\n\ts_waitcnt vmcnt(0)" : "=&v"(v) : "v"(p) : "memory"); return v; }
DI void st128_sc1(char* p, u32x4 v) { asm volatile("global_store_dwordx4 %0, %1, off sc1\n\ts_nop 1" ::"v"(p), "v"(v) : "memory"); }
DI void st64_sc1(char* p, u32x2 v) { asm volatile("global_store_dwordx2 %0, %1, off sc1" ::"v"(p), "v"(v) : "memory"); }
#define WAITN_BAR(N) asm volatile("s_waitcnt vmcnt(%0) lgkmcnt(0)\n\ts_barrier" ::"n"(N) : "memory")
#define LGKM_BAR() asm volatile("s_waitcnt lgkmcnt(0)\n\ts_barrier" ::: "memory")
#define LGKM0() asm volatile("s_waitcnt lgkmcnt(0)" ::: "memory")
#define MEMFENCE() asm volatile("" ::: "memory")
#define SCHED0() __builtin_amdgcn_sched_barrier(0)

DI bf16x8 ld128(ldsp p) { return *(const LAS bf16x8*)p; }
DI s16x4 tr1(ldsp p) { return __builtin_amdgcn_ds_read_tr16_b64_v4i16((LAS s16x4*)p); }
DI bf16x8 cat(s16x4 a, s16x4 b) { return __builtin_bit_cast(bf16x8, __builtin_shufflevector(a, b, 0, 1, 2, 3, 4, 5, 6, 7)); }
DI bf16x8 tr2(ldsp p0, ldsp p1) { return cat(tr1(p0), tr1(p1)); }
DI f32x4 mfma32(bf16x8 a, bf16x8 b, f32x4 c) { return __builtin_amdgcn_mfma_f32_16x16x32_bf16(a, b, c, 0, 0, 0); }
DI bf16x8 asb(u32x4 v) { return __builtin_bit_cast(bf16x8, v); }
DI unsigned pk2(float a, float b) {
  bf16x2 v;
  v[0] = (__bf16)a;
  v[1] = (__bf16)b;
  return __builtin_bit_cast(unsigned, v);
}
DI void swap16(float& a, float& b) {
  auto r = __builtin_amdgcn_permlane16_swap(f2u(a), f2u(b), false, false);
  a = u2f(r[0]); b = u2f(r[1]);
}
DI void swap32(float& a, float& b) {
  auto r = __builtin_amdgcn_permlane32_swap(f2u(a), f2u(b), false, false);
  a = u2f(r[0]); b = u2f(r[1]);
}
template <int CTRL> DI float dpp(float x) { return u2f(__builtin_amdgcn_update_dpp(0u, f2u(x), CTRL, 0xf, 0xf, false)); }
DI float fmx(float a, float b) { return __builtin_fmaxf(a, b); }
DI float fmn(float a, float b) { return __builtin_fminf(a, b); }
DI float rawmax(float a, float b) { float r; asm("v_max_f32 %0, %1, %2" : "=v"(r) : "v"(a), "v"(b)); return r; }
DI float opq(float x) { asm("" : "+v"(x)); return x; }
DI f32x2 lo2(f32x4 v) { return __builtin_shufflevector(v, v, 0, 1); }
DI f32x2 hi2(f32x4 v) { return __builtin_shufflevector(v, v, 2, 3); }
DI f32x2 sp2(float x) { return f32x2{x, x}; }
DI f32x2 fma2(f32x2 a, f32x2 b, f32x2 c) { return __builtin_elementwise_fma(a, b, c); }
DI int rs_of(int rg) { return (512 * rg) / 3; }
DI int rg_of(int row) { return (3 * row + 2) >> 9; }
DI bool all_ok(bool ok) { return __builtin_amdgcn_ballot_w64(ok) == ~0ull; }

DI void wait_flags(const char* p, bool need, u32x2 ep) {
  for (unsigned spins = 0; spins < SPIN_MAX; ++spins) {
    u32x2 v = ld64_sc1(p);
    if (all_ok(!need || (v[0] == ep[0] && v[1] == ep[1]))) break;
    __builtin_amdgcn_s_sleep(POLLS);
  }
}

DI f32x2 ex2(f32x2 v) { return f32x2{__builtin_amdgcn_exp2f(v[0]), __builtin_amdgcn_exp2f(v[1])}; }
DI unsigned pk2v(f32x2 v) { return pk2(v[0], v[1]); }
DI void featmap2(f32x2 a01, f32x2 a23, f32x2 b01, f32x2 b23, f32x2 e4, bool livea, bool liveb, int g, unsigned (&oa)[5], unsigned (&ob)[5]) {
  float a0 = fmx(fmx(fmx(a01[0], a01[1]), a23[0]), fmx(a23[1], e4[0]));
  float a1 = -fmn(fmn(fmn(a01[0], a01[1]), a23[0]), fmn(a23[1], e4[0]));
  float b0 = fmx(fmx(fmx(b01[0], b01[1]), b23[0]), fmx(b23[1], e4[1]));
  float b1 = -fmn(fmn(fmn(b01[0], b01[1]), b23[0]), fmn(b23[1], e4[1]));
  swap16(a0, a1); swap16(b0, b1);
  float ca = rawmax(a0, a1), cb = rawmax(b0, b1);
  swap32(ca, cb);
  float d0 = rawmax(ca, cb), d1 = d0;
  swap32(d0, d1);
  float Ma = d0, Na = d0, Mb = d1, Nb = d1;
  swap16(Ma, Na); swap16(Mb, Nb);
  const f32x2 pa01 = ex2(a01 - sp2(Ma)), pa23 = ex2(a23 - sp2(Ma)), na01 = ex2(-a01 - sp2(Na)), na23 = ex2(-a23 - sp2(Na));
  const f32x2 pb01 = ex2(b01 - sp2(Mb)), pb23 = ex2(b23 - sp2(Mb)), nb01 = ex2(-b01 - sp2(Nb)), nb23 = ex2(-b23 - sp2(Nb));
  const float pa4 = __builtin_amdgcn_exp2f(e4[0] - Ma), na4 = __builtin_amdgcn_exp2f(-e4[0] - Na);
  const float pb4 = __builtin_amdgcn_exp2f(e4[1] - Mb), nb4 = __builtin_amdgcn_exp2f(-e4[1] - Nb);
  f32x2 t;
  t = pa01 + pa23; a0 = (t[0] + t[1]) + pa4;
  t = na01 + na23; a1 = (t[0] + t[1]) + na4;
  t = pb01 + pb23; b0 = (t[0] + t[1]) + pb4;
  t = nb01 + nb23; b1 = (t[0] + t[1]) + nb4;
  swap16(a0, a1); swap16(b0, b1);
  ca = a0 + a1; cb = b0 + b1;
  swap32(ca, cb);
  d0 = __builtin_amdgcn_rcpf(ca + cb);
  if (!(g < 2 ? livea : liveb)) d0 = 0.f;
  d1 = d0;
  swap32(d0, d1);
  Ma = d0; Na = d0; Mb = d1; Nb = d1;
  swap16(Ma, Na); swap16(Mb, Nb);
  oa[0] = pk2v(pa01 * sp2(Ma)); oa[1] = pk2v(pa23 * sp2(Ma));
  oa[2] = pk2v(na01 * sp2(Na)); oa[3] = pk2v(na23 * sp2(Na));
  oa[4] = pk2(pa4 * Ma, na4 * Na);
  ob[0] = pk2v(pb01 * sp2(Mb)); ob[1] = pk2v(pb23 * sp2(Mb));
  ob[2] = pk2v(nb01 * sp2(Nb)); ob[3] = pk2v(nb23 * sp2(Nb));
  ob[4] = pk2(pb4 * Mb, nb4 * Nb);
}

struct P2Ctx { unsigned vB, vR; int c0; };
DI unsigned p2_vB(int w, int lane) {
  const int l3 = lane >> 3, l7 = lane & 7;
  int fb = ((l3 >> 1) & 1) | ((w & 1) << 1);
  int ch = (((l7 >> 1) ^ fb) << 1) | (l7 & 1);
  return l3 * 1280 + ch * 16;
}
DI int p2_rot(int s, int rot) { int r = s + rot; return r >= 12 ? r - 12 : r; }
DI void p2_issueB_down(u32x4 srd_wd, unsigned vB, int c0, int w, unsigned smb, int s, int rot) {
  const int sr = p2_rot(s, rot);
  dma_b(srd_wd, vB, (128 * sr + 8 * w) * 1280 + c0 * 2, smb + P2_B + (s % 3) * 16384 + w * 1024);
  dma_b(srd_wd, vB, (128 * sr + 8 * w + 64) * 1280 + c0 * 2, smb + P2_B + (s % 3) * 16384 + w * 1024 + 8192);
}

DI void phase2(ldsp sm, const char* x, const char* wo, const u32x4 srd_wd, char* ws, char* out, int rt, int ct, int rot, int w, int lane, u32x2 ep) {
  const int r0 = rt * 128, c0 = ct * 64;
  const int g = lane >> 4, fr = lane & 15, l3 = lane >> 3, l7 = lane & 7;
  const int kh = w & 1, nh = (w >> 1) & 1, mh = w >> 2;
  const unsigned smb = (unsigned)(size_t)sm;
  const u32x4 srd_h = make_srd(ws + WS_H, S_ * DF_ * 2);
  const u32x4 srd_wo = make_srd(wo, D_ * D_ * 2);
  const u32x4 srd_om = make_srd(ws + WS_OM, NH * S_ * 32);
  const u32x4 srd_or = make_srd(ws + WS_OR, NH * S_ * 8);
  const unsigned vB = p2_vB(w, lane);
  const unsigned vA = l3 * 3072 + ((l7 ^ ((4 * (w & 1) + (l3 >> 1)) & 7)) * 16);
  const unsigned vR = vB - l3 * 1280 + (20 * (lane >> 5) + 16 + (l3 & 3)) * 1280;
  const unsigned vL = lane * 16;
  const int rgl = rg_of(r0), rgh = rg_of(r0 + 127);

#define P2_PIECE(s, p) do { \
    constexpr int i_ = (p) < 2 ? (p) : (p) - 2; \
    if ((s) < 12) { \
      if ((p) < 2) dma_b(srd_wd, vB, (128 * p2_rot((s), rot) + 8 * w + 64 * i_) * 1280 + c0 * 2, smb + P2_B + ((s) % 3) * 16384 + i_ * 8192 + w * 1024); \
      else dma_b_sc1(srd_h, vA, (r0 + 8 * w + 64 * (i_ & 1)) * 3072 + p2_rot((s), rot) * 256 + (i_ >> 1) * 128, smb + P2_A + ((s) % 3) * 32768 + (i_ >> 1) * 16384 + (i_ & 1) * 8192 + w * 1024); \
    } else if ((s) < 16) { \
      if ((p) < 2) dma_b(srd_wo, vB, (160 * ((s) - 12) + 20 * ((w + 8 * i_) >> 1) + 8 * (w & 1)) * 1280 + c0 * 2, smb + P2_B + ((s) % 3) * 16384 + i_ * 8192 + w * 1024); \
      else dma_b_sc1(srd_om, vL, ((8 * ((s) - 12) + 2 * i_ + (w >> 2)) * S_ + r0) * 32 + (w & 3) * 1024, smb + P2_A + ((s) % 3) * 32768 + i_ * 8192 + w * 1024); \
    } else { \
      if ((p) < 2) dma_b(srd_wo, vR, (40 * (w + 8 * i_)) * 1280 + c0 * 2, smb + P2_B + ((s) % 3) * 16384 + i_ * 8192 + w * 1024); \
      else dma_b_sc1(srd_or, vL, ((8 * i_ + w) * S_ + r0) * 8, smb + P2_A + ((s) % 3) * 32768 + i_ * 8192 + w * 1024); \
    } } while (0)

  const int aD = P2_A + kh * 16384 + (64 * mh + fr) * 128 + ((g ^ ((fr >> 1) & 7)) * 16);
  const int aM = P2_A + (4 * kh + (g >> 1)) * 4096 + (64 * mh + fr) * 32 + (g & 1) * 16;
  const int aR = P2_A + (16 * kh + 2 * g) * 1024 + (64 * mh + fr) * 8;
  int bO[2];
  {
    const int q = fr >> 2, p = lane & 3;
    const int f = ((q >> 1) & 1) | ((g & 1) << 1);
#pragma unroll
    for (int j = 0; j < 2; ++j) bO[j] = P2_B + (64 * kh + 8 * g + q) * 128 + (((2 * nh + j) ^ f) * 32) + p * 8;
  }
  f32x4 acc[4][2];
#pragma unroll
  for (int m = 0; m < 4; ++m)
#pragma unroll
    for (int j = 0; j < 2; ++j) acc[m][j] = f32x4{0.f, 0.f, 0.f, 0.f};

  STAMPAT(8);
  if (w == 0) wait_flags(ws + WS_HF + (size_t)(lane < 16 ? rgl : rgh) * 128 + (lane & 15) * 8, lane < 32 && (lane & 15) < 12, ep);
  WAITN_BAR(0);
  STAMPAT(9);
#define P2_A3(s) do { P2_PIECE(s, 2); P2_PIECE(s, 3); P2_PIECE(s, 4); P2_PIECE(s, 5); } while (0)
  P2_A3(0); P2_A3(1); P2_A3(2);
  u32x4 xr[2];
  bf16x8 fa[2][8];
  s16x4 fb[2][8];
#define P2_RD(s, i) do { \
    if ((i) < 8) fb[(s) & 1][i] = tr1(sm + ((s) % 3) * 16384 + bO[((i) >> 1) & 1] + ((i) >> 2) * 4096 + ((i) & 1) * 512); \
    else if ((i) < 16) { \
      constexpr int sl_ = ((i) - 8) >> 2, m_ = ((i) - 8) & 3; \
      if ((s) < 12) fa[(s) & 1][((i) - 8) & 7] = ld128(sm + ((s) % 3) * 32768 + (sl_ ? (aD ^ 64) : aD) + m_ * 2048); \
      else if ((s) < 16) fa[(s) & 1][((i) - 8) & 7] = ld128(sm + ((s) % 3) * 32768 + aM + sl_ * 8192 + m_ * 512); \
      else fa[(s) & 1][((i) - 8) & 7] = cat(*(const LAS s16x4*)(sm + ((s) % 3) * 32768 + aR + sl_ * 8192 + m_ * 128), *(const LAS s16x4*)(sm + ((s) % 3) * 32768 + aR + sl_ * 8192 + 1024 + m_ * 128)); } } while (0)
#define P2_MM(s, i) acc[((i) >> 1) & 3][(i) & 1] = mfma32(cat(fb[(s) & 1][((i) >> 3) * 4 + 2 * ((i) & 1)], fb[(s) & 1][((i) >> 3) * 4 + 2 * ((i) & 1) + 1]), fa[(s) & 1][((i) >> 3) * 4 + (((i) >> 1) & 3)], acc[((i) >> 1) & 3][(i) & 1])
#define P2_GROUP(s, q) do { \
    if ((s) >= 1 && (s) + 2 < 17) { \
      if ((s) + 2 == 12 && (q) == 2) { \
        STAMPAT(10); \
        if (w == 0) wait_flags(ws + WS_OF + (size_t)(lane < 16 ? rgl : rgh) * 128 + (lane & 15) * 8, lane < 32 && (lane & 15) < 8, ep); \
        asm volatile("s_barrier" ::: "memory"); \
        STAMPAT(11); \
      } \
      P2_PIECE((s) + 2, q); } \
    SCHED0(); \
    if ((s) < 17) { P2_RD(s, 3 * (q)); P2_RD(s, 3 * (q) + 1); P2_RD(s, 3 * (q) + 2); } \
    SCHED0(); \
    if ((s) >= 1) { P2_MM((s) - 1, ((q) * 8) / 3); P2_MM((s) - 1, ((q) * 8) / 3 + 1); if (((q) * 8) / 3 + 2 < (((q) + 1) * 8) / 3) P2_MM((s) - 1, ((q) * 8) / 3 + 2); } \
    SCHED0(); } while (0)
#define P2_STAGE(s) do { \
    SCHED0(); \
    if ((s) < 17) WAITN_BAR((s) == 0 ? 8 : ((s) == 1 ? 4 : ((s) < 16 ? 6 : 0))); \
    SCHED0(); \
    P2_GROUP(s, 0); P2_GROUP(s, 1); P2_GROUP(s, 2); P2_GROUP(s, 3); P2_GROUP(s, 4); P2_GROUP(s, 5); } while (0)
  P2_STAGE(0); P2_STAGE(1); P2_STAGE(2); P2_STAGE(3); P2_STAGE(4); P2_STAGE(5); P2_STAGE(6); P2_STAGE(7); P2_STAGE(8);
  P2_STAGE(9); P2_STAGE(10); P2_STAGE(11); P2_STAGE(12); P2_STAGE(13); P2_STAGE(14); P2_STAGE(15);
  const int orow = 64 * mh + 32 * kh + (lane >> 2), ocol = c0 * 2 + nh * 64 + (lane & 3) * 16;
#pragma unroll
  for (int i = 0; i < 2; ++i)
    asm volatile("global_load_dwordx4 %0, %1, off" : "=&v"(xr[i]) : "v"(x + (size_t)(r0 + orow + 16 * i) * 1280 + ocol) : "memory");
  P2_STAGE(16); P2_STAGE(17);
  SCHED0();
  STAMPAT(12);
  f32x4 keep[2][2];
  {
    ldsp ex = sm + w * 4096 + lane * 16;
#pragma unroll
    for (int i = 0; i < 2; ++i)
#pragma unroll
      for (int j = 0; j < 2; ++j) {
        f32x4 snd;
#pragma unroll
        for (int e = 0; e < 4; ++e) {
          const float lo_ = acc[i][j][e], hi_ = acc[2 + i][j][e];
          snd[e] = kh ? lo_ : hi_;
          keep[i][j][e] = kh ? hi_ : lo_;
        }
        *(LAS f32x4*)(ex + (i * 2 + j) * 1024) = snd;
      }
  }
  LGKM_BAR();
  {
    ldsp ex = sm + (w ^ 1) * 4096 + lane * 16;
    ldsp tp = sm + 65536 + w * 4608;
#pragma unroll
    for (int i = 0; i < 2; ++i)
#pragma unroll
      for (int j = 0; j < 2; ++j) {
        const f32x4 e = *(const LAS f32x4*)(ex + (i * 2 + j) * 1024);
        *(LAS f32x4*)(tp + (i * 16 + fr) * 144 + j * 64 + g * 16) = keep[i][j] + e;
      }
    asm volatile("" : "+v"(xr[0]), "+v"(xr[1]));
#pragma unroll
    for (int i = 0; i < 2; ++i) {
      const f32x4 s0 = *(const LAS f32x4*)(tp + (i * 16 + (lane >> 2)) * 144 + (lane & 3) * 32);
      const f32x4 s1 = *(const LAS f32x4*)(tp + (i * 16 + (lane >> 2)) * 144 + (lane & 3) * 32 + 16);
      u32x4 o;
#pragma unroll
      for (int e = 0; e < 2; ++e) {
        o[e] = pk2(s0[2 * e] + u2f(xr[i][e] << 16), s0[2 * e + 1] + u2f(xr[i][e] & 0xffff0000u));
        o[2 + e] = pk2(s1[2 * e] + u2f(xr[i][2 + e] << 16), s1[2 * e + 1] + u2f(xr[i][2 + e] & 0xffff0000u));
      }
      *(u32x4*)(out + (size_t)(r0 + orow + 16 * i) * 1280 + ocol) = o;
    }
  }
}

template <int MT, bool GU>
DI u32x2 phase1(ldsp sm, const char* x, const char* wqkv, const char* wphi, const char* wg, const char* wu, const u32x4 srd_wd,
                char* ws, int rg, int ct, int c0p2, int rot, int w, int lane) {
  STAMPAT(0);
  constexpr int mh = (MT == 6) ? 0 : 1;
  const int wn = w & 3;
  const int r0 = rs_of(rg), nrow = rs_of(rg + 1) - r0;
  const unsigned smb = (unsigned)(size_t)sm;
  const int g = lane >> 4, fr = lane & 15;

  const u32x4 srdx = make_srd(x, S_ * D_ * 2);
  unsigned voffA[3];
#pragma unroll
  for (int c = 0; c < 3; ++c) {
    int slot = 8 * (w + 8 * c) + (lane >> 3);
    int ch = (lane & 7) ^ ((slot >> 1) & 7);
    int ri = slot < 95 ? slot : slot - 1;
    ri = ri < nrow ? ri : nrow - 1;
    voffA[c] = (unsigned)((r0 + ri) * 1280 + ch * 16);
  }
  if (w >= 6) voffA[2] = 0x40000000u;
  const unsigned long long FULL = ~0ull, PART = 0x00FFFFFFFFFFFFFFull;
  const unsigned long long mask1 = (w == 3) ? PART : FULL, mask2 = (w == 5) ? PART : FULL;
  const unsigned adst0 = smb + L_A + w * 1024;
  u32x4 srd0, srd1;
  unsigned voff0, voff1;
  constexpr unsigned SSTEP = GU ? 32 * 3072 : 32 * 3840;
  if (GU) {
    srd0 = make_srd(wg, D_ * DF_ * 2);
    srd1 = make_srd(wu, D_ * DF_ * 2);
    int krow = 4 * w + (lane >> 4);
    int chp = lane & 15;
    int f = (krow & 3) | (((krow >> 3) & 1) << 2);
    int ch = (((chp >> 1) ^ f) << 1) | (chp & 1);
    voff0 = voff1 = krow * 3072 + ct * 256 + ch * 16;
  } else {
    srd0 = srd1 = make_srd(wqkv, D_ * 3 * D_ * 2);
    unsigned vo[2];
#pragma unroll
    for (int c = 0; c < 2; ++c) {
      int cidx = (w + 8 * c) * 64 + lane;
      int rho = cidx / 30, cc = cidx % 30;
      int seg = cc / 10, ch = cc % 10;
      int k = (rho & 16) | ((rho & 4) << 1) | ((rho & 8) >> 1) | (rho & 3);
      vo[c] = (cidx < 960) ? (unsigned)(k * 3840 + seg * 1280 + ct * 160 + ch * 16) : 0x40000000u;
    }
    voff0 = vo[0];
    voff1 = vo[1];
  }
  const unsigned bdst0 = smb + L_B + w * 1024;
#define KROT (((rg%6)*5/3+ct*2)%10)
  const unsigned krot = KROT;
#define RA(so) ((so) + krot * 128u >= 1280u ? (so) + krot * 128u - 1280u : (so) + krot * 128u)
#define RB(so) ((so) + krot * 2u * SSTEP >= 20u * SSTEP ? (so) + krot * 2u * SSTEP - 20u * SSTEP : (so) + krot * 2u * SSTEP)
#define DMA_B0(soff, dst) dma_b(srd0, voff0, RB(soff), (dst))
#define DMA_B1(soff, dst) dma_b(srd1, voff1, RB(soff), (dst))
#define DMA_A0(soff, dst) dma_b(srdx, voffA[0], RA(soff), (dst))
#define DMA_A1(soff, dst) dma_b_mask(srdx, voffA[1], RA(soff), (dst), mask1)
#define DMA_A2(soff, dst) dma_b_mask(srdx, voffA[2], RA(soff), (dst), mask2)
  const unsigned a2base = (w >= 6) ? smb + L_DUMMY : adst0 + 16384;
  const unsigned a2mul = (w >= 6) ? 0u : 1u;
  unsigned wraw[16];
  if (!GU) {
    const char* wp = wphi + (ct * 4 + wn) * 800;
    const char* p0 = wp + (5 * (fr >> 2) + (fr & 3)) * 2;
    const char* p1 = wp + (5 * (fr >> 2) + 4) * 2;
    const char* q0 = p0 + g * 160;
    const char* q1 = p1 + g * 160;
#pragma unroll
    for (int j = 0; j < 4; ++j) {
      asm volatile("global_load_ushort %0, %1, off offset:%2" : "=&v"(wraw[j]) : "v"(q0), "n"(j * 40) : "memory");
      asm volatile("global_load_ushort %0, %1, off offset:%2" : "=&v"(wraw[4 + j]) : "v"(p0), "n"(640 + j * 40) : "memory");
      asm volatile("global_load_ushort %0, %1, off offset:%2" : "=&v"(wraw[8 + j]) : "v"(q1), "n"(j * 40) : "memory");
      asm volatile("global_load_ushort %0, %1, off offset:%2" : "=&v"(wraw[12 + j]) : "v"(p1), "n"(640 + j * 40) : "memory");
    }
  }
  DMA_B0(0, bdst0); DMA_B1(0, bdst0 + 8192);
  DMA_A0(0, adst0); DMA_A1(0, adst0 + 8192);
  DMA_A2(0, a2base);
  DMA_B0(SSTEP, bdst0 + B_SLOT); DMA_B1(SSTEP, bdst0 + B_SLOT + 8192);
  DMA_B0(2 * SSTEP, bdst0 + 2 * B_SLOT); DMA_B1(2 * SSTEP, bdst0 + 2 * B_SLOT + 8192);
  DMA_A0(128, adst0 + A_SLOT); DMA_A1(128, adst0 + A_SLOT + 8192);
  DMA_A2(128, a2base + a2mul * A_SLOT);
  DMA_B0(3 * SSTEP, bdst0 + 3 * B_SLOT); DMA_B1(3 * SSTEP, bdst0 + 3 * B_SLOT + 8192);
  DMA_B0(4 * SSTEP, bdst0 + 4 * B_SLOT); DMA_B1(4 * SSTEP, bdst0 + 4 * B_SLOT + 8192);
  DMA_A0(256, adst0 + 2 * A_SLOT); DMA_A1(256, adst0 + 2 * A_SLOT + 8192);
  if (w == 0 && lane < 48) {
    int rr = lane >> 3;
    *(LAS u32x4*)(sm + L_A + (rr >> 1) * A_SLOT + ((rr & 1) ? 175 : 95) * 128 + (lane & 7) * 16) =
        u32x4{0x3F803F80u, 0x3F803F80u, 0x3F803F80u, 0x3F803F80u};
  }
  const int bid = __builtin_amdgcn_workgroup_id_x();
  if (w == 1) {
    unsigned long long nn = __builtin_amdgcn_s_memrealtime();
    u32x2 nv = u32x2{(unsigned)nn, (unsigned)(nn >> 32)};
    if (lane == 0) {
      st64_sc1(ws + WS_REQ + bid * 64, nv);
      *(LAS u32x2*)(sm + L_NONCE) = nv;
    }
  }

  const int aoff0 = L_A + mh * 6 * 2048 + fr * 128 + ((g ^ ((fr >> 1) & 7)) * 16);
  const int aoff1 = aoff0 ^ 64;
  int boff[4];
  constexpr int TSTEP = GU ? 1024 : 3840;
  {
    const int q = fr >> 2, p = lane & 3;
    if (GU) {
      int f = q | ((g & 1) << 2);
#pragma unroll
      for (int j = 0; j < 4; ++j)
        boff[j] = L_B + (j >> 1) * 8192 + (8 * g + q) * 256 + (((2 * wn + (j & 1)) ^ f) * 32) + p * 8;
    } else {
      int rho = 16 * (g >> 1) + 4 * (g & 1) + q;
#pragma unroll
      for (int j = 0; j < 3; ++j) boff[j] = L_B + rho * 480 + (20 * j + 5 * wn + p) * 8;
      boff[3] = L_B + rho * 480 + (20 * (p == 3 ? 0 : p) + 5 * wn + 4) * 8;
    }
  }

  f32x4 acc[MT][4];
#pragma unroll
  for (int m = 0; m < MT; ++m)
#pragma unroll
    for (int j = 0; j < 4; ++j) acc[m][j] = f32x4{0.f, 0.f, 0.f, 0.f};
  bf16x8 fa0[MT], fa1[MT];
  s16x4 fb0[8], fb1[8];

  STAMPAT(1);
  WAITN_BAR(13);
  STAMPAT(2);
  unsigned wpk[8];
  if (!GU) {
    asm volatile("" : "+v"(wraw[0]), "+v"(wraw[1]), "+v"(wraw[2]), "+v"(wraw[3]), "+v"(wraw[4]), "+v"(wraw[5]), "+v"(wraw[6]), "+v"(wraw[7]),
                 "+v"(wraw[8]), "+v"(wraw[9]), "+v"(wraw[10]), "+v"(wraw[11]), "+v"(wraw[12]), "+v"(wraw[13]), "+v"(wraw[14]), "+v"(wraw[15]));
#pragma unroll
    for (int i = 0; i < 8; ++i) {
      wpk[i] = wraw[2 * i] | (wraw[2 * i + 1] << 16);
      if (i >= 4 && (fr & 3) != 0) wpk[i] = 0u;
    }
    asm volatile("" : "+v"(wpk[0]), "+v"(wpk[1]), "+v"(wpk[2]), "+v"(wpk[3]), "+v"(wpk[4]), "+v"(wpk[5]), "+v"(wpk[6]), "+v"(wpk[7]));
  }
#pragma unroll
  for (int m = 0; m < MT; ++m) fa0[m] = ld128(sm + aoff0 + m * 2048);
#pragma unroll
  for (int j = 0; j < 8; ++j) fb0[j] = tr1(sm + boff[j >> 1] + (j & 1) * TSTEP);
  MEMFENCE();

  constexpr int NM = MT * 4;
  constexpr int DS = (MT == 6) ? 6 : 5;
#define MMA(i, FA, FB) acc[(i) >> 2][(i) & 3] = mfma32(cat(FB[2 * ((i) & 3)], FB[2 * ((i) & 3) + 1]), FA[(i) >> 2], acc[(i) >> 2][(i) & 3])
#define BADDR(sl, i) (sm + (sl) * B_SLOT + boff[(i) >> 1] + ((i) & 1) * TSTEP)
  u32x4 ackv, stv[MT];
  const char* ackp = ws + WS_ACK + bid * 64;
  const char* stp = ws + WS_STAT + (size_t)(rg * 176 + mh * 96 + fr) * 16;
#pragma unroll
  for (int t = 0; t < 10; ++t) {
    SCHED0();
    WAITN_BAR(t < 8 ? 11 : (t == 8 ? 7 : 1 + MT));
    SCHED0();
#pragma unroll
    for (int i = 0; i < NM; ++i) {
      MMA(i, fa0, fb0);
      SCHED0();
      if (i < 8) fb1[i] = tr1(BADDR((2 * t + 1) % 5, i));
      else if (i < 8 + MT) fa1[i - 8] = ld128(sm + (t % 3) * A_SLOT + aoff1 + (i - 8) * 2048);
      if (t < 8) {
        if (i == 2) { if (MT == 6) DMA_A2((t + 2) * 128, adst0 + 16384 + ((t + 2) % 3) * A_SLOT); else DMA_A2((t + 2) * 128, a2base + a2mul * (((t + 2) % 3) * A_SLOT)); }
        if (i == 2 + DS) DMA_B0((2 * t + 5) * SSTEP, bdst0 + ((2 * t) % 5) * B_SLOT);
        if (i == 2 + 2 * DS) DMA_B1((2 * t + 5) * SSTEP, bdst0 + ((2 * t) % 5) * B_SLOT + 8192);
      }
      SCHED0();
    }
    if (t < 9) WAITN_BAR(t < 8 ? 9 : 2); else LGKM_BAR();
    SCHED0();
#pragma unroll
    for (int i = 0; i < NM; ++i) {
      MMA(i, fa1, fb1);
      SCHED0();
      if (t < 9) {
        if (i < 8) fb0[i] = tr1(BADDR((2 * t + 2) % 5, i));
        else if (i < 8 + MT) fa0[i - 8] = ld128(sm + ((t + 1) % 3) * A_SLOT + aoff0 + (i - 8) * 2048);
      }
      if (t < 7) {
        if (i == 1) DMA_B0((2 * t + 6) * SSTEP, bdst0 + ((2 * t + 1) % 5) * B_SLOT);
        if (i == 1 + DS) DMA_B1((2 * t + 6) * SSTEP, bdst0 + ((2 * t + 1) % 5) * B_SLOT + 8192);
        if (i == 1 + 2 * DS) DMA_A0((t + 3) * 128, adst0 + (t % 3) * A_SLOT);
        if (i == 1 + 3 * DS) DMA_A1((t + 3) * 128, adst0 + (t % 3) * A_SLOT + 8192);
      }
      if (i == 4 && t == 8) {
        asm volatile("global_load_dwordx4 %0, %1, off sc1" : "=&v"(ackv) : "v"(ackp) : "memory");
#pragma unroll
        for (int m = 0; m < MT; ++m) asm volatile("global_load_dwordx4 %0, %1, off offset:%2 sc1" : "=&v"(stv[m]) : "v"(stp), "n"(m * 256) : "memory");
      }
      SCHED0();
    }
  }
  SCHED0();
  STAMPAT(3);

  if (fr == 15) {
#pragma unroll
    for (int j = 0; j < 4; ++j) *(LAS f32x4*)(sm + L_CS + w * 256 + j * 64 + g * 16) = acc[MT - 1][j];
  }
  if (MT == 6) asm volatile("s_waitcnt vmcnt(0) lgkmcnt(0)" : "+v"(ackv), "+v"(stv[0]), "+v"(stv[1]), "+v"(stv[2]), "+v"(stv[3]), "+v"(stv[4]), "+v"(stv[MT - 1]) :: "memory");
  else asm volatile("s_waitcnt vmcnt(0) lgkmcnt(0)" : "+v"(ackv), "+v"(stv[0]), "+v"(stv[1]), "+v"(stv[2]), "+v"(stv[3]), "+v"(stv[4]) :: "memory");
  f32x4 cs[4];
#pragma unroll
  for (int j = 0; j < 4; ++j) cs[j] = *(const LAS f32x4*)(sm + L_CS + w * 256 + j * 64 + g * 16);
  {
    const u32x2 nv = *(const LAS u32x2*)(sm + L_NONCE);
    for (unsigned spins = 0; spins < SPIN_MAX; ++spins) {
      bool ok = (ackv[0] == nv[0]) && (ackv[1] == nv[1]);
#pragma unroll
      for (int m = 0; m < MT; ++m) ok = ok && (stv[m][2] == ackv[2]) && (stv[m][3] == ackv[3]);
      if (all_ok(ok)) break;
      __builtin_amdgcn_s_sleep(4);
      ackv = ld128_sc1(ackp);
#pragma unroll
      for (int m = 0; m < MT; ++m) stv[m] = ld128_sc1(stp + m * 256);
    }
  }
  const u32x2 ep = u32x2{ackv[2], ackv[3]};
  if (w == 0 && lane == 0) st64_sc1(ws + WS_DONE + bid * 64, ep);
  if (GU) {
    const unsigned vB = p2_vB(w, lane);
    p2_issueB_down(srd_wd, vB, c0p2, w, smb, 0, rot);
    p2_issueB_down(srd_wd, vB, c0p2, w, smb, 1, rot);
    p2_issueB_down(srd_wd, vB, c0p2, w, smb, 2, rot);
  }
  float rstd[MT];
#pragma unroll
  for (int m = 0; m < MT; ++m) {
    const f32x2 nm = sp2(opq(-u2f(stv[m][0])));
    rstd[m] = opq(u2f(stv[m][1]));
#pragma unroll
    for (int j = 0; j < 4; ++j) {
      f32x2 l = fma2(nm, lo2(cs[j]), lo2(acc[m][j])), h = fma2(nm, hi2(cs[j]), hi2(acc[m][j]));
      acc[m][j] = f32x4{l[0], l[1], h[0], h[1]};
    }
  }
  STAMPAT(4);

  if (GU) {
    const float K0 = -2.0f * 0.7978845608028654f * 1.4426950408889634f;
    const float K1c = K0 * 0.044715f;
    ldsp stg = sm + w * 7680;
    char* hg = ws + WS_H + ct * 256 + wn * 64 + (lane & 3) * 16;
#pragma unroll
    for (int m = 0; m <= MT; ++m) {
      if (m < MT) {
        const float r = rstd[m], r2 = r * r;
        const f32x2 c0 = sp2(opq(K0 * r)), c1 = sp2(opq(K1c * r2 * r)), ir2 = sp2(opq(__builtin_amdgcn_rcpf(r2)));
        u32x2 hp[2];
#pragma unroll
        for (int j = 0; j < 2; ++j) {
#pragma unroll
          for (int q = 0; q < 2; ++q) {
            const f32x2 a = q ? hi2(acc[m][j]) : lo2(acc[m][j]), b = q ? hi2(acc[m][j + 2]) : lo2(acc[m][j + 2]);
            const f32x2 z = a * fma2(a * a, c1, c0);
            const f32x2 e = f32x2{__builtin_amdgcn_exp2f(z[0]), __builtin_amdgcn_exp2f(z[1])};
            const f32x2 d = fma2(e, ir2, ir2);
            const f32x2 hv = (a * b) * f32x2{__builtin_amdgcn_rcpf(d[0]), __builtin_amdgcn_rcpf(d[1])};
            hp[j][q] = pk2(hv[0], hv[1]);
          }
        }
        *(LAS u32x2*)(stg + (m * 16 + fr) * 80 + g * 8) = hp[0];
        *(LAS u32x2*)(stg + (m * 16 + fr) * 80 + 32 + g * 8) = hp[1];
      }
      if (m >= 1) {
        const int rl = (m - 1) * 16 + (lane >> 2);
        u32x4 v = *(const LAS u32x4*)(stg + rl * 80 + (lane & 3) * 16);
        const int sl = mh * 96 + rl;
        const int ri = sl < 95 ? sl : sl - 1;
        if (sl != 95 && ri < nrow) st128_sc1(hg + (size_t)(r0 + ri) * 3072, v);
      }
    }
    STAMPAT(5);
    WAITN_BAR(0);
    if (w == 0 && lane == 0) st64_sc1(ws + WS_HF + rg * 128 + ct * 8, ep);
    STAMPAT(6);
  } else {
    const int head = ct * 4 + wn;
    bf16x8 wq[2], wk[2];
#pragma unroll
    for (int ft = 0; ft < 2; ++ft) {
      wq[ft] = asb(u32x4{wpk[4 * ft], wpk[4 * ft + 1], g == 0 ? wpk[4 * ft + 2] : 0u, g == 0 ? wpk[4 * ft + 3] : 0u});
      wk[ft] = asb(u32x4{wpk[4 * ft], wpk[4 * ft + 1], g == 1 ? wpk[4 * ft + 2] : 0u, g == 1 ? wpk[4 * ft + 3] : 0u});
    }
    const unsigned tag = ep[0] ^ (ep[1] * 0x9E3779B1u);
    const float L2E = 1.4426950408889634f;
    const f32x4 z4 = f32x4{0.f, 0.f, 0.f, 0.f};
    bool live[6];
    u32x2 rem[6];
#pragma unroll
    for (int m = 0; m < 6; ++m) {
      int s = mh * 96 + m * 16 + fr;
      int ri = s < 95 ? s : s - 1;
      live[m] = (m < MT) && (s != 95) && (ri < nrow);
      if (m < MT) rem[m] = u32x2{pk2(acc[m][3][0], acc[m][3][1]), pk2(acc[m][3][2], acc[m][3][3])};
    }
    ldsp pk_img = sm + w * 17408;
    ldsp v_img = pk_img + 96 * 104;
    {
      unsigned kf[6][5];
      u32x2 vv[6], ve[6];
#pragma unroll
      for (int mp = 0; mp < 3; ++mp) {
        f32x2 y01[2], y23[2], y4;
#pragma unroll
        for (int hh = 0; hh < 2; ++hh) {
          const int m = 2 * mp + hh;
          if (m < MT) {
            const f32x2 r = sp2(rstd[m]);
            const float c = opq(rstd[m] * L2E);
            u32x4 kb = u32x4{pk2(acc[m][1][0], acc[m][1][1]), pk2(acc[m][1][2], acc[m][1][3]), rem[m][0], rem[m][1]};
            f32x4 k0 = mfma32(wk[0], asb(kb), z4);
            f32x4 k1 = mfma32(wk[1], asb(kb), z4);
            y01[hh] = lo2(k0) * sp2(c); y23[hh] = hi2(k0) * sp2(c); y4[hh] = k1[0] * c;
            vv[m] = u32x2{pk2v(lo2(acc[m][2]) * r), pk2v(hi2(acc[m][2]) * r)};
            u32x2 vr = u32x2{pk2v(lo2(acc[m][3]) * r), pk2v(hi2(acc[m][3]) * r)};
            ve[m] = (g == 2) ? vr : u32x2{0x3F803F80u, 0x3F803F80u};
          } else {
            y01[hh] = sp2(0.f); y23[hh] = sp2(0.f); y4[hh] = 0.f;
            vv[m] = u32x2{0u, 0u}; ve[m] = vv[m];
          }
        }
        featmap2(y01[0], y23[0], y01[1], y23[1], y4, live[2 * mp], live[2 * mp + 1], g, kf[2 * mp], kf[2 * mp + 1]);
      }
      SCHED0();
#pragma unroll
      for (int m = 0; m < 6; ++m) {
        int tok = m * 16 + fr;
        *(LAS u32x2*)(pk_img + tok * 104 + g * 8) = u32x2{kf[m][0], kf[m][1]};
        *(LAS u32x2*)(pk_img + tok * 104 + 32 + g * 8) = u32x2{kf[m][2], kf[m][3]};
        *(LAS u32x2*)(pk_img + tok * 104 + 64 + g * 8) = u32x2{kf[m][4], 0u};
        *(LAS u32x2*)(v_img + tok * 72 + g * 8) = vv[m];
        *(LAS u32x2*)(v_img + tok * 72 + 32 + (g ^ 2) * 8) = ve[m];
      }
      LGKM0();
      SCHED0();
    }
    f32x4 kv[3][2];
#pragma unroll
    for (int ft = 0; ft < 3; ++ft)
#pragma unroll
      for (int dt = 0; dt < 2; ++dt) kv[ft][dt] = z4;
    {
      const int q = fr >> 2, p = lane & 3;
#pragma unroll
      for (int s = 0; s < 3; ++s) {
        int row = 32 * s + 8 * g + q;
        bf16x8 a[3], b[2];
#pragma unroll
        for (int ft = 0; ft < 3; ++ft) a[ft] = tr2(pk_img + row * 104 + ft * 32 + p * 8, pk_img + (row + 4) * 104 + ft * 32 + p * 8);
#pragma unroll
        for (int dt = 0; dt < 2; ++dt) b[dt] = tr2(v_img + row * 72 + dt * 32 + p * 8, v_img + (row + 4) * 72 + dt * 32 + p * 8);
#pragma unroll
        for (int ft = 0; ft < 3; ++ft)
#pragma unroll
          for (int dt = 0; dt < 2; ++dt) kv[ft][dt] = mfma32(a[ft], b[dt], kv[ft][dt]);
      }
    }
    {
      f32x4 o5[5] = {kv[0][0], kv[1][0], kv[0][1], kv[1][1], f32x4{kv[2][0][0], kv[2][0][1], kv[2][1][0], kv[2][1][1]}};
      ldsp ex = sm + (4 + wn) * 17408;
      if (mh == 1) {
        LGKM0();
#pragma unroll
        for (int i = 0; i < 5; ++i) *(LAS f32x4*)(ex + i * 1024 + lane * 16) = o5[i];
      }
      LGKM_BAR();
      if (mh == 0) {
        unsigned pp[10];
#pragma unroll
        for (int i = 0; i < 5; ++i) {
          f32x4 e = *(const LAS f32x4*)(ex + i * 1024 + lane * 16);
          pp[2 * i] = pk2(o5[i][0] + e[0], o5[i][1] + e[1]);
          pp[2 * i + 1] = pk2(o5[i][2] + e[2], o5[i][3] + e[3]);
        }
        char* kp = ws + WS_KV + (size_t)(rg * NH + head) * 4096 + lane * 16;
        st128_sc1(kp, u32x4{pp[0], pp[1], pp[2], tag});
        st128_sc1(kp + 1024, u32x4{pp[3], pp[4], pp[5], tag});
        st128_sc1(kp + 2048, u32x4{pp[6], pp[7], pp[8], tag});
        st128_sc1(kp + 3072, u32x4{pp[9], 0u, 0u, tag});
      }
    }
    STAMPAT(5);
    unsigned qf[6][5];
#pragma unroll
    for (int mp = 0; mp < 3; ++mp) {
      f32x2 y01[2], y23[2], y4;
#pragma unroll
      for (int hh = 0; hh < 2; ++hh) {
        const int m = 2 * mp + hh;
        if (m < MT) {
          const float c = opq(rstd[m] * L2E);
          u32x4 qb = u32x4{pk2(acc[m][0][0], acc[m][0][1]), pk2(acc[m][0][2], acc[m][0][3]), rem[m][0], rem[m][1]};
          f32x4 q0 = mfma32(wq[0], asb(qb), z4);
          f32x4 q1 = mfma32(wq[1], asb(qb), z4);
          y01[hh] = lo2(q0) * sp2(c); y23[hh] = hi2(q0) * sp2(c); y4[hh] = q1[0] * c;
        } else {
          y01[hh] = sp2(0.f); y23[hh] = sp2(0.f); y4[hh] = 0.f;
        }
      }
      featmap2(y01[0], y23[0], y01[1], y23[1], y4, true, true, g, qf[2 * mp], qf[2 * mp + 1]);
    }
    STAMPAT(6);
    float ks[20];
    {
      {
        const char* p1 = ws + WS_KV + ((size_t)(mh * 6 + (lane < 6 ? lane : 0)) * NH + head) * 4096 + 3072 + 1008;
        for (unsigned spins = 0; spins < SPIN_MAX; ++spins) {
          u32x4 v = ld128_sc1(p1);
          if (all_ok(v[3] == tag)) break;
          __builtin_amdgcn_s_sleep(POLLS);
        }
      }
      u32x4 pv[6][4];
      const char* kp = ws + WS_KV + (size_t)((mh * 6) * NH + head) * 4096 + lane * 16;
      for (unsigned spins = 0; spins < SPIN_MAX; ++spins) {
#pragma unroll
        for (int c = 0; c < 6; ++c) {
          const char* p = kp + (size_t)c * NH * 4096;
          asm volatile("global_load_dwordx4 %0, %4, off sc1\n\tglobal_load_dwordx4 %1, %4, off offset:1024 sc1\n\t"
                       "global_load_dwordx4 %2, %4, off offset:2048 sc1\n\tglobal_load_dwordx4 %3, %4, off offset:3072 sc1"
                       : "=&v"(pv[c][0]), "=&v"(pv[c][1]), "=&v"(pv[c][2]), "=&v"(pv[c][3]) : "v"(p) : "memory");
        }
        asm volatile("s_waitcnt vmcnt(0)" : "+v"(pv[0][0]), "+v"(pv[0][1]), "+v"(pv[0][2]), "+v"(pv[0][3]), "+v"(pv[1][0]), "+v"(pv[1][1]), "+v"(pv[1][2]), "+v"(pv[1][3]),
                     "+v"(pv[2][0]), "+v"(pv[2][1]), "+v"(pv[2][2]), "+v"(pv[2][3]), "+v"(pv[3][0]), "+v"(pv[3][1]), "+v"(pv[3][2]), "+v"(pv[3][3]),
                     "+v"(pv[4][0]), "+v"(pv[4][1]), "+v"(pv[4][2]), "+v"(pv[4][3]), "+v"(pv[5][0]), "+v"(pv[5][1]), "+v"(pv[5][2]), "+v"(pv[5][3]) :: "memory");
        bool ok = true;
#pragma unroll
        for (int c = 0; c < 6; ++c)
#pragma unroll
          for (int i = 0; i < 4; ++i) ok = ok && (pv[c][i][3] == tag);
        if (all_ok(ok)) break;
        __builtin_amdgcn_s_sleep(2);
      }
      STAMPAT(7);
#pragma unroll
      for (int i = 0; i < 10; ++i) {
        float lo = 0.f, hi = 0.f;
#pragma unroll
        for (int c = 0; c < 6; ++c) {
          unsigned u = pv[c][i / 3][i % 3];
          lo += u2f(u << 16); hi += u2f(u & 0xffff0000u);
        }
        ks[2 * i] = lo; ks[2 * i + 1] = hi;
      }
      ldsp mine = sm + w * 17408 + 12288, other = sm + (w ^ 4) * 17408 + 12288;
      SCHED0();
#pragma unroll
      for (int i = 0; i < 5; ++i) *(LAS f32x4*)(mine + i * 1024 + lane * 16) = f32x4{ks[4 * i], ks[4 * i + 1], ks[4 * i + 2], ks[4 * i + 3]};
      LGKM_BAR();
#pragma unroll
      for (int i = 0; i < 5; ++i) {
        f32x4 e = *(const LAS f32x4*)(other + i * 1024 + lane * 16);
#pragma unroll
        for (int q = 0; q < 4; ++q) ks[4 * i + q] = (mh == 0) ? ks[4 * i + q] + e[q] : e[q] + ks[4 * i + q];
      }
    }
    const bf16x8 ka0 = asb(u32x4{pk2(ks[0], ks[1]), pk2(ks[2], ks[3]), pk2(ks[4], ks[5]), pk2(ks[6], ks[7])});
    const bf16x8 ka1 = asb(u32x4{pk2(ks[8], ks[9]), pk2(ks[10], ks[11]), pk2(ks[12], ks[13]), pk2(ks[14], ks[15])});
    const bf16x8 kb0 = asb(u32x4{pk2(ks[16], ks[17]), 0u, 0u, 0u});
    const bf16x8 kb1 = asb(u32x4{pk2(ks[18], ks[19]), 0u, 0u, 0u});
    {
      u32x2 om_[MT], or_[MT];
#pragma unroll
      for (int m = 0; m < MT; ++m) {
        bf16x8 b0 = asb(u32x4{qf[m][0], qf[m][1], qf[m][2], qf[m][3]});
        bf16x8 b1 = asb(u32x4{qf[m][4], 0u, 0u, 0u});
        f32x4 n0 = mfma32(kb0, b1, mfma32(ka0, b0, z4));
        f32x4 n1 = mfma32(kb1, b1, mfma32(ka1, b0, z4));
        float da = n1[0], db = n1[0];
        swap16(da, db);
        const f32x2 inv = sp2(__builtin_amdgcn_rcpf(db + 1e-6f));
        om_[m] = u32x2{pk2v(lo2(n0) * inv), pk2v(hi2(n0) * inv)};
        or_[m] = u32x2{pk2v(lo2(n1) * inv), pk2v(hi2(n1) * inv)};
      }
      SCHED0();
      ldsp stg = sm + w * 17408 + 8192;
#pragma unroll
      for (int m = 0; m < MT; ++m) {
        *(LAS u32x2*)(stg + (m * 16 + fr) * 32 + g * 8) = om_[m];
        if (g == 0) *(LAS u32x2*)(stg + 3072 + (m * 16 + fr) * 8) = or_[m];
      }
      LGKM0();
      const int tok0 = r0 + mh * 95;
      const int ntok = mh == 0 ? 95 : nrow - 95;
      char* om = ws + WS_OM + ((size_t)head * S_ + tok0) * 32;
      char* orr = ws + WS_OR + ((size_t)head * S_ + tok0) * 8;
      u32x4 tv[3];
      u32x2 tr[2];
#pragma unroll
      for (int i = 0; i < 3; ++i) tv[i] = *(const LAS u32x4*)(stg + i * 1024 + lane * 16);
#pragma unroll
      for (int i = 0; i < 2; ++i) tr[i] = *(const LAS u32x2*)(stg + 3072 + ((i * 64 + lane) < 96 ? (i * 64 + lane) : 0) * 8);
#pragma unroll
      for (int i = 0; i < 3; ++i)
        if (i * 32 + (lane >> 1) < ntok) st128_sc1(om + i * 1024 + lane * 16, tv[i]);
#pragma unroll
      for (int i = 0; i < 2; ++i)
        if (i * 64 + lane < ntok) st64_sc1(orr + (i * 64 + lane) * 8, tr[i]);
    }
    WAITN_BAR(0);
    if (w == 0 && lane == 0) st64_sc1(ws + WS_OF + rg * 128 + ct * 8, ep);
    STAMPAT(8);
  }
  return ep;
}

DI u32x2 service(ldsp sm, const char* x, char* ws, int sid, int w, int lane) {
  const int bid = __builtin_amdgcn_workgroup_id_x();
  const int g = lane >> 4, fr = lane & 15;
  STAMPAT(0);
  if (w == 0) {
    unsigned long long nn = __builtin_amdgcn_s_memrealtime();
    u32x2 nv = u32x2{(unsigned)nn, (unsigned)(nn >> 32)};
    if (lane == 0) {
      if (sid != 15) st64_sc1(ws + WS_REQ + bid * 64, nv);
      *(LAS u32x2*)(sm + L_NONCE) = nv;
    }
  }
  float mean[6], rstd[6];
  if (sid < 12) {
    const int r0 = rs_of(sid), nrow = rs_of(sid + 1) - r0;
    u32x4 v[6][5];
#pragma unroll
    for (int i = 0; i < 6; ++i) {
      int slot = (i * 8 + w) * 4 + g;
      int ri = slot < 95 ? slot : slot - 1;
      ri = ri < nrow ? ri : nrow - 1;
      const char* p = x + (size_t)(r0 + ri) * 1280 + fr * 16;
#pragma unroll
      for (int c = 0; c < 5; ++c) v[i][c] = *(const u32x4*)(p + c * 256);
    }
#pragma unroll
    for (int i = 0; i < 6; ++i) {
      float a = 0.f;
#pragma unroll
      for (int c = 0; c < 5; ++c)
#pragma unroll
        for (int e = 0; e < 4; ++e) a += u2f(v[i][c][e] << 16) + u2f(v[i][c][e] & 0xffff0000u);
      a += dpp<0xB1>(a); a += dpp<0x4E>(a); a += dpp<0x141>(a); a += dpp<0x140>(a);
      mean[i] = a * (1.0f / 640.0f);
      float b = 0.f;
#pragma unroll
      for (int c = 0; c < 5; ++c)
#pragma unroll
        for (int e = 0; e < 4; ++e) {
          float lo = u2f(v[i][c][e] << 16) - mean[i], hi = u2f(v[i][c][e] & 0xffff0000u) - mean[i];
          b = __builtin_fmaf(lo, lo, b);
          b = __builtin_fmaf(hi, hi, b);
        }
      b += dpp<0xB1>(b); b += dpp<0x4E>(b); b += dpp<0x141>(b); b += dpp<0x140>(b);
      rstd[i] = __builtin_amdgcn_rsqf(b * (1.0f / 640.0f) + 1e-5f);
    }
  }
  LGKM_BAR();
  const u32x2 nv = *(const LAS u32x2*)(sm + L_NONCE);
  u32x2 ep;
  if (sid == 15) {
    ep = u32x2{nv[0] ^ 0x5bd1e995u, nv[1] ^ 0x80000000u};
    if (w < 4) {
      const int cb = w * 64 + lane;
      const bool me = (cb == bid);
      for (unsigned spins = 0; spins < SPIN_MAX; ++spins) {
        u32x2 rq = ld64_sc1(ws + WS_REQ + cb * 64);
        st128_sc1(ws + WS_ACK + cb * 64, u32x4{rq[0], rq[1], ep[0], ep[1]});
        u32x2 dn = ld64_sc1(ws + WS_DONE + cb * 64);
        if (all_ok(me || (dn[0] == ep[0] && dn[1] == ep[1]))) break;
        __builtin_amdgcn_s_sleep(16);
      }
    }
  } else {
    u32x4 ack;
    for (unsigned spins = 0; spins < SPIN_MAX; ++spins) {
      ack = ld128_sc1(ws + WS_ACK + bid * 64);
      if (all_ok(ack[0] == nv[0] && ack[1] == nv[1])) break;
      __builtin_amdgcn_s_sleep(4);
    }
    ep = u32x2{ack[2], ack[3]};
    if (w == 0 && lane == 0) st64_sc1(ws + WS_DONE + bid * 64, ep);
    if (sid < 12) {
#pragma unroll
      for (int i = 0; i < 6; ++i) {
        int slot = (i * 8 + w) * 4 + g;
        if (fr == 0 && slot < 176) st128_sc1(ws + WS_STAT + (size_t)(sid * 176 + slot) * 16, u32x4{f2u(mean[i]), f2u(rstd[i]), ep[0], ep[1]});
      }
    }
  }
  STAMPAT(1);
  return ep;
}

__global__ __launch_bounds__(512, 2) void fused(const char* __restrict__ x, const char* __restrict__ wqkv,
                                                const char* __restrict__ wphi, const char* __restrict__ wo,
                                                const char* __restrict__ wg, const char* __restrict__ wu,
                                                const char* __restrict__ wd, char* __restrict__ ws, char* __restrict__ out) {
  extern __shared__ __attribute__((aligned(16))) char smem[];
  ldsp sm = (ldsp)smem;
  const int tid = __builtin_amdgcn_workitem_id_x();
  const int bp = __builtin_amdgcn_workgroup_id_x(), xcd = bp & 7, ip = bp >> 3;
  const int idx = ip < 2 ? 30 + ip : (ip < 14 ? ((ip - 2) >> 1) * 5 + 3 + ((ip - 2) & 1) : ((ip - 14) / 3) * 5 + (ip - 14) % 3);
  const int b = idx * 8 + xcd;
  const int w = __builtin_amdgcn_readfirstlane(tid >> 6), lane = tid & 63;
  const u32x4 srd_wd = make_srd(wd, DF_ * D_ * 2);
  const int rgl = idx / 5, c5 = idx % 5;
  const int rg = (xcd >> 2) * 6 + rgl, cq = xcd & 3;
  if (idx < 30 && c5 >= 3) {
    const int ct = cq * 2 + (c5 - 3);
    if (w < 4) phase1<6, false>(sm, x, wqkv, wphi, wg, wu, srd_wd, ws, rg, ct, 0, 0, w, lane);
    else phase1<5, false>(sm, x, wqkv, wphi, wg, wu, srd_wd, ws, rg, ct, 0, 0, w, lane);
    return;
  }
  const int p2i = idx < 30 ? rgl * 3 + c5 : 18 + (idx - 30);
  const int rt = (xcd >> 1) * 4 + p2i / 5, ct2 = (xcd & 1) * 5 + p2i % 5;
#define ROTF ((((p2i % 5) * 12) / 5 + (p2i / 5) * 3) % 12)
  const int rot = ROTF;
  u32x2 ep;
  if (idx < 30) {
    const int ct = cq * 3 + c5;
    if (w < 4) ep = phase1<6, true>(sm, x, wqkv, wphi, wg, wu, srd_wd, ws, rg, ct, ct2 * 64, rot, w, lane);
    else ep = phase1<5, true>(sm, x, wqkv, wphi, wg, wu, srd_wd, ws, rg, ct, ct2 * 64, rot, w, lane);
  } else {
    ep = service(sm, x, ws, b - 240, w, lane);
    const unsigned vB = p2_vB(w, lane);
    const unsigned smb = (unsigned)(size_t)sm;
    p2_issueB_down(srd_wd, vB, ct2 * 64, w, smb, 0, rot);
    p2_issueB_down(srd_wd, vB, ct2 * 64, w, smb, 1, rot);
    p2_issueB_down(srd_wd, vB, ct2 * 64, w, smb, 2, rot);
  }
  phase2(sm, x, wo, srd_wd, ws, out, rt, ct2, rot, w, lane, ep);
}

#if defined(__HIP_DEVICE_COMPILE__)

#endif

extern "C" void kernel_launch(void* const* d_in, const int* in_sizes, int n_in, void* d_out, int out_size, void* d_ws,
                              size_t ws_size, hipStream_t stream) {
  (void)hipFuncSetAttribute((const void*)fused, hipFuncAttributeMaxDynamicSharedMemorySize, K_LDS);
  fused<<<256, 512, K_LDS, stream>>>((const char*)d_in[0], (const char*)d_in[1], (const char*)d_in[2], (const char*)d_in[3],
                                     (const char*)d_in[5], (const char*)d_in[6], (const char*)d_in[7], (char*)d_ws, (char*)d_out);
}
```

```cpp
#define ORDER 2
#define POLLS 6
#include <hip/hip_runtime.h>
#include <hip/hip_bf16.h>
#include <stdint.h>

typedef __attribute__((ext_vector_type(8))) __bf16 bf16x8;
typedef __attribute__((ext_vector_type(2))) __bf16 bf16x2;
typedef __attribute__((ext_vector_type(4))) short s16x4;
typedef __attribute__((ext_vector_type(8))) short s16x8;
typedef __attribute__((ext_vector_type(4))) float f32x4;
typedef __attribute__((ext_vector_type(2))) float f32x2;
typedef __attribute__((ext_vector_type(4))) unsigned u32x4;
typedef __attribute__((ext_vector_type(2))) unsigned u32x2;
#define LAS __attribute__((address_space(3)))
#define DI __device__ __forceinline__
typedef LAS char* ldsp;

constexpr int S_ = 2048, D_ = 640, DF_ = 1536, NH = 32;
constexpr int NRG = 12;
constexpr size_t WS_H = 0;
constexpr size_t WS_KV = WS_H + (size_t)S_ * DF_ * 2;
constexpr size_t WS_OM = WS_KV + (size_t)NRG * NH * 4096;
constexpr size_t WS_OR = WS_OM + (size_t)NH * S_ * 32;
constexpr size_t WS_REQ = WS_OR + (size_t)NH * S_ * 8;
constexpr size_t WS_ACK = WS_REQ + 256 * 64;
constexpr size_t WS_DONE = WS_ACK + 256 * 64;
constexpr size_t WS_STAT = WS_DONE + 256 * 64;
constexpr size_t WS_HF = WS_STAT + 2048 * 16;
constexpr size_t WS_KF = WS_HF + 12 * 128;
constexpr size_t WS_OF = WS_KF + 8 * 128;
constexpr size_t WS_DBG = 64u << 20;

constexpr int A_SLOT = 22528, B_SLOT = 16384;
constexpr int L_A = 0;
constexpr int L_B = 3 * A_SLOT;
constexpr int L_MISC = L_B + 5 * B_SLOT;
constexpr int L_DUMMY = L_MISC;
constexpr int L_NONCE = L_MISC + 1024;
constexpr int L_CS = L_NONCE + 64;
constexpr int K_LDS = L_CS + 2048;
constexpr int P2_A = 0, P2_B = 98304;
constexpr unsigned SPIN_MAX = 1u << 20;

#if defined(__HIP_DEVICE_COMPILE__)

#endif

#define STAMPAT(i)

DI float u2f(unsigned u) { return __builtin_bit_cast(float, u); }
DI unsigned f2u(float f) { return __builtin_bit_cast(unsigned, f); }
DI u32x4 make_srd(const void* p, unsigned bytes) {
  uint64_t a = (uint64_t)p;
  u32x4 r;
  r[0] = (unsigned)a;
  r[1] = (unsigned)(a >> 32) & 0xffffu;
  r[2] = bytes;
  r[3] = 0x00020000u;
  return r;
}
DI void dma_b(u32x4 srd, unsigned voff, unsigned soff, unsigned m0v) {
  asm volatile("s_mov_b32 m0, %3\n\ts_nop 0\n\tbuffer_load_dwordx4 %0, %1, %2 offen lds" ::"v"(voff), "s"(srd), "s"(soff), "s"(m0v) : "memory");
}
DI void dma_b_mask(u32x4 srd, unsigned voff, unsigned soff, unsigned m0v, unsigned long long mask) {
  asm volatile("s_mov_b32 m0, %3\n\ts_mov_b64 exec, %4\n\tbuffer_load_dwordx4 %0, %1, %2 offen lds\n\ts_mov_b64 exec, -1" ::"v"(voff), "s"(srd), "s"(soff), "s"(m0v), "s"(mask) : "memory");
}
DI void dma_b_sc1(u32x4 srd, unsigned voff, unsigned soff, unsigned m0v) {
  asm volatile("s_mov_b32 m0, %3\n\ts_nop 0\n\tbuffer_load_dwordx4 %0, %1, %2 offen sc1 lds" ::"v"(voff), "s"(srd), "s"(soff), "s"(m0v) : "memory");
}
DI u32x4 ld128_sc1(const char* p) { u32x4 v; asm volatile("global_load_dwordx4 %0, %1, off sc1\n\ts_waitcnt vmcnt(0)" : "=&v"(v) : "v"(p) : "memory"); return v; }
DI u32x2 ld64_sc1(const char* p) { u32x2 v; asm volatile("global_load_dwordx2 %0, %1, off sc1\n\ts_waitcnt vmcnt(0)" : "=&v"(v) : "v"(p) : "memory"); return v; }
DI void st128_sc1(char* p, u32x4 v) { asm volatile("global_store_dwordx4 %0, %1, off sc1\n\ts_nop 1" ::"v"(p), "v"(v) : "memory"); }
DI void st64_sc1(char* p, u32x2 v) { asm volatile("global_store_dwordx2 %0, %1, off sc1" ::"v"(p), "v"(v) : "memory"); }
#define WAITN_BAR(N) asm volatile("s_waitcnt vmcnt(%0) lgkmcnt(0)\n\ts_barrier" ::"n"(N) : "memory")
#define LGKM_BAR() asm volatile("s_waitcnt lgkmcnt(0)\n\ts_barrier" ::: "memory")
#define LGKM0() asm volatile("s_waitcnt lgkmcnt(0)" ::: "memory")
#define MEMFENCE() asm volatile("" ::: "memory")
#define SCHED0() __builtin_amdgcn_sched_barrier(0)

DI bf16x8 ld128(ldsp p) { return *(const LAS bf16x8*)p; }
DI s16x4 tr1(ldsp p) { return __builtin_amdgcn_ds_read_tr16_b64_v4i16((LAS s16x4*)p); }
DI bf16x8 cat(s16x4 a, s16x4 b) { return __builtin_bit_cast(bf16x8, __builtin_shufflevector(a, b, 0, 1, 2, 3, 4, 5, 6, 7)); }
DI bf16x8 tr2(ldsp p0, ldsp p1) { return cat(tr1(p0), tr1(p1)); }
DI f32x4 mfma32(bf16x8 a, bf16x8 b, f32x4 c) { return __builtin_amdgcn_mfma_f32_16x16x32_bf16(a, b, c, 0, 0, 0); }
DI bf16x8 asb(u32x4 v) { return __builtin_bit_cast(bf16x8, v); }
DI unsigned pk2(float a, float b) {
  bf16x2 v;
  v[0] = (__bf16)a;
  v[1] = (__bf16)b;
  return __builtin_bit_cast(unsigned, v);
}
DI void swap16(float& a, float& b) {
  auto r = __builtin_amdgcn_permlane16_swap(f2u(a), f2u(b), false, false);
  a = u2f(r[0]); b = u2f(r[1]);
}
DI void swap32(float& a, float& b) {
  auto r = __builtin_amdgcn_permlane32_swap(f2u(a), f2u(b), false, false);
  a = u2f(r[0]); b = u2f(r[1]);
}
template <int CTRL> DI float dpp(float x) { return u2f(__builtin_amdgcn_update_dpp(0u, f2u(x), CTRL, 0xf, 0xf, false)); }
DI float fmx(float a, float b) { return __builtin_fmaxf(a, b); }
DI float fmn(float a, float b) { return __builtin_fminf(a, b); }
DI float rawmax(float a, float b) { float r; asm("v_max_f32 %0, %1, %2" : "=v"(r) : "v"(a), "v"(b)); return r; }
DI float opq(float x) { asm("" : "+v"(x)); return x; }
DI f32x2 lo2(f32x4 v) { return __builtin_shufflevector(v, v, 0, 1); }
DI f32x2 hi2(f32x4 v) { return __builtin_shufflevector(v, v, 2, 3); }
DI f32x2 sp2(float x) { return f32x2{x, x}; }
DI f32x2 fma2(f32x2 a, f32x2 b, f32x2 c) { return __builtin_elementwise_fma(a, b, c); }
DI int strow(int s, int nrow) { int ri = s < 95 ? s : s - 1; return ri < nrow ? ri : nrow - 1; }
DI int rs_of(int rg) { return (512 * rg) / 3; }
DI int rg_of(int row) { return (3 * row + 2) >> 9; }
DI bool all_ok(bool ok) { return __builtin_amdgcn_ballot_w64(ok) == ~0ull; }

DI void wait_flags(const char* p, bool need, u32x2 ep) {
  for (unsigned spins = 0; spins < SPIN_MAX; ++spins) {
    u32x2 v = ld64_sc1(p);
    if (all_ok(!need || (v[0] == ep[0] && v[1] == ep[1]))) break;
    __builtin_amdgcn_s_sleep(POLLS);
  }
}

DI f32x2 ex2(f32x2 v) { return f32x2{__builtin_amdgcn_exp2f(v[0]), __builtin_amdgcn_exp2f(v[1])}; }
DI unsigned pk2v(f32x2 v) { return pk2(v[0], v[1]); }
DI void featmap2(f32x2 a01, f32x2 a23, f32x2 b01, f32x2 b23, f32x2 e4, bool livea, bool liveb, int g, unsigned (&oa)[5], unsigned (&ob)[5]) {
  float a0 = fmx(fmx(fmx(a01[0], a01[1]), a23[0]), fmx(a23[1], e4[0]));
  float a1 = -fmn(fmn(fmn(a01[0], a01[1]), a23[0]), fmn(a23[1], e4[0]));
  float b0 = fmx(fmx(fmx(b01[0], b01[1]), b23[0]), fmx(b23[1], e4[1]));
  float b1 = -fmn(fmn(fmn(b01[0], b01[1]), b23[0]), fmn(b23[1], e4[1]));
  swap16(a0, a1); swap16(b0, b1);
  float ca = rawmax(a0, a1), cb = rawmax(b0, b1);
  swap32(ca, cb);
  float d0 = rawmax(ca, cb), d1 = d0;
  swap32(d0, d1);
  float Ma = d0, Na = d0, Mb = d1, Nb = d1;
  swap16(Ma, Na); swap16(Mb, Nb);
  const f32x2 pa01 = ex2(a01 - sp2(Ma)), pa23 = ex2(a23 - sp2(Ma)), na01 = ex2(-a01 - sp2(Na)), na23 = ex2(-a23 - sp2(Na));
  const f32x2 pb01 = ex2(b01 - sp2(Mb)), pb23 = ex2(b23 - sp2(Mb)), nb01 = ex2(-b01 - sp2(Nb)), nb23 = ex2(-b23 - sp2(Nb));
  const float pa4 = __builtin_amdgcn_exp2f(e4[0] - Ma), na4 = __builtin_amdgcn_exp2f(-e4[0] - Na);
  const float pb4 = __builtin_amdgcn_exp2f(e4[1] - Mb), nb4 = __builtin_amdgcn_exp2f(-e4[1] - Nb);
  f32x2 t;
  t = pa01 + pa23; a0 = (t[0] + t[1]) + pa4;
  t = na01 + na23; a1 = (t[0] + t[1]) + na4;
  t = pb01 + pb23; b0 = (t[0] + t[1]) + pb4;
  t = nb01 + nb23; b1 = (t[0] + t[1]) + nb4;
  swap16(a0, a1); swap16(b0, b1);
  ca = a0 + a1; cb = b0 + b1;
  swap32(ca, cb);
  d0 = __builtin_amdgcn_rcpf(ca + cb);
  if (!(g < 2 ? livea : liveb)) d0 = 0.f;
  d1 = d0;
  swap32(d0, d1);
  Ma = d0; Na = d0; Mb = d1; Nb = d1;
  swap16(Ma, Na); swap16(Mb, Nb);
  oa[0] = pk2v(pa01 * sp2(Ma)); oa[1] = pk2v(pa23 * sp2(Ma));
  oa[2] = pk2v(na01 * sp2(Na)); oa[3] = pk2v(na23 * sp2(Na));
  oa[4] = pk2(pa4 * Ma, na4 * Na);
  ob[0] = pk2v(pb01 * sp2(Mb)); ob[1] = pk2v(pb23 * sp2(Mb));
  ob[2] = pk2v(nb01 * sp2(Nb)); ob[3] = pk2v(nb23 * sp2(Nb));
  ob[4] = pk2(pb4 * Mb, nb4 * Nb);
}

struct P2Ctx { unsigned vB, vR; int c0; };
DI unsigned p2_vB(int w, int lane) {
  const int l3 = lane >> 3, l7 = lane & 7;
  int fb = ((l3 >> 1) & 1) | ((w & 1) << 1);
  int ch = (((l7 >> 1) ^ fb) << 1) | (l7 & 1);
  return l3 * 1280 + ch * 16;
}
DI int p2_rot(int s, int rot) { int r = s + rot; return r >= 12 ? r - 12 : r; }
DI void p2_issueB_down(u32x4 srd_wd, unsigned vB, int c0, int w, unsigned smb, int s, int rot) {
  const int sr = p2_rot(s, rot);
  dma_b(srd_wd, vB, (128 * sr + 8 * w) * 1280 + c0 * 2, smb + P2_B + (s % 3) * 16384 + w * 1024);
  dma_b(srd_wd, vB, (128 * sr + 8 * w + 64) * 1280 + c0 * 2, smb + P2_B + (s % 3) * 16384 + w * 1024 + 8192);
}

DI void phase2(ldsp sm, const char* x, const char* wo, const u32x4 srd_wd, char* ws, char* out, int rt, int ct, int rot, int w, int lane, u32x2 ep) {
  const int r0 = rt * 128, c0 = ct * 64;
  const int g = lane >> 4, fr = lane & 15, l3 = lane >> 3, l7 = lane & 7;
  const int kh = w & 1, nh = (w >> 1) & 1, mh = w >> 2;
  const unsigned smb = (unsigned)(size_t)sm;
  const u32x4 srd_h = make_srd(ws + WS_H, S_ * DF_ * 2);
  const u32x4 srd_wo = make_srd(wo, D_ * D_ * 2);
  const u32x4 srd_om = make_srd(ws + WS_OM, NH * S_ * 32);
  const u32x4 srd_or = make_srd(ws + WS_OR, NH * S_ * 8);
  const unsigned vB = p2_vB(w, lane);
  const unsigned vA = l3 * 3072 + ((l7 ^ ((4 * (w & 1) + (l3 >> 1)) & 7)) * 16);
  const unsigned vR = vB - l3 * 1280 + (20 * (lane >> 5) + 16 + (l3 & 3)) * 1280;
  const unsigned vL = lane * 16;
  const int rgl = rg_of(r0), rgh = rg_of(r0 + 127);

#define P2_PIECE(s, p) do { \
    constexpr int i_ = (p) < 2 ? (p) : (p) - 2; \
    if ((s) < 12) { \
      if ((p) < 2) dma_b(srd_wd, vB, (128 * p2_rot((s), rot) + 8 * w + 64 * i_) * 1280 + c0 * 2, smb + P2_B + ((s) % 3) * 16384 + i_ * 8192 + w * 1024); \
      else dma_b_sc1(srd_h, vA, (r0 + 8 * w + 64 * (i_ & 1)) * 3072 + p2_rot((s), rot) * 256 + (i_ >> 1) * 128, smb + P2_A + ((s) % 3) * 32768 + (i_ >> 1) * 16384 + (i_ & 1) * 8192 + w * 1024); \
    } else if ((s) < 16) { \
      if ((p) < 2) dma_b(srd_wo, vB, (160 * ((s) - 12) + 20 * ((w + 8 * i_) >> 1) + 8 * (w & 1)) * 1280 + c0 * 2, smb + P2_B + ((s) % 3) * 16384 + i_ * 8192 + w * 1024); \
      else dma_b_sc1(srd_om, vL, ((8 * ((s) - 12) + 2 * i_ + (w >> 2)) * S_ + r0) * 32 + (w & 3) * 1024, smb + P2_A + ((s) % 3) * 32768 + i_ * 8192 + w * 1024); \
    } else { \
      if ((p) < 2) dma_b(srd_wo, vR, (40 * (w + 8 * i_)) * 1280 + c0 * 2, smb + P2_B + ((s) % 3) * 16384 + i_ * 8192 + w * 1024); \
      else dma_b_sc1(srd_or, vL, ((8 * i_ + w) * S_ + r0) * 8, smb + P2_A + ((s) % 3) * 32768 + i_ * 8192 + w * 1024); \
    } } while (0)

  const int aD = P2_A + kh * 16384 + (64 * mh + fr) * 128 + ((g ^ ((fr >> 1) & 7)) * 16);
  const int aM = P2_A + (4 * kh + (g >> 1)) * 4096 + (64 * mh + fr) * 32 + (g & 1) * 16;
  const int aR = P2_A + (16 * kh + 2 * g) * 1024 + (64 * mh + fr) * 8;
  int bO[2];
  {
    const int q = fr >> 2, p = lane & 3;
    const int f = ((q >> 1) & 1) | ((g & 1) << 1);
#pragma unroll
    for (int j = 0; j < 2; ++j) bO[j] = P2_B + (64 * kh + 8 * g + q) * 128 + (((2 * nh + j) ^ f) * 32) + p * 8;
  }
  f32x4 acc[4][2];
#pragma unroll
  for (int m = 0; m < 4; ++m)
#pragma unroll
    for (int j = 0; j < 2; ++j) acc[m][j] = f32x4{0.f, 0.f, 0.f, 0.f};

  STAMPAT(8);
  if (w == 0) wait_flags(ws + WS_HF + (size_t)(lane < 16 ? rgl : rgh) * 128 + (lane & 15) * 8, lane < 32 && (lane & 15) < 12, ep);
  WAITN_BAR(0);
  STAMPAT(9);
#define P2_A3(s) do { P2_PIECE(s, 2); P2_PIECE(s, 3); P2_PIECE(s, 4); P2_PIECE(s, 5); } while (0)
  P2_A3(0); P2_A3(1); P2_A3(2);
  u32x4 xr[2];
  bf16x8 fa[2][8];
  s16x4 fb[2][8];
#define P2_RD(s, i) do { \
    if ((i) < 8) fb[(s) & 1][i] = tr1(sm + ((s) % 3) * 16384 + bO[((i) >> 1) & 1] + ((i) >> 2) * 4096 + ((i) & 1) * 512); \
    else if ((i) < 16) { \
      constexpr int sl_ = ((i) - 8) >> 2, m_ = ((i) - 8) & 3; \
      if ((s) < 12) fa[(s) & 1][((i) - 8) & 7] = ld128(sm + ((s) % 3) * 32768 + (sl_ ? (aD ^ 64) : aD) + m_ * 2048); \
      else if ((s) < 16) fa[(s) & 1][((i) - 8) & 7] = ld128(sm + ((s) % 3) * 32768 + aM + sl_ * 8192 + m_ * 512); \
      else fa[(s) & 1][((i) - 8) & 7] = cat(*(const LAS s16x4*)(sm + ((s) % 3) * 32768 + aR + sl_ * 8192 + m_ * 128), *(const LAS s16x4*)(sm + ((s) % 3) * 32768 + aR + sl_ * 8192 + 1024 + m_ * 128)); } } while (0)
#define P2_MM(s, i) acc[((i) >> 1) & 3][(i) & 1] = mfma32(cat(fb[(s) & 1][((i) >> 3) * 4 + 2 * ((i) & 1)], fb[(s) & 1][((i) >> 3) * 4 + 2 * ((i) & 1) + 1]), fa[(s) & 1][((i) >> 3) * 4 + (((i) >> 1) & 3)], acc[((i) >> 1) & 3][(i) & 1])
#define P2_GROUP(s, q) do { \
    if ((s) >= 1 && (s) + 2 < 17) { \
      if ((s) + 2 == 12 && (q) == 2) { \
        STAMPAT(10); \
        if (w == 0) wait_flags(ws + WS_OF + (size_t)(lane < 16 ? rgl : rgh) * 128 + (lane & 15) * 8, lane < 32 && (lane & 15) < 8, ep); \
        asm volatile("s_barrier" ::: "memory"); \
        STAMPAT(11); \
      } \
      P2_PIECE((s) + 2, q); } \
    SCHED0(); \
    if ((s) < 17) { P2_RD(s, 3 * (q)); P2_RD(s, 3 * (q) + 1); P2_RD(s, 3 * (q) + 2); } \
    SCHED0(); \
    if ((s) >= 1) { P2_MM((s) - 1, ((q) * 8) / 3); P2_MM((s) - 1, ((q) * 8) / 3 + 1); if (((q) * 8) / 3 + 2 < (((q) + 1) * 8) / 3) P2_MM((s) - 1, ((q) * 8) / 3 + 2); } \
    SCHED0(); } while (0)
#define P2_STAGE(s) do { \
    SCHED0(); \
    if ((s) < 17) WAITN_BAR((s) == 0 ? 8 : ((s) == 1 ? 4 : ((s) < 16 ? 6 : 0))); \
    SCHED0(); \
    P2_GROUP(s, 0); P2_GROUP(s, 1); P2_GROUP(s, 2); P2_GROUP(s, 3); P2_GROUP(s, 4); P2_GROUP(s, 5); } while (0)
  P2_STAGE(0); P2_STAGE(1); P2_STAGE(2); P2_STAGE(3); P2_STAGE(4); P2_STAGE(5); P2_STAGE(6); P2_STAGE(7); P2_STAGE(8);
  P2_STAGE(9); P2_STAGE(10); P2_STAGE(11); P2_STAGE(12); P2_STAGE(13); P2_STAGE(14); P2_STAGE(15);
  const int orow = 64 * mh + 32 * kh + (lane >> 2), ocol = c0 * 2 + nh * 64 + (lane & 3) * 16;
#pragma unroll
  for (int i = 0; i < 2; ++i)
    asm volatile("global_load_dwordx4 %0, %1, off" : "=&v"(xr[i]) : "v"(x + (size_t)(r0 + orow + 16 * i) * 1280 + ocol) : "memory");
  P2_STAGE(16); P2_STAGE(17);
  SCHED0();
  STAMPAT(12);
  f32x4 keep[2][2];
  {
    ldsp ex = sm + w * 4096 + lane * 16;
#pragma unroll
    for (int i = 0; i < 2; ++i)
#pragma unroll
      for (int j = 0; j < 2; ++j) {
        f32x4 snd;
#pragma unroll
        for (int e = 0; e < 4; ++e) {
          const float lo_ = acc[i][j][e], hi_ = acc[2 + i][j][e];
          snd[e] = kh ? lo_ : hi_;
          keep[i][j][e] = kh ? hi_ : lo_;
        }
        *(LAS f32x4*)(ex + (i * 2 + j) * 1024) = snd;
      }
  }
  LGKM_BAR();
  {
    ldsp ex = sm + (w ^ 1) * 4096 + lane * 16;
    ldsp tp = sm + 65536 + w * 4608;
#pragma unroll
    for (int i = 0; i < 2; ++i)
#pragma unroll
      for (int j = 0; j < 2; ++j) {
        const f32x4 e = *(const LAS f32x4*)(ex + (i * 2 + j) * 1024);
        *(LAS f32x4*)(tp + (i * 16 + fr) * 144 + j * 64 + g * 16) = keep[i][j] + e;
      }
    asm volatile("" : "+v"(xr[0]), "+v"(xr[1]));
#pragma unroll
    for (int i = 0; i < 2; ++i) {
      const f32x4 s0 = *(const LAS f32x4*)(tp + (i * 16 + (lane >> 2)) * 144 + (lane & 3) * 32);
      const f32x4 s1 = *(const LAS f32x4*)(tp + (i * 16 + (lane >> 2)) * 144 + (lane & 3) * 32 + 16);
      u32x4 o;
#pragma unroll
      for (int e = 0; e < 2; ++e) {
        o[e] = pk2(s0[2 * e] + u2f(xr[i][e] << 16), s0[2 * e + 1] + u2f(xr[i][e] & 0xffff0000u));
        o[2 + e] = pk2(s1[2 * e] + u2f(xr[i][2 + e] << 16), s1[2 * e + 1] + u2f(xr[i][2 + e] & 0xffff0000u));
      }
      *(u32x4*)(out + (size_t)(r0 + orow + 16 * i) * 1280 + ocol) = o;
    }
  }
}

template <int MT, bool GU>
DI u32x2 phase1(ldsp sm, const char* x, const char* wqkv, const char* wphi, const char* wg, const char* wu, const u32x4 srd_wd,
                char* ws, int rg, int ct, int c0p2, int rot, int w, int lane) {
  STAMPAT(0);
  constexpr int mh = (MT == 6) ? 0 : 1;
  const int wn = w & 3;
  const int r0 = rs_of(rg), nrow = rs_of(rg + 1) - r0;
  const unsigned smb = (unsigned)(size_t)sm;
  const int g = lane >> 4, fr = lane & 15;

  const u32x4 srdx = make_srd(x, S_ * D_ * 2);
  unsigned voffA[3];
#pragma unroll
  for (int c = 0; c < 3; ++c) {
    int slot = 8 * (w + 8 * c) + (lane >> 3);
    int ch = (lane & 7) ^ ((slot >> 1) & 7);
    int ri = slot < 95 ? slot : slot - 1;
    ri = ri < nrow ? ri : nrow - 1;
    voffA[c] = (unsigned)((r0 + ri) * 1280 + ch * 16);
  }
  if (w >= 6) voffA[2] = 0x40000000u;
  const unsigned long long FULL = ~0ull, PART = 0x00FFFFFFFFFFFFFFull;
  const unsigned long long mask1 = (w == 3) ? PART : FULL, mask2 = (w == 5) ? PART : FULL;
  const unsigned adst0 = smb + L_A + w * 1024;
  u32x4 srd0, srd1;
  unsigned voff0, voff1;
  constexpr unsigned SSTEP = GU ? 32 * 3072 : 32 * 3840;
  if (GU) {
    srd0 = make_srd(wg, D_ * DF_ * 2);
    srd1 = make_srd(wu, D_ * DF_ * 2);
    int krow = 4 * w + (lane >> 4);
    int chp = lane & 15;
    int f = (krow & 3) | (((krow >> 3) & 1) << 2);
    int ch = (((chp >> 1) ^ f) << 1) | (chp & 1);
    voff0 = voff1 = krow * 3072 + ct * 256 + ch * 16;
  } else {
    srd0 = srd1 = make_srd(wqkv, D_ * 3 * D_ * 2);
    unsigned vo[2];
#pragma unroll
    for (int c = 0; c < 2; ++c) {
      int cidx = (w + 8 * c) * 64 + lane;
      int rho = cidx / 30, cc = cidx % 30;
      int seg = cc / 10, ch = cc % 10;
      int k = (rho & 16) | ((rho & 4) << 1) | ((rho & 8) >> 1) | (rho & 3);
      vo[c] = (cidx < 960) ? (unsigned)(k * 3840 + seg * 1280 + ct * 160 + ch * 16) : 0x40000000u;
    }
    voff0 = vo[0];
    voff1 = vo[1];
  }
  const unsigned bdst0 = smb + L_B + w * 1024;
#define KROT (((rg%6)*5/3+ct*2)%10)
  const unsigned krot = KROT;
#define RA(so) ((so) + krot * 128u >= 1280u ? (so) + krot * 128u - 1280u : (so) + krot * 128u)
#define RB(so) ((so) + krot * 2u * SSTEP >= 20u * SSTEP ? (so) + krot * 2u * SSTEP - 20u * SSTEP : (so) + krot * 2u * SSTEP)
#define DMA_B0(soff, dst) dma_b(srd0, voff0, RB(soff), (dst))
#define DMA_B1(soff, dst) dma_b(srd1, voff1, RB(soff), (dst))
#define DMA_A0(soff, dst) dma_b(srdx, voffA[0], RA(soff), (dst))
#define DMA_A1(soff, dst) dma_b_mask(srdx, voffA[1], RA(soff), (dst), mask1)
#define DMA_A2(soff, dst) dma_b_mask(srdx, voffA[2], RA(soff), (dst), mask2)
  const unsigned a2base = (w >= 6) ? smb + L_DUMMY : adst0 + 16384;
  const unsigned a2mul = (w >= 6) ? 0u : 1u;
  unsigned wraw[16];
  if (!GU) {
    const char* wp = wphi + (ct * 4 + wn) * 800;
    const char* p0 = wp + (5 * (fr >> 2) + (fr & 3)) * 2;
    const char* p1 = wp + (5 * (fr >> 2) + 4) * 2;
    const char* q0 = p0 + g * 160;
    const char* q1 = p1 + g * 160;
#pragma unroll
    for (int j = 0; j < 4; ++j) {
      asm volatile("global_load_ushort %0, %1, off offset:%2" : "=&v"(wraw[j]) : "v"(q0), "n"(j * 40) : "memory");
      asm volatile("global_load_ushort %0, %1, off offset:%2" : "=&v"(wraw[4 + j]) : "v"(p0), "n"(640 + j * 40) : "memory");
      asm volatile("global_load_ushort %0, %1, off offset:%2" : "=&v"(wraw[8 + j]) : "v"(q1), "n"(j * 40) : "memory");
      asm volatile("global_load_ushort %0, %1, off offset:%2" : "=&v"(wraw[12 + j]) : "v"(p1), "n"(640 + j * 40) : "memory");
    }
  }
  DMA_B0(0, bdst0); DMA_B1(0, bdst0 + 8192);
  DMA_A0(0, adst0); DMA_A1(0, adst0 + 8192);
  DMA_A2(0, a2base);
  DMA_B0(SSTEP, bdst0 + B_SLOT); DMA_B1(SSTEP, bdst0 + B_SLOT + 8192);
  DMA_B0(2 * SSTEP, bdst0 + 2 * B_SLOT); DMA_B1(2 * SSTEP, bdst0 + 2 * B_SLOT + 8192);
  DMA_A0(128, adst0 + A_SLOT); DMA_A1(128, adst0 + A_SLOT + 8192);
  DMA_A2(128, a2base + a2mul * A_SLOT);
  DMA_B0(3 * SSTEP, bdst0 + 3 * B_SLOT); DMA_B1(3 * SSTEP, bdst0 + 3 * B_SLOT + 8192);
  DMA_B0(4 * SSTEP, bdst0 + 4 * B_SLOT); DMA_B1(4 * SSTEP, bdst0 + 4 * B_SLOT + 8192);
  DMA_A0(256, adst0 + 2 * A_SLOT); DMA_A1(256, adst0 + 2 * A_SLOT + 8192);
  if (w == 0 && lane < 48) {
    int rr = lane >> 3;
    *(LAS u32x4*)(sm + L_A + (rr >> 1) * A_SLOT + ((rr & 1) ? 175 : 95) * 128 + (lane & 7) * 16) =
        u32x4{0x3F803F80u, 0x3F803F80u, 0x3F803F80u, 0x3F803F80u};
  }
  const int bid = __builtin_amdgcn_workgroup_id_x();
  if (w == 1) {
    unsigned long long nn = __builtin_amdgcn_s_memrealtime();
    u32x2 nv = u32x2{(unsigned)nn, (unsigned)(nn >> 32)};
    if (lane == 0) {
      st64_sc1(ws + WS_REQ + bid * 64, nv);
      *(LAS u32x2*)(sm + L_NONCE) = nv;
    }
  }

  const int aoff0 = L_A + mh * 6 * 2048 + fr * 128 + ((g ^ ((fr >> 1) & 7)) * 16);
  const int aoff1 = aoff0 ^ 64;
  int boff[4];
  constexpr int TSTEP = GU ? 1024 : 3840;
  {
    const int q = fr >> 2, p = lane & 3;
    if (GU) {
      int f = q | ((g & 1) << 2);
#pragma unroll
      for (int j = 0; j < 4; ++j)
        boff[j] = L_B + (j >> 1) * 8192 + (8 * g + q) * 256 + (((2 * wn + (j & 1)) ^ f) * 32) + p * 8;
    } else {
      int rho = 16 * (g >> 1) + 4 * (g & 1) + q;
#pragma unroll
      for (int j = 0; j < 3; ++j) boff[j] = L_B + rho * 480 + (20 * j + 5 * wn + p) * 8;
      boff[3] = L_B + rho * 480 + (20 * (p == 3 ? 0 : p) + 5 * wn + 4) * 8;
    }
  }

  f32x4 acc[MT][4];
#pragma unroll
  for (int m = 0; m < MT; ++m)
#pragma unroll
    for (int j = 0; j < 4; ++j) acc[m][j] = f32x4{0.f, 0.f, 0.f, 0.f};
  bf16x8 fa0[MT], fa1[MT];
  s16x4 fb0[8], fb1[8];

  STAMPAT(1);
  WAITN_BAR(13);
  STAMPAT(2);
  unsigned wpk[8];
  if (!GU) {
    asm volatile("" : "+v"(wraw[0]), "+v"(wraw[1]), "+v"(wraw[2]), "+v"(wraw[3]), "+v"(wraw[4]), "+v"(wraw[5]), "+v"(wraw[6]), "+v"(wraw[7]),
                 "+v"(wraw[8]), "+v"(wraw[9]), "+v"(wraw[10]), "+v"(wraw[11]), "+v"(wraw[12]), "+v"(wraw[13]), "+v"(wraw[14]), "+v"(wraw[15]));
#pragma unroll
    for (int i = 0; i < 8; ++i) {
      wpk[i] = wraw[2 * i] | (wraw[2 * i + 1] << 16);
      if (i >= 4 && (fr & 3) != 0) wpk[i] = 0u;
    }
    asm volatile("" : "+v"(wpk[0]), "+v"(wpk[1]), "+v"(wpk[2]), "+v"(wpk[3]), "+v"(wpk[4]), "+v"(wpk[5]), "+v"(wpk[6]), "+v"(wpk[7]));
  }
#pragma unroll
  for (int m = 0; m < MT; ++m) fa0[m] = ld128(sm + aoff0 + m * 2048);
#pragma unroll
  for (int j = 0; j < 8; ++j) fb0[j] = tr1(sm + boff[j >> 1] + (j & 1) * TSTEP);
  MEMFENCE();

  constexpr int NM = MT * 4;
  constexpr int DS = (MT == 6) ? 6 : 5;
#define MMA(i, FA, FB) acc[(i) >> 2][(i) & 3] = mfma32(cat(FB[2 * ((i) & 3)], FB[2 * ((i) & 3) + 1]), FA[(i) >> 2], acc[(i) >> 2][(i) & 3])
#define BADDR(sl, i) (sm + (sl) * B_SLOT + boff[(i) >> 1] + ((i) & 1) * TSTEP)
  u32x4 ackv, stv[MT];
  const char* ackp = ws + WS_ACK + bid * 64;
#define STP(m) (ws + WS_STAT + (size_t)(r0 + strow(mh * 96 + (m) * 16 + fr, nrow)) * 16)
#pragma unroll
  for (int t = 0; t < 10; ++t) {
    SCHED0();
    WAITN_BAR(t < 8 ? 11 : (t == 8 ? 7 : 1 + MT));
    SCHED0();
#pragma unroll
    for (int i = 0; i < NM; ++i) {
      MMA(i, fa0, fb0);
      SCHED0();
      if (i < 8) fb1[i] = tr1(BADDR((2 * t + 1) % 5, i));
      else if (i < 8 + MT) fa1[i - 8] = ld128(sm + (t % 3) * A_SLOT + aoff1 + (i - 8) * 2048);
      if (t < 8) {
        if (i == 2) { if (MT == 6) DMA_A2((t + 2) * 128, adst0 + 16384 + ((t + 2) % 3) * A_SLOT); else DMA_A2((t + 2) * 128, a2base + a2mul * (((t + 2) % 3) * A_SLOT)); }
        if (i == 2 + DS) DMA_B0((2 * t + 5) * SSTEP, bdst0 + ((2 * t) % 5) * B_SLOT);
        if (i == 2 + 2 * DS) DMA_B1((2 * t + 5) * SSTEP, bdst0 + ((2 * t) % 5) * B_SLOT + 8192);
      }
      SCHED0();
    }
    if (t < 9) WAITN_BAR(t < 8 ? 9 : 2); else LGKM_BAR();
    SCHED0();
#pragma unroll
    for (int i = 0; i < NM; ++i) {
      MMA(i, fa1, fb1);
      SCHED0();
      if (t < 9) {
        if (i < 8) fb0[i] = tr1(BADDR((2 * t + 2) % 5, i));
        else if (i < 8 + MT) fa0[i - 8] = ld128(sm + ((t + 1) % 3) * A_SLOT + aoff0 + (i - 8) * 2048);
      }
      if (t < 7) {
        if (i == 1) DMA_B0((2 * t + 6) * SSTEP, bdst0 + ((2 * t + 1) % 5) * B_SLOT);
        if (i == 1 + DS) DMA_B1((2 * t + 6) * SSTEP, bdst0 + ((2 * t + 1) % 5) * B_SLOT + 8192);
        if (i == 1 + 2 * DS) DMA_A0((t + 3) * 128, adst0 + (t % 3) * A_SLOT);
        if (i == 1 + 3 * DS) DMA_A1((t + 3) * 128, adst0 + (t % 3) * A_SLOT + 8192);
      }
      if (i == 4 && t == 8) {
        asm volatile("global_load_dwordx4 %0, %1, off sc1" : "=&v"(ackv) : "v"(ackp) : "memory");
#pragma unroll
        for (int m = 0; m < MT; ++m) asm volatile("global_load_dwordx4 %0, %1, off sc1" : "=&v"(stv[m]) : "v"(STP(m)) : "memory");
      }
      SCHED0();
    }
  }
  SCHED0();
  STAMPAT(3);

  if (fr == 15) {
#pragma unroll
    for (int j = 0; j < 4; ++j) *(LAS f32x4*)(sm + L_CS + w * 256 + j * 64 + g * 16) = acc[MT - 1][j];
  }
  if (MT == 6) asm volatile("s_waitcnt vmcnt(0) lgkmcnt(0)" : "+v"(ackv), "+v"(stv[0]), "+v"(stv[1]), "+v"(stv[2]), "+v"(stv[3]), "+v"(stv[4]), "+v"(stv[MT - 1]) :: "memory");
  else asm volatile("s_waitcnt vmcnt(0) lgkmcnt(0)" : "+v"(ackv), "+v"(stv[0]), "+v"(stv[1]), "+v"(stv[2]), "+v"(stv[3]), "+v"(stv[4]) :: "memory");
  f32x4 cs[4];
#pragma unroll
  for (int j = 0; j < 4; ++j) cs[j] = *(const LAS f32x4*)(sm + L_CS + w * 256 + j * 64 + g * 16);
  {
    const u32x2 nv = *(const LAS u32x2*)(sm + L_NONCE);
    for (unsigned spins = 0; spins < SPIN_MAX; ++spins) {
      bool ok = (ackv[0] == nv[0]) && (ackv[1] == nv[1]);
#pragma unroll
      for (int m = 0; m < MT; ++m) ok = ok && (stv[m][2] == ackv[2]) && (stv[m][3] == ackv[3]);
      if (all_ok(ok)) break;
      __builtin_amdgcn_s_sleep(2);
#pragma unroll
      for (int m = 0; m < MT; ++m) asm volatile("global_load_dwordx4 %0, %1, off sc1" : "=&v"(stv[m]) : "v"(STP(m)) : "memory");
      ackv = ld128_sc1(ackp);
#pragma unroll
      for (int m = 0; m < MT; ++m) asm volatile("" : "+v"(stv[m]) :: "memory");
    }
  }
  const u32x2 ep = u32x2{ackv[2], ackv[3]};
  if (w == 0 && lane == 0) st64_sc1(ws + WS_DONE + bid * 64, ep);
  if (GU) {
    const unsigned vB = p2_vB(w, lane);
    p2_issueB_down(srd_wd, vB, c0p2, w, smb, 0, rot);
    p2_issueB_down(srd_wd, vB, c0p2, w, smb, 1, rot);
    p2_issueB_down(srd_wd, vB, c0p2, w, smb, 2, rot);
  }
  float rstd[MT];
#pragma unroll
  for (int m = 0; m < MT; ++m) {
    const f32x2 nm = sp2(opq(-u2f(stv[m][0])));
    rstd[m] = opq(u2f(stv[m][1]));
#pragma unroll
    for (int j = 0; j < 4; ++j) {
      f32x2 l = fma2(nm, lo2(cs[j]), lo2(acc[m][j])), h = fma2(nm, hi2(cs[j]), hi2(acc[m][j]));
      acc[m][j] = f32x4{l[0], l[1], h[0], h[1]};
    }
  }
  STAMPAT(4);

  if (GU) {
    const float K0 = -2.0f * 0.7978845608028654f * 1.4426950408889634f;
    const float K1c = K0 * 0.044715f;
    ldsp stg = sm + w * 7680;
    char* hg = ws + WS_H + ct * 256 + wn * 64 + (lane & 3) * 16;
#pragma unroll
    for (int m = 0; m <= MT; ++m) {
      if (m < MT) {
        const float r = rstd[m], r2 = r * r;
        const f32x2 c0 = sp2(opq(K0 * r)), c1 = sp2(opq(K1c * r2 * r)), ir2 = sp2(opq(__builtin_amdgcn_rcpf(r2)));
        u32x2 hp[2];
#pragma unroll
        for (int j = 0; j < 2; ++j) {
#pragma unroll
          for (int q = 0; q < 2; ++q) {
            const f32x2 a = q ? hi2(acc[m][j]) : lo2(acc[m][j]), b = q ? hi2(acc[m][j + 2]) : lo2(acc[m][j + 2]);
            const f32x2 z = a * fma2(a * a, c1, c0);
            const f32x2 e = f32x2{__builtin_amdgcn_exp2f(z[0]), __builtin_amdgcn_exp2f(z[1])};
            const f32x2 d = fma2(e, ir2, ir2);
            const f32x2 hv = (a * b) * f32x2{__builtin_amdgcn_rcpf(d[0]), __builtin_amdgcn_rcpf(d[1])};
            hp[j][q] = pk2(hv[0], hv[1]);
          }
        }
        *(LAS u32x2*)(stg + (m * 16 + fr) * 80 + g * 8) = hp[0];
        *(LAS u32x2*)(stg + (m * 16 + fr) * 80 + 32 + g * 8) = hp[1];
      }
      if (m >= 1) {
        const int rl = (m - 1) * 16 + (lane >> 2);
        u32x4 v = *(const LAS u32x4*)(stg + rl * 80 + (lane & 3) * 16);
        const int sl = mh * 96 + rl;
        const int ri = sl < 95 ? sl : sl - 1;
        if (sl != 95 && ri < nrow) st128_sc1(hg + (size_t)(r0 + ri) * 3072, v);
      }
    }
    STAMPAT(5);
    WAITN_BAR(0);
    if (w == 0 && lane == 0) st64_sc1(ws + WS_HF + rg * 128 + ct * 8, ep);
    STAMPAT(6);
  } else {
    const int head = ct * 4 + wn;
    bf16x8 wq[2], wk[2];
#pragma unroll
    for (int ft = 0; ft < 2; ++ft) {
      wq[ft] = asb(u32x4{wpk[4 * ft], wpk[4 * ft + 1], g == 0 ? wpk[4 * ft + 2] : 0u, g == 0 ? wpk[4 * ft + 3] : 0u});
      wk[ft] = asb(u32x4{wpk[4 * ft], wpk[4 * ft + 1], g == 1 ? wpk[4 * ft + 2] : 0u, g == 1 ? wpk[4 * ft + 3] : 0u});
    }
    const unsigned tag = ep[0] ^ (ep[1] * 0x9E3779B1u);
    const float L2E = 1.4426950408889634f;
    const f32x4 z4 = f32x4{0.f, 0.f, 0.f, 0.f};
    bool live[6];
    u32x2 rem[6];
#pragma unroll
    for (int m = 0; m < 6; ++m) {
      int s = mh * 96 + m * 16 + fr;
      int ri = s < 95 ? s : s - 1;
      live[m] = (m < MT) && (s != 95) && (ri < nrow);
      if (m < MT) rem[m] = u32x2{pk2(acc[m][3][0], acc[m][3][1]), pk2(acc[m][3][2], acc[m][3][3])};
    }
    ldsp pk_img = sm + w * 17408;
    ldsp v_img = pk_img + 96 * 104;
    {
      unsigned kf[6][5];
      u32x2 vv[6], ve[6];
#pragma unroll
      for (int mp = 0; mp < 3; ++mp) {
        f32x2 y01[2], y23[2], y4;
#pragma unroll
        for (int hh = 0; hh < 2; ++hh) {
          const int m = 2 * mp + hh;
          if (m < MT) {
            const f32x2 r = sp2(rstd[m]);
            const float c = opq(rstd[m] * L2E);
            u32x4 kb = u32x4{pk2(acc[m][1][0], acc[m][1][1]), pk2(acc[m][1][2], acc[m][1][3]), rem[m][0], rem[m][1]};
            f32x4 k0 = mfma32(wk[0], asb(kb), z4);
            f32x4 k1 = mfma32(wk[1], asb(kb), z4);
            y01[hh] = lo2(k0) * sp2(c); y23[hh] = hi2(k0) * sp2(c); y4[hh] = k1[0] * c;
            vv[m] = u32x2{pk2v(lo2(acc[m][2]) * r), pk2v(hi2(acc[m][2]) * r)};
            u32x2 vr = u32x2{pk2v(lo2(acc[m][3]) * r), pk2v(hi2(acc[m][3]) * r)};
            ve[m] = (g == 2) ? vr : u32x2{0x3F803F80u, 0x3F803F80u};
          } else {
            y01[hh] = sp2(0.f); y23[hh] = sp2(0.f); y4[hh] = 0.f;
            vv[m] = u32x2{0u, 0u}; ve[m] = vv[m];
          }
        }
        featmap2(y01[0], y23[0], y01[1], y23[1], y4, live[2 * mp], live[2 * mp + 1], g, kf[2 * mp], kf[2 * mp + 1]);
      }
      SCHED0();
#pragma unroll
      for (int m = 0; m < 6; ++m) {
        int tok = m * 16 + fr;
        *(LAS u32x2*)(pk_img + tok * 104 + g * 8) = u32x2{kf[m][0], kf[m][1]};
        *(LAS u32x2*)(pk_img + tok * 104 + 32 + g * 8) = u32x2{kf[m][2], kf[m][3]};
        *(LAS u32x2*)(pk_img + tok * 104 + 64 + g * 8) = u32x2{kf[m][4], 0u};
        *(LAS u32x2*)(v_img + tok * 72 + g * 8) = vv[m];
        *(LAS u32x2*)(v_img + tok * 72 + 32 + (g ^ 2) * 8) = ve[m];
      }
      LGKM0();
      SCHED0();
    }
    f32x4 kv[3][2];
#pragma unroll
    for (int ft = 0; ft < 3; ++ft)
#pragma unroll
      for (int dt = 0; dt < 2; ++dt) kv[ft][dt] = z4;
    {
      const int q = fr >> 2, p = lane & 3;
#pragma unroll
      for (int s = 0; s < 3; ++s) {
        int row = 32 * s + 8 * g + q;
        bf16x8 a[3], b[2];
#pragma unroll
        for (int ft = 0; ft < 3; ++ft) a[ft] = tr2(pk_img + row * 104 + ft * 32 + p * 8, pk_img + (row + 4) * 104 + ft * 32 + p * 8);
#pragma unroll
        for (int dt = 0; dt < 2; ++dt) b[dt] = tr2(v_img + row * 72 + dt * 32 + p * 8, v_img + (row + 4) * 72 + dt * 32 + p * 8);
#pragma unroll
        for (int ft = 0; ft < 3; ++ft)
#pragma unroll
          for (int dt = 0; dt < 2; ++dt) kv[ft][dt] = mfma32(a[ft], b[dt], kv[ft][dt]);
      }
    }
    {
      f32x4 o5[5] = {kv[0][0], kv[1][0], kv[0][1], kv[1][1], f32x4{kv[2][0][0], kv[2][0][1], kv[2][1][0], kv[2][1][1]}};
      ldsp ex = sm + (4 + wn) * 17408;
      if (mh == 1) {
        LGKM0();
#pragma unroll
        for (int i = 0; i < 5; ++i) *(LAS f32x4*)(ex + i * 1024 + lane * 16) = o5[i];
      }
      LGKM_BAR();
      if (mh == 0) {
        unsigned pp[10];
#pragma unroll
        for (int i = 0; i < 5; ++i) {
          f32x4 e = *(const LAS f32x4*)(ex + i * 1024 + lane * 16);
          pp[2 * i] = pk2(o5[i][0] + e[0], o5[i][1] + e[1]);
          pp[2 * i + 1] = pk2(o5[i][2] + e[2], o5[i][3] + e[3]);
        }
        char* kp = ws + WS_KV + (size_t)(rg * NH + head) * 4096 + lane * 16;
        st128_sc1(kp, u32x4{pp[0], pp[1], pp[2], tag});
        st128_sc1(kp + 1024, u32x4{pp[3], pp[4], pp[5], tag});
        st128_sc1(kp + 2048, u32x4{pp[6], pp[7], pp[8], tag});
        st128_sc1(kp + 3072, u32x4{pp[9], 0u, 0u, tag});
      }
    }
    STAMPAT(5);
    unsigned qf[6][5];
#pragma unroll
    for (int mp = 0; mp < 3; ++mp) {
      f32x2 y01[2], y23[2], y4;
#pragma unroll
      for (int hh = 0; hh < 2; ++hh) {
        const int m = 2 * mp + hh;
        if (m < MT) {
          const float c = opq(rstd[m] * L2E);
          u32x4 qb = u32x4{pk2(acc[m][0][0], acc[m][0][1]), pk2(acc[m][0][2], acc[m][0][3]), rem[m][0], rem[m][1]};
          f32x4 q0 = mfma32(wq[0], asb(qb), z4);
          f32x4 q1 = mfma32(wq[1], asb(qb), z4);
          y01[hh] = lo2(q0) * sp2(c); y23[hh] = hi2(q0) * sp2(c); y4[hh] = q1[0] * c;
        } else {
          y01[hh] = sp2(0.f); y23[hh] = sp2(0.f); y4[hh] = 0.f;
        }
      }
      featmap2(y01[0], y23[0], y01[1], y23[1], y4, true, true, g, qf[2 * mp], qf[2 * mp + 1]);
    }
    STAMPAT(6);
    float ks[20];
    {
      {
        const char* p1 = ws + WS_KV + ((size_t)(mh * 6 + (lane < 6 ? lane : 0)) * NH + head) * 4096 + 3072 + 1008;
        for (unsigned spins = 0; spins < SPIN_MAX; ++spins) {
          u32x4 v = ld128_sc1(p1);
          if (all_ok(v[3] == tag)) break;
          __builtin_amdgcn_s_sleep(POLLS);
        }
      }
      u32x4 pv[6][4];
      const char* kp = ws + WS_KV + (size_t)((mh * 6) * NH + head) * 4096 + lane * 16;
      for (unsigned spins = 0; spins < SPIN_MAX; ++spins) {
#pragma unroll
        for (int c = 0; c < 6; ++c) {
          const char* p = kp + (size_t)c * NH * 4096;
          asm volatile("global_load_dwordx4 %0, %4, off sc1\n\tglobal_load_dwordx4 %1, %4, off offset:1024 sc1\n\t"
                       "global_load_dwordx4 %2, %4, off offset:2048 sc1\n\tglobal_load_dwordx4 %3, %4, off offset:3072 sc1"
                       : "=&v"(pv[c][0]), "=&v"(pv[c][1]), "=&v"(pv[c][2]), "=&v"(pv[c][3]) : "v"(p) : "memory");
        }
        asm volatile("s_waitcnt vmcnt(0)" : "+v"(pv[0][0]), "+v"(pv[0][1]), "+v"(pv[0][2]), "+v"(pv[0][3]), "+v"(pv[1][0]), "+v"(pv[1][1]), "+v"(pv[1][2]), "+v"(pv[1][3]),
                     "+v"(pv[2][0]), "+v"(pv[2][1]), "+v"(pv[2][2]), "+v"(pv[2][3]), "+v"(pv[3][0]), "+v"(pv[3][1]), "+v"(pv[3][2]), "+v"(pv[3][3]),
                     "+v"(pv[4][0]), "+v"(pv[4][1]), "+v"(pv[4][2]), "+v"(pv[4][3]), "+v"(pv[5][0]), "+v"(pv[5][1]), "+v"(pv[5][2]), "+v"(pv[5][3]) :: "memory");
        bool ok = true;
#pragma unroll
        for (int c = 0; c < 6; ++c)
#pragma unroll
          for (int i = 0; i < 4; ++i) ok = ok && (pv[c][i][3] == tag);
        if (all_ok(ok)) break;
        __builtin_amdgcn_s_sleep(2);
      }
      STAMPAT(7);
#pragma unroll
      for (int i = 0; i < 10; ++i) {
        float lo = 0.f, hi = 0.f;
#pragma unroll
        for (int c = 0; c < 6; ++c) {
          unsigned u = pv[c][i / 3][i % 3];
          lo += u2f(u << 16); hi += u2f(u & 0xffff0000u);
        }
        ks[2 * i] = lo; ks[2 * i + 1] = hi;
      }
      ldsp mine = sm + w * 17408 + 12288, other = sm + (w ^ 4) * 17408 + 12288;
      SCHED0();
#pragma unroll
      for (int i = 0; i < 5; ++i) *(LAS f32x4*)(mine + i * 1024 + lane * 16) = f32x4{ks[4 * i], ks[4 * i + 1], ks[4 * i + 2], ks[4 * i + 3]};
      LGKM_BAR();
#pragma unroll
      for (int i = 0; i < 5; ++i) {
        f32x4 e = *(const LAS f32x4*)(other + i * 1024 + lane * 16);
#pragma unroll
        for (int q = 0; q < 4; ++q) ks[4 * i + q] = (mh == 0) ? ks[4 * i + q] + e[q] : e[q] + ks[4 * i + q];
      }
    }
    const bf16x8 ka0 = asb(u32x4{pk2(ks[0], ks[1]), pk2(ks[2], ks[3]), pk2(ks[4], ks[5]), pk2(ks[6], ks[7])});
    const bf16x8 ka1 = asb(u32x4{pk2(ks[8], ks[9]), pk2(ks[10], ks[11]), pk2(ks[12], ks[13]), pk2(ks[14], ks[15])});
    const bf16x8 kb0 = asb(u32x4{pk2(ks[16], ks[17]), 0u, 0u, 0u});
    const bf16x8 kb1 = asb(u32x4{pk2(ks[18], ks[19]), 0u, 0u, 0u});
    {
      u32x2 om_[MT], or_[MT];
#pragma unroll
      for (int m = 0; m < MT; ++m) {
        bf16x8 b0 = asb(u32x4{qf[m][0], qf[m][1], qf[m][2], qf[m][3]});
        bf16x8 b1 = asb(u32x4{qf[m][4], 0u, 0u, 0u});
        f32x4 n0 = mfma32(kb0, b1, mfma32(ka0, b0, z4));
        f32x4 n1 = mfma32(kb1, b1, mfma32(ka1, b0, z4));
        float da = n1[0], db = n1[0];
        swap16(da, db);
        const f32x2 inv = sp2(__builtin_amdgcn_rcpf(db + 1e-6f));
        om_[m] = u32x2{pk2v(lo2(n0) * inv), pk2v(hi2(n0) * inv)};
        or_[m] = u32x2{pk2v(lo2(n1) * inv), pk2v(hi2(n1) * inv)};
      }
      SCHED0();
      ldsp stg = sm + w * 17408 + 8192;
#pragma unroll
      for (int m = 0; m < MT; ++m) {
        *(LAS u32x2*)(stg + (m * 16 + fr) * 32 + g * 8) = om_[m];
        if (g == 0) *(LAS u32x2*)(stg + 3072 + (m * 16 + fr) * 8) = or_[m];
      }
      LGKM0();
      const int tok0 = r0 + mh * 95;
      const int ntok = mh == 0 ? 95 : nrow - 95;
      char* om = ws + WS_OM + ((size_t)head * S_ + tok0) * 32;
      char* orr = ws + WS_OR + ((size_t)head * S_ + tok0) * 8;
      u32x4 tv[3];
      u32x2 tr[2];
#pragma unroll
      for (int i = 0; i < 3; ++i) tv[i] = *(const LAS u32x4*)(stg + i * 1024 + lane * 16);
#pragma unroll
      for (int i = 0; i < 2; ++i) tr[i] = *(const LAS u32x2*)(stg + 3072 + ((i * 64 + lane) < 96 ? (i * 64 + lane) : 0) * 8);
#pragma unroll
      for (int i = 0; i < 3; ++i)
        if (i * 32 + (lane >> 1) < ntok) st128_sc1(om + i * 1024 + lane * 16, tv[i]);
#pragma unroll
      for (int i = 0; i < 2; ++i)
        if (i * 64 + lane < ntok) st64_sc1(orr + (i * 64 + lane) * 8, tr[i]);
    }
    WAITN_BAR(0);
    if (w == 0 && lane == 0) st64_sc1(ws + WS_OF + rg * 128 + ct * 8, ep);
    STAMPAT(8);
  }
  return ep;
}

DI u32x2 service(ldsp sm, const char* x, char* ws, int sid, int w, int lane) {
  const int bid = __builtin_amdgcn_workgroup_id_x();
  const int g = lane >> 4, fr = lane & 15;
  STAMPAT(0);
  if (w == 0) {
    unsigned long long nn = __builtin_amdgcn_s_memrealtime();
    u32x2 nv = u32x2{(unsigned)nn, (unsigned)(nn >> 32)};
    if (lane == 0) {
      if (sid != 15) st64_sc1(ws + WS_REQ + bid * 64, nv);
      *(LAS u32x2*)(sm + L_NONCE) = nv;
    }
  }
  LGKM_BAR();
  { const unsigned long long t0_ = __builtin_amdgcn_s_memrealtime(); while (__builtin_amdgcn_s_memrealtime() - t0_ < 200) __builtin_amdgcn_s_sleep(1); }
  const u32x2 nv = *(const LAS u32x2*)(sm + L_NONCE);
  u32x2 ep = u32x2{nv[0] ^ 0x5bd1e995u, nv[1] ^ 0x80000000u};
  if (sid == 15 && w < 4) {
    const int cb = w * 64 + lane;
    const bool me = (cb == bid);
    u32x2 lastq = u32x2{0u, 0u};
    for (unsigned spins = 0; spins < SPIN_MAX; ++spins) {
      u32x2 rq, dn;
      asm volatile("global_load_dwordx2 %0, %2, off sc1\n\tglobal_load_dwordx2 %1, %3, off sc1\n\ts_waitcnt vmcnt(0)"
                   : "=&v"(rq), "=&v"(dn) : "v"(ws + WS_REQ + cb * 64), "v"(ws + WS_DONE + cb * 64) : "memory");
      if (all_ok(me || (dn[0] == ep[0] && dn[1] == ep[1]))) break;
      if (spins == 0 || rq[0] != lastq[0] || rq[1] != lastq[1]) st128_sc1(ws + WS_ACK + cb * 64, u32x4{rq[0], rq[1], ep[0], ep[1]});
      lastq = rq;
      if (spins > 12) __builtin_amdgcn_s_sleep(16);
    }
  } else {
    const int npass = sid == 15 ? 2 : 1;
    bool have = (sid == 15);
#pragma unroll 1
    for (int ps = 0; ps < npass; ++ps) {
      const int rbase = sid * 128 + (sid == 15 ? (w - 4) * 32 + ps * 16 : w * 16) + g;
      u32x4 v[4][5];
#pragma unroll
      for (int i = 0; i < 4; ++i)
#pragma unroll
        for (int c = 0; c < 5; ++c) v[i][c] = *(const u32x4*)(x + (size_t)(rbase + 4 * i) * 1280 + fr * 16 + c * 256);
      float mean[4], rstd[4];
#pragma unroll
      for (int i = 0; i < 4; ++i) {
        f32x2 a = sp2(0.f);
#pragma unroll
        for (int c = 0; c < 5; ++c)
#pragma unroll
          for (int e = 0; e < 4; ++e) a += f32x2{u2f(v[i][c][e] << 16), u2f(v[i][c][e] & 0xffff0000u)};
        float s = a[0] + a[1];
        s += dpp<0xB1>(s); s += dpp<0x4E>(s); s += dpp<0x141>(s); s += dpp<0x140>(s);
        mean[i] = s * (1.0f / 640.0f);
        const f32x2 m2 = sp2(opq(mean[i]));
        f32x2 b = sp2(0.f);
#pragma unroll
        for (int c = 0; c < 5; ++c)
#pragma unroll
          for (int e = 0; e < 4; ++e) {
            const f32x2 d = f32x2{u2f(v[i][c][e] << 16), u2f(v[i][c][e] & 0xffff0000u)} - m2;
            b = fma2(d, d, b);
          }
        float t = b[0] + b[1];
        t += dpp<0xB1>(t); t += dpp<0x4E>(t); t += dpp<0x141>(t); t += dpp<0x140>(t);
        rstd[i] = __builtin_amdgcn_rsqf(t * (1.0f / 640.0f) + 1e-5f);
      }
      if (!have) {
        u32x4 ack;
        for (unsigned spins = 0; spins < SPIN_MAX; ++spins) {
          ack = ld128_sc1(ws + WS_ACK + bid * 64);
          if (all_ok(ack[0] == nv[0] && ack[1] == nv[1])) break;
          __builtin_amdgcn_s_sleep(2);
        }
        ep = u32x2{ack[2], ack[3]};
        have = true;
        if (w == 0 && lane == 0) st64_sc1(ws + WS_DONE + bid * 64, ep);
      }
#pragma unroll
      for (int i = 0; i < 4; ++i)
        if (fr == 0) st128_sc1(ws + WS_STAT + (size_t)(rbase + 4 * i) * 16, u32x4{f2u(mean[i]), f2u(rstd[i]), ep[0], ep[1]});
    }
  }
  STAMPAT(1);
  return ep;
}

__global__ __launch_bounds__(512, 2) void fused(const char* __restrict__ x, const char* __restrict__ wqkv,
                                                const char* __restrict__ wphi, const char* __restrict__ wo,
                                                const char* __restrict__ wg, const char* __restrict__ wu,
                                                const char* __restrict__ wd, char* __restrict__ ws, char* __restrict__ out) {
  extern __shared__ __attribute__((aligned(16))) char smem[];
  ldsp sm = (ldsp)smem;
  const int tid = __builtin_amdgcn_workitem_id_x();
  const int bp = __builtin_amdgcn_workgroup_id_x(), xcd = bp & 7, ip = bp >> 3;
  const int idx = ip < 2 ? 30 + ip : ip - 2;
  const int b = idx * 8 + xcd;
  const int w = __builtin_amdgcn_readfirstlane(tid >> 6), lane = tid & 63;
  const u32x4 srd_wd = make_srd(wd, DF_ * D_ * 2);
  const int rgl = idx / 5, c5 = idx % 5;
  const int rg = (xcd >> 2) * 6 + rgl, cq = xcd & 3;
  if (idx < 30 && c5 >= 3) {
    const int ct = cq * 2 + (c5 - 3);
    if (w < 4) phase1<6, false>(sm, x, wqkv, wphi, wg, wu, srd_wd, ws, rg, ct, 0, 0, w, lane);
    else phase1<5, false>(sm, x, wqkv, wphi, wg, wu, srd_wd, ws, rg, ct, 0, 0, w, lane);
    return;
  }
  const int p2i = idx < 30 ? rgl * 3 + c5 : 18 + (idx - 30);
  const int rt = (xcd >> 1) * 4 + p2i / 5, ct2 = (xcd & 1) * 5 + p2i % 5;
#define ROTF ((((p2i % 5) * 12) / 5 + (p2i / 5) * 3) % 12)
  const int rot = ROTF;
  u32x2 ep;
  if (idx < 30) {
    const int ct = cq * 3 + c5;
    if (w < 4) ep = phase1<6, true>(sm, x, wqkv, wphi, wg, wu, srd_wd, ws, rg, ct, ct2 * 64, rot, w, lane);
    else ep = phase1<5, true>(sm, x, wqkv, wphi, wg, wu, srd_wd, ws, rg, ct, ct2 * 64, rot, w, lane);
  } else {
    ep = service(sm, x, ws, b - 240, w, lane);
    {
      const u32x4 srd_wo = make_srd(wo, D_ * D_ * 2);
      const unsigned vp = (lane >> 3) * 1280 + (lane & 7) * 16 + (xcd & 1) * 640;
      const unsigned dst = (unsigned)(size_t)sm + w * 1024;
      const int w16 = (idx - 30) * 8 + w;
      if (!(b == 255 && w < 4)) {
#pragma unroll 1
        for (int p = w16; p < 192; p += 16) {
#pragma unroll
          for (int c = 0; c < 5; ++c) dma_b(srd_wd, vp, p * 8 * 1280 + c * 128, dst);
          asm volatile("s_waitcnt vmcnt(30)" ::: "memory");
        }
#pragma unroll 1
        for (int p = w16; p < 80; p += 16) {
#pragma unroll
          for (int c = 0; c < 5; ++c) dma_b(srd_wo, vp, p * 8 * 1280 + c * 128, dst);
          asm volatile("s_waitcnt vmcnt(30)" ::: "memory");
        }
      }
      WAITN_BAR(0);
    }
    const unsigned vB = p2_vB(w, lane);
    const unsigned smb = (unsigned)(size_t)sm;
    p2_issueB_down(srd_wd, vB, ct2 * 64, w, smb, 0, rot);
    p2_issueB_down(srd_wd, vB, ct2 * 64, w, smb, 1, rot);
    p2_issueB_down(srd_wd, vB, ct2 * 64, w, smb, 2, rot);
  }
  phase2(sm, x, wo, srd_wd, ws, out, rt, ct2, rot, w, lane, ep);
}

#if defined(__HIP_DEVICE_COMPILE__)

#endif

extern "C" void kernel_launch(void* const* d_in, const int* in_sizes, int n_in, void* d_out, int out_size, void* d_ws,
                              size_t ws_size, hipStream_t stream) {
  (void)hipFuncSetAttribute((const void*)fused, hipFuncAttributeMaxDynamicSharedMemorySize, K_LDS);
  fused<<<256, 512, K_LDS, stream>>>((const char*)d_in[0], (const char*)d_in[1], (const char*)d_in[2], (const char*)d_in[3],
                                     (const char*)d_in[5], (const char*)d_in[6], (const char*)d_in[7], (char*)d_ws, (char*)d_out);
}
```

```cpp
#define ORDER 2
#define POLLS 6
#include <hip/hip_runtime.h>
#include <hip/hip_bf16.h>
#include <stdint.h>

typedef __attribute__((ext_vector_type(8))) __bf16 bf16x8;
typedef __attribute__((ext_vector_type(2))) __bf16 bf16x2;
typedef __attribute__((ext_vector_type(4))) short s16x4;
typedef __attribute__((ext_vector_type(8))) short s16x8;
typedef __attribute__((ext_vector_type(4))) float f32x4;
typedef __attribute__((ext_vector_type(2))) float f32x2;
typedef __attribute__((ext_vector_type(4))) unsigned u32x4;
typedef __attribute__((ext_vector_type(2))) unsigned u32x2;
#define LAS __attribute__((address_space(3)))
#define DI __device__ __forceinline__
typedef LAS char* ldsp;

constexpr int S_ = 2048, D_ = 640, DF_ = 1536, NH = 32;
constexpr int NRG = 12;
constexpr size_t WS_H = 0;
constexpr size_t WS_KV = WS_H + (size_t)S_ * DF_ * 2;
constexpr size_t WS_OM = WS_KV + (size_t)NRG * NH * 4096;
constexpr size_t WS_OR = WS_OM + (size_t)NH * S_ * 32;
constexpr size_t WS_REQ = WS_OR + (size_t)NH * S_ * 8;
constexpr size_t WS_ACK = WS_REQ + 256 * 64;
constexpr size_t WS_DONE = WS_ACK + 256 * 64;
constexpr size_t WS_STAT = WS_DONE + 256 * 64;
constexpr size_t WS_HF = WS_STAT + 2048 * 16;
constexpr size_t WS_KF = WS_HF + 12 * 128;
constexpr size_t WS_OF = WS_KF + 8 * 128;
constexpr size_t WS_DBG = 64u << 20;

constexpr int A_SLOT = 22528, B_SLOT = 16384;
constexpr int L_A = 0;
constexpr int L_B = 3 * A_SLOT;
constexpr int L_MISC = L_B + 5 * B_SLOT;
constexpr int L_DUMMY = L_MISC;
constexpr int L_NONCE = L_MISC + 1024;
constexpr int L_CS = L_NONCE + 64;
constexpr int K_LDS = L_CS + 2048;
constexpr int P2_A = 0, P2_B = 98304;
constexpr unsigned SPIN_MAX = 1u << 20;

#if defined(__HIP_DEVICE_COMPILE__)

#endif

#define STAMPAT(i)

DI float u2f(unsigned u) { return __builtin_bit_cast(float, u); }
DI unsigned f2u(float f) { return __builtin_bit_cast(unsigned, f); }
DI u32x4 make_srd(const void* p, unsigned bytes) {
  uint64_t a = (uint64_t)p;
  u32x4 r;
  r[0] = (unsigned)a;
  r[1] = (unsigned)(a >> 32) & 0xffffu;
  r[2] = bytes;
  r[3] = 0x00020000u;
  return r;
}
DI void dma_b(u32x4 srd, unsigned voff, unsigned soff, unsigned m0v) {
  asm volatile("s_mov_b32 m0, %3\n\ts_nop 0\n\tbuffer_load_dwordx4 %0, %1, %2 offen lds" ::"v"(voff), "s"(srd), "s"(soff), "s"(m0v) : "memory");
}
DI void dma_b_mask(u32x4 srd, unsigned voff, unsigned soff, unsigned m0v, unsigned long long mask) {
  asm volatile("s_mov_b32 m0, %3\n\ts_mov_b64 exec, %4\n\tbuffer_load_dwordx4 %0, %1, %2 offen lds\n\ts_mov_b64 exec, -1" ::"v"(voff), "s"(srd), "s"(soff), "s"(m0v), "s"(mask) : "memory");
}
DI void dma_b_sc1(u32x4 srd, unsigned voff, unsigned soff, unsigned m0v) {
  asm volatile("s_mov_b32 m0, %3\n\ts_nop 0\n\tbuffer_load_dwordx4 %0, %1, %2 offen sc1 lds" ::"v"(voff), "s"(srd), "s"(soff), "s"(m0v) : "memory");
}
DI u32x4 ld128_sc1(const char* p) { u32x4 v; asm volatile("global_load_dwordx4 %0, %1, off sc1\n\ts_waitcnt vmcnt(0)" : "=&v"(v) : "v"(p) : "memory"); return v; }
DI u32x2 ld64_sc1(const char* p) { u32x2 v; asm volatile("global_load_dwordx2 %0, %1, off sc1\n\ts_waitcnt vmcnt(0)" : "=&v"(v) : "v"(p) : "memory"); return v; }
DI void st128_sc1(char* p, u32x4 v) { asm volatile("global_store_dwordx4 %0, %1, off sc1\n\ts_nop 1" ::"v"(p), "v"(v) : "memory"); }
DI void st64_sc1(char* p, u32x2 v) { asm volatile("global_store_dwordx2 %0, %1, off sc1" ::"v"(p), "v"(v) : "memory"); }
#define WAITN_BAR(N) asm volatile("s_waitcnt vmcnt(%0) lgkmcnt(0)\n\ts_barrier" ::"n"(N) : "memory")
#define LGKM_BAR() asm volatile("s_waitcnt lgkmcnt(0)\n\ts_barrier" ::: "memory")
#define LGKM0() asm volatile("s_waitcnt lgkmcnt(0)" ::: "memory")
#define MEMFENCE() asm volatile("" ::: "memory")
#define SCHED0() __builtin_amdgcn_sched_barrier(0)

DI bf16x8 ld128(ldsp p) { return *(const LAS bf16x8*)p; }
DI s16x4 tr1(ldsp p) { return __builtin_amdgcn_ds_read_tr16_b64_v4i16((LAS s16x4*)p); }
DI bf16x8 cat(s16x4 a, s16x4 b) { return __builtin_bit_cast(bf16x8, __builtin_shufflevector(a, b, 0, 1, 2, 3, 4, 5, 6, 7)); }
DI bf16x8 tr2(ldsp p0, ldsp p1) { return cat(tr1(p0), tr1(p1)); }
DI f32x4 mfma32(bf16x8 a, bf16x8 b, f32x4 c) { return __builtin_amdgcn_mfma_f32_16x16x32_bf16(a, b, c, 0, 0, 0); }
DI bf16x8 asb(u32x4 v) { return __builtin_bit_cast(bf16x8, v); }
DI unsigned pk2(float a, float b) {
  bf16x2 v;
  v[0] = (__bf16)a;
  v[1] = (__bf16)b;
  return __builtin_bit_cast(unsigned, v);
}
DI void swap16(float& a, float& b) {
  auto r = __builtin_amdgcn_permlane16_swap(f2u(a), f2u(b), false, false);
  a = u2f(r[0]); b = u2f(r[1]);
}
DI void swap32(float& a, float& b) {
  auto r = __builtin_amdgcn_permlane32_swap(f2u(a), f2u(b), false, false);
  a = u2f(r[0]); b = u2f(r[1]);
}
template <int CTRL> DI float dpp(float x) { return u2f(__builtin_amdgcn_update_dpp(0u, f2u(x), CTRL, 0xf, 0xf, false)); }
DI float fmx(float a, float b) { return __builtin_fmaxf(a, b); }
DI float fmn(float a, float b) { return __builtin_fminf(a, b); }
DI float rawmax(float a, float b) { float r; asm("v_max_f32 %0, %1, %2" : "=v"(r) : "v"(a), "v"(b)); return r; }
DI float opq(float x) { asm("" : "+v"(x)); return x; }
DI f32x2 lo2(f32x4 v) { return __builtin_shufflevector(v, v, 0, 1); }
DI f32x2 hi2(f32x4 v) { return __builtin_shufflevector(v, v, 2, 3); }
DI f32x2 sp2(float x) { return f32x2{x, x}; }
DI f32x2 fma2(f32x2 a, f32x2 b, f32x2 c) { return __builtin_elementwise_fma(a, b, c); }
DI int strow(int s, int nrow) { int ri = s < 95 ? s : s - 1; return ri < nrow ? ri : nrow - 1; }
DI int rs_of(int rg) { return (512 * rg) / 3; }
DI int rg_of(int row) { return (3 * row + 2) >> 9; }
DI bool all_ok(bool ok) { return __builtin_amdgcn_ballot_w64(ok) == ~0ull; }

DI void wait_flags(const char* p, bool need, u32x2 ep) {
  for (unsigned spins = 0; spins < SPIN_MAX; ++spins) {
    u32x2 v = ld64_sc1(p);
    if (all_ok(!need || (v[0] == ep[0] && v[1] == ep[1]))) break;
    __builtin_amdgcn_s_sleep(POLLS);
  }
}

DI f32x2 ex2(f32x2 v) { return f32x2{__builtin_amdgcn_exp2f(v[0]), __builtin_amdgcn_exp2f(v[1])}; }
DI unsigned pk2v(f32x2 v) { return pk2(v[0], v[1]); }
DI void featmap2(f32x2 a01, f32x2 a23, f32x2 b01, f32x2 b23, f32x2 e4, bool livea, bool liveb, int g, unsigned (&oa)[5], unsigned (&ob)[5]) {
  float a0 = fmx(fmx(fmx(a01[0], a01[1]), a23[0]), fmx(a23[1], e4[0]));
  float a1 = -fmn(fmn(fmn(a01[0], a01[1]), a23[0]), fmn(a23[1], e4[0]));
  float b0 = fmx(fmx(fmx(b01[0], b01[1]), b23[0]), fmx(b23[1], e4[1]));
  float b1 = -fmn(fmn(fmn(b01[0], b01[1]), b23[0]), fmn(b23[1], e4[1]));
  swap16(a0, a1); swap16(b0, b1);
  float ca = rawmax(a0, a1), cb = rawmax(b0, b1);
  swap32(ca, cb);
  float d0 = rawmax(ca, cb), d1 = d0;
  swap32(d0, d1);
  float Ma = d0, Na = d0, Mb = d1, Nb = d1;
  swap16(Ma, Na); swap16(Mb, Nb);
  const f32x2 pa01 = ex2(a01 - sp2(Ma)), pa23 = ex2(a23 - sp2(Ma)), na01 = ex2(-a01 - sp2(Na)), na23 = ex2(-a23 - sp2(Na));
  const f32x2 pb01 = ex2(b01 - sp2(Mb)), pb23 = ex2(b23 - sp2(Mb)), nb01 = ex2(-b01 - sp2(Nb)), nb23 = ex2(-b23 - sp2(Nb));
  const float pa4 = __builtin_amdgcn_exp2f(e4[0] - Ma), na4 = __builtin_amdgcn_exp2f(-e4[0] - Na);
  const float pb4 = __builtin_amdgcn_exp2f(e4[1] - Mb), nb4 = __builtin_amdgcn_exp2f(-e4[1] - Nb);
  f32x2 t;
  t = pa01 + pa23; a0 = (t[0] + t[1]) + pa4;
  t = na01 + na23; a1 = (t[0] + t[1]) + na4;
  t = pb01 + pb23; b0 = (t[0] + t[1]) + pb4;
  t = nb01 + nb23; b1 = (t[0] + t[1]) + nb4;
  swap16(a0, a1); swap16(b0, b1);
  ca = a0 + a1; cb = b0 + b1;
  swap32(ca, cb);
  d0 = __builtin_amdgcn_rcpf(ca + cb);
  if (!(g < 2 ? livea : liveb)) d0 = 0.f;
  d1 = d0;
  swap32(d0, d1);
  Ma = d0; Na = d0; Mb = d1; Nb = d1;
  swap16(Ma, Na); swap16(Mb, Nb);
  oa[0] = pk2v(pa01 * sp2(Ma)); oa[1] = pk2v(pa23 * sp2(Ma));
  oa[2] = pk2v(na01 * sp2(Na)); oa[3] = pk2v(na23 * sp2(Na));
  oa[4] = pk2(pa4 * Ma, na4 * Na);
  ob[0] = pk2v(pb01 * sp2(Mb)); ob[1] = pk2v(pb23 * sp2(Mb));
  ob[2] = pk2v(nb01 * sp2(Nb)); ob[3] = pk2v(nb23 * sp2(Nb));
  ob[4] = pk2(pb4 * Mb, nb4 * Nb);
}

struct P2Ctx { unsigned vB, vR; int c0; };
DI unsigned p2_vB(int w, int lane) {
  const int l3 = lane >> 3, l7 = lane & 7;
  int fb = ((l3 >> 1) & 1) | ((w & 1) << 1);
  int ch = (((l7 >> 1) ^ fb) << 1) | (l7 & 1);
  return l3 * 1280 + ch * 16;
}
DI int p2_rot(int s, int rot) { int r = s + rot; return r >= 12 ? r - 12 : r; }
DI void p2_issueB_down(u32x4 srd_wd, unsigned vB, int c0, int w, unsigned smb, int s, int rot) {
  const int sr = p2_rot(s, rot);
  dma_b(srd_wd, vB, (128 * sr + 8 * w) * 1280 + c0 * 2, smb + P2_B + (s % 3) * 16384 + w * 1024);
  dma_b(srd_wd, vB, (128 * sr + 8 * w + 64) * 1280 + c0 * 2, smb + P2_B + (s % 3) * 16384 + w * 1024 + 8192);
}

DI void phase2(ldsp sm, const char* x, const char* wo, const u32x4 srd_wd, char* ws, char* out, int rt, int ct, int rot, int w, int lane, u32x2 ep) {
  const int r0 = rt * 128, c0 = ct * 64;
  const int g = lane >> 4, fr = lane & 15, l3 = lane >> 3, l7 = lane & 7;
  const int kh = w & 1, nh = (w >> 1) & 1, mh = w >> 2;
  const unsigned smb = (unsigned)(size_t)sm;
  const u32x4 srd_h = make_srd(ws + WS_H, S_ * DF_ * 2);
  const u32x4 srd_wo = make_srd(wo, D_ * D_ * 2);
  const u32x4 srd_om = make_srd(ws + WS_OM, NH * S_ * 32);
  const u32x4 srd_or = make_srd(ws + WS_OR, NH * S_ * 8);
  const unsigned vB = p2_vB(w, lane);
  const unsigned vA = l3 * 3072 + ((l7 ^ ((4 * (w & 1) + (l3 >> 1)) & 7)) * 16);
  const unsigned vR = vB - l3 * 1280 + (20 * (lane >> 5) + 16 + (l3 & 3)) * 1280;
  const unsigned vL = lane * 16;
  const int rgl = rg_of(r0), rgh = rg_of(r0 + 127);

#define P2_PIECE(s, p) do { \
    constexpr int i_ = (p) < 2 ? (p) : (p) - 2; \
    if ((s) < 12) { \
      if ((p) < 2) dma_b(srd_wd, vB, (128 * p2_rot((s), rot) + 8 * w + 64 * i_) * 1280 + c0 * 2, smb + P2_B + ((s) % 3) * 16384 + i_ * 8192 + w * 1024); \
      else dma_b_sc1(srd_h, vA, (r0 + 8 * w + 64 * (i_ & 1)) * 3072 + p2_rot((s), rot) * 256 + (i_ >> 1) * 128, smb + P2_A + ((s) % 3) * 32768 + (i_ >> 1) * 16384 + (i_ & 1) * 8192 + w * 1024); \
    } else if ((s) < 16) { \
      if ((p) < 2) dma_b(srd_wo, vB, (160 * ((s) - 12) + 20 * ((w + 8 * i_) >> 1) + 8 * (w & 1)) * 1280 + c0 * 2, smb + P2_B + ((s) % 3) * 16384 + i_ * 8192 + w * 1024); \
      else dma_b_sc1(srd_om, vL, ((8 * ((s) - 12) + 2 * i_ + (w >> 2)) * S_ + r0) * 32 + (w & 3) * 1024, smb + P2_A + ((s) % 3) * 32768 + i_ * 8192 + w * 1024); \
    } else { \
      if ((p) < 2) dma_b(srd_wo, vR, (40 * (w + 8 * i_)) * 1280 + c0 * 2, smb + P2_B + ((s) % 3) * 16384 + i_ * 8192 + w * 1024); \
      else dma_b_sc1(srd_or, vL, ((8 * i_ + w) * S_ + r0) * 8, smb + P2_A + ((s) % 3) * 32768 + i_ * 8192 + w * 1024); \
    } } while (0)

  const int aD = P2_A + kh * 16384 + (64 * mh + fr) * 128 + ((g ^ ((fr >> 1) & 7)) * 16);
  const int aM = P2_A + (4 * kh + (g >> 1)) * 4096 + (64 * mh + fr) * 32 + (g & 1) * 16;
  const int aR = P2_A + (16 * kh + 2 * g) * 1024 + (64 * mh + fr) * 8;
  int bO[2];
  {
    const int q = fr >> 2, p = lane & 3;
    const int f = ((q >> 1) & 1) | ((g & 1) << 1);
#pragma unroll
    for (int j = 0; j < 2; ++j) bO[j] = P2_B + (64 * kh + 8 * g + q) * 128 + (((2 * nh + j) ^ f) * 32) + p * 8;
  }
  f32x4 acc[4][2];
#pragma unroll
  for (int m = 0; m < 4; ++m)
#pragma unroll
    for (int j = 0; j < 2; ++j) acc[m][j] = f32x4{0.f, 0.f, 0.f, 0.f};

  STAMPAT(8);
  if (w == 0) wait_flags(ws + WS_HF + (size_t)(lane < 16 ? rgl : rgh) * 128 + (lane & 15) * 8, lane < 32 && (lane & 15) < 12, ep);
  WAITN_BAR(0);
  STAMPAT(9);
#define P2_A3(s) do { P2_PIECE(s, 2); P2_PIECE(s, 3); P2_PIECE(s, 4); P2_PIECE(s, 5); } while (0)
  P2_A3(0); P2_A3(1); P2_A3(2);
  u32x4 xr[2];
  bf16x8 fa[2][8];
  s16x4 fb[2][8];
#define P2_RD(s, i) do { \
    if ((i) < 8) fb[(s) & 1][i] = tr1(sm + ((s) % 3) * 16384 + bO[((i) >> 1) & 1] + ((i) >> 2) * 4096 + ((i) & 1) * 512); \
    else if ((i) < 16) { \
      constexpr int sl_ = ((i) - 8) >> 2, m_ = ((i) - 8) & 3; \
      if ((s) < 12) fa[(s) & 1][((i) - 8) & 7] = ld128(sm + ((s) % 3) * 32768 + (sl_ ? (aD ^ 64) : aD) + m_ * 2048); \
      else if ((s) < 16) fa[(s) & 1][((i) - 8) & 7] = ld128(sm + ((s) % 3) * 32768 + aM + sl_ * 8192 + m_ * 512); \
      else fa[(s) & 1][((i) - 8) & 7] = cat(*(const LAS s16x4*)(sm + ((s) % 3) * 32768 + aR + sl_ * 8192 + m_ * 128), *(const LAS s16x4*)(sm + ((s) % 3) * 32768 + aR + sl_ * 8192 + 1024 + m_ * 128)); } } while (0)
#define P2_MM(s, i) acc[((i) >> 1) & 3][(i) & 1] = mfma32(cat(fb[(s) & 1][((i) >> 3) * 4 + 2 * ((i) & 1)], fb[(s) & 1][((i) >> 3) * 4 + 2 * ((i) & 1) + 1]), fa[(s) & 1][((i) >> 3) * 4 + (((i) >> 1) & 3)], acc[((i) >> 1) & 3][(i) & 1])
#define P2_GROUP(s, q) do { \
    if ((s) >= 1 && (s) + 2 < 17 && (s) != 12 && !(((s) == 10 || (s) == 11) && (q) >= 2)) P2_PIECE((s) + 2, q); \
    SCHED0(); \
    if ((s) < 17) { P2_RD(s, 3 * (q)); P2_RD(s, 3 * (q) + 1); P2_RD(s, 3 * (q) + 2); } \
    SCHED0(); \
    if ((s) >= 1 && (s) != 12) { P2_MM((s) - 1, ((q) * 8) / 3); P2_MM((s) - 1, ((q) * 8) / 3 + 1); if (((q) * 8) / 3 + 2 < (((q) + 1) * 8) / 3) P2_MM((s) - 1, ((q) * 8) / 3 + 2); } \
    SCHED0(); } while (0)
#define P2_STAGE(s) do { \
    SCHED0(); \
    if ((s) < 17) WAITN_BAR(((s) == 0 || (s) == 12) ? 8 : (((s) == 1 || (s) == 13) ? 4 : ((s) == 11 ? 2 : ((s) < 16 ? 6 : 0)))); \
    SCHED0(); \
    P2_GROUP(s, 0); P2_GROUP(s, 1); P2_GROUP(s, 2); P2_GROUP(s, 3); P2_GROUP(s, 4); P2_GROUP(s, 5); } while (0)
  P2_STAGE(0); P2_STAGE(1); P2_STAGE(2); P2_STAGE(3); P2_STAGE(4); P2_STAGE(5); P2_STAGE(6); P2_STAGE(7); P2_STAGE(8);
  P2_STAGE(9); P2_STAGE(10); P2_STAGE(11);
  SCHED0();
#pragma unroll
  for (int i = 0; i < 16; ++i) P2_MM(11, i);
  SCHED0();
  STAMPAT(10);
  if (w == 0) wait_flags(ws + WS_OF + (size_t)(lane < 16 ? rgl : rgh) * 128 + (lane & 15) * 8, lane < 32 && (lane & 15) < 8, ep);
  LGKM_BAR();
  STAMPAT(11);
  P2_PIECE(14, 0); P2_PIECE(14, 1);
  P2_A3(12); P2_A3(13); P2_A3(14);
  P2_STAGE(12); P2_STAGE(13); P2_STAGE(14); P2_STAGE(15);
  const int orow = 64 * mh + 32 * kh + (lane >> 2), ocol = c0 * 2 + nh * 64 + (lane & 3) * 16;
#pragma unroll
  for (int i = 0; i < 2; ++i)
    asm volatile("global_load_dwordx4 %0, %1, off" : "=&v"(xr[i]) : "v"(x + (size_t)(r0 + orow + 16 * i) * 1280 + ocol) : "memory");
  P2_STAGE(16); P2_STAGE(17);
  SCHED0();
  STAMPAT(12);
  f32x4 keep[2][2];
  {
    ldsp ex = sm + w * 4096 + lane * 16;
#pragma unroll
    for (int i = 0; i < 2; ++i)
#pragma unroll
      for (int j = 0; j < 2; ++j) {
        f32x4 snd;
#pragma unroll
        for (int e = 0; e < 4; ++e) {
          const float lo_ = acc[i][j][e], hi_ = acc[2 + i][j][e];
          snd[e] = kh ? lo_ : hi_;
          keep[i][j][e] = kh ? hi_ : lo_;
        }
        *(LAS f32x4*)(ex + (i * 2 + j) * 1024) = snd;
      }
  }
  LGKM_BAR();
  {
    ldsp ex = sm + (w ^ 1) * 4096 + lane * 16;
    ldsp tp = sm + 65536 + w * 4608;
#pragma unroll
    for (int i = 0; i < 2; ++i)
#pragma unroll
      for (int j = 0; j < 2; ++j) {
        const f32x4 e = *(const LAS f32x4*)(ex + (i * 2 + j) * 1024);
        *(LAS f32x4*)(tp + (i * 16 + fr) * 144 + j * 64 + g * 16) = keep[i][j] + e;
      }
    asm volatile("" : "+v"(xr[0]), "+v"(xr[1]));
#pragma unroll
    for (int i = 0; i < 2; ++i) {
      const f32x4 s0 = *(const LAS f32x4*)(tp + (i * 16 + (lane >> 2)) * 144 + (lane & 3) * 32);
      const f32x4 s1 = *(const LAS f32x4*)(tp + (i * 16 + (lane >> 2)) * 144 + (lane & 3) * 32 + 16);
      u32x4 o;
#pragma unroll
      for (int e = 0; e < 2; ++e) {
        o[e] = pk2(s0[2 * e] + u2f(xr[i][e] << 16), s0[2 * e + 1] + u2f(xr[i][e] & 0xffff0000u));
        o[2 + e] = pk2(s1[2 * e] + u2f(xr[i][2 + e] << 16), s1[2 * e + 1] + u2f(xr[i][2 + e] & 0xffff0000u));
      }
      *(u32x4*)(out + (size_t)(r0 + orow + 16 * i) * 1280 + ocol) = o;
    }
  }
}

template <int MT, bool GU>
DI u32x2 phase1(ldsp sm, const char* x, const char* wqkv, const char* wphi, const char* wg, const char* wu, const u32x4 srd_wd,
                char* ws, int rg, int ct, int c0p2, int rot, int w, int lane) {
  STAMPAT(0);
  constexpr int mh = (MT == 6) ? 0 : 1;
  const int wn = w & 3;
  const int r0 = rs_of(rg), nrow = rs_of(rg + 1) - r0;
  const unsigned smb = (unsigned)(size_t)sm;
  const int g = lane >> 4, fr = lane & 15;

  const u32x4 srdx = make_srd(x, S_ * D_ * 2);
  unsigned voffA[3];
#pragma unroll
  for (int c = 0; c < 3; ++c) {
    int slot = 8 * (w + 8 * c) + (lane >> 3);
    int ch = (lane & 7) ^ ((slot >> 1) & 7);
    int ri = slot < 95 ? slot : slot - 1;
    ri = ri < nrow ? ri : nrow - 1;
    voffA[c] = (unsigned)((r0 + ri) * 1280 + ch * 16);
  }
  if (w >= 6) voffA[2] = 0x40000000u;
  const unsigned long long FULL = ~0ull, PART = 0x00FFFFFFFFFFFFFFull;
  const unsigned long long mask1 = (w == 3) ? PART : FULL, mask2 = (w == 5) ? PART : FULL;
  const unsigned adst0 = smb + L_A + w * 1024;
  u32x4 srd0, srd1;
  unsigned voff0, voff1;
  constexpr unsigned SSTEP = GU ? 32 * 3072 : 32 * 3840;
  if (GU) {
    srd0 = make_srd(wg, D_ * DF_ * 2);
    srd1 = make_srd(wu, D_ * DF_ * 2);
    int krow = 4 * w + (lane >> 4);
    int chp = lane & 15;
    int f = (krow & 3) | (((krow >> 3) & 1) << 2);
    int ch = (((chp >> 1) ^ f) << 1) | (chp & 1);
    voff0 = voff1 = krow * 3072 + ct * 256 + ch * 16;
  } else {
    srd0 = srd1 = make_srd(wqkv, D_ * 3 * D_ * 2);
    unsigned vo[2];
#pragma unroll
    for (int c = 0; c < 2; ++c) {
      int cidx = (w + 8 * c) * 64 + lane;
      int rho = cidx / 30, cc = cidx % 30;
      int seg = cc / 10, ch = cc % 10;
      int k = (rho & 16) | ((rho & 4) << 1) | ((rho & 8) >> 1) | (rho & 3);
      vo[c] = (cidx < 960) ? (unsigned)(k * 3840 + seg * 1280 + ct * 160 + ch * 16) : 0x40000000u;
    }
    voff0 = vo[0];
    voff1 = vo[1];
  }
  const unsigned bdst0 = smb + L_B + w * 1024;
#define KROT (((rg%6)*5/3+ct*2)%10)
  const unsigned krot = KROT;
#define RA(so) ((so) + krot * 128u >= 1280u ? (so) + krot * 128u - 1280u : (so) + krot * 128u)
#define RB(so) ((so) + krot * 2u * SSTEP >= 20u * SSTEP ? (so) + krot * 2u * SSTEP - 20u * SSTEP : (so) + krot * 2u * SSTEP)
#define DMA_B0(soff, dst) dma_b(srd0, voff0, RB(soff), (dst))
#define DMA_B1(soff, dst) dma_b(srd1, voff1, RB(soff), (dst))
#define DMA_A0(soff, dst) dma_b(srdx, voffA[0], RA(soff), (dst))
#define DMA_A1(soff, dst) dma_b_mask(srdx, voffA[1], RA(soff), (dst), mask1)
#define DMA_A2(soff, dst) dma_b_mask(srdx, voffA[2], RA(soff), (dst), mask2)
  const unsigned a2base = (w >= 6) ? smb + L_DUMMY : adst0 + 16384;
  const unsigned a2mul = (w >= 6) ? 0u : 1u;
  unsigned wraw[16];
  if (!GU) {
    const char* wp = wphi + (ct * 4 + wn) * 800;
    const char* p0 = wp + (5 * (fr >> 2) + (fr & 3)) * 2;
    const char* p1 = wp + (5 * (fr >> 2) + 4) * 2;
    const char* q0 = p0 + g * 160;
    const char* q1 = p1 + g * 160;
#pragma unroll
    for (int j = 0; j < 4; ++j) {
      asm volatile("global_load_ushort %0, %1, off offset:%2" : "=&v"(wraw[j]) : "v"(q0), "n"(j * 40) : "memory");
      asm volatile("global_load_ushort %0, %1, off offset:%2" : "=&v"(wraw[4 + j]) : "v"(p0), "n"(640 + j * 40) : "memory");
      asm volatile("global_load_ushort %0, %1, off offset:%2" : "=&v"(wraw[8 + j]) : "v"(q1), "n"(j * 40) : "memory");
      asm volatile("global_load_ushort %0, %1, off offset:%2" : "=&v"(wraw[12 + j]) : "v"(p1), "n"(640 + j * 40) : "memory");
    }
  }
  DMA_B0(0, bdst0); DMA_B1(0, bdst0 + 8192);
  DMA_A0(0, adst0); DMA_A1(0, adst0 + 8192);
  DMA_A2(0, a2base);
  DMA_B0(SSTEP, bdst0 + B_SLOT); DMA_B1(SSTEP, bdst0 + B_SLOT + 8192);
  DMA_B0(2 * SSTEP, bdst0 + 2 * B_SLOT); DMA_B1(2 * SSTEP, bdst0 + 2 * B_SLOT + 8192);
  DMA_A0(128, adst0 + A_SLOT); DMA_A1(128, adst0 + A_SLOT + 8192);
  DMA_A2(128, a2base + a2mul * A_SLOT);
  DMA_B0(3 * SSTEP, bdst0 + 3 * B_SLOT); DMA_B1(3 * SSTEP, bdst0 + 3 * B_SLOT + 8192);
  DMA_B0(4 * SSTEP, bdst0 + 4 * B_SLOT); DMA_B1(4 * SSTEP, bdst0 + 4 * B_SLOT + 8192);
  DMA_A0(256, adst0 + 2 * A_SLOT); DMA_A1(256, adst0 + 2 * A_SLOT + 8192);
  if (w == 0 && lane < 48) {
    int rr = lane >> 3;
    *(LAS u32x4*)(sm + L_A + (rr >> 1) * A_SLOT + ((rr & 1) ? 175 : 95) * 128 + (lane & 7) * 16) =
        u32x4{0x3F803F80u, 0x3F803F80u, 0x3F803F80u, 0x3F803F80u};
  }
  const int bid = __builtin_amdgcn_workgroup_id_x();
  if (w == 1) {
    unsigned long long nn = __builtin_amdgcn_s_memrealtime();
    u32x2 nv = u32x2{(unsigned)nn, (unsigned)(nn >> 32)};
    if (lane == 0) {
      st64_sc1(ws + WS_REQ + bid * 64, nv);
      *(LAS u32x2*)(sm + L_NONCE) = nv;
    }
  }

  const int aoff0 = L_A + mh * 6 * 2048 + fr * 128 + ((g ^ ((fr >> 1) & 7)) * 16);
  const int aoff1 = aoff0 ^ 64;
  int boff[4];
  constexpr int TSTEP = GU ? 1024 : 3840;
  {
    const int q = fr >> 2, p = lane & 3;
    if (GU) {
      int f = q | ((g & 1) << 2);
#pragma unroll
      for (int j = 0; j < 4; ++j)
        boff[j] = L_B + (j >> 1) * 8192 + (8 * g + q) * 256 + (((2 * wn + (j & 1)) ^ f) * 32) + p * 8;
    } else {
      int rho = 16 * (g >> 1) + 4 * (g & 1) + q;
#pragma unroll
      for (int j = 0; j < 3; ++j) boff[j] = L_B + rho * 480 + (20 * j + 5 * wn + p) * 8;
      boff[3] = L_B + rho * 480 + (20 * (p == 3 ? 0 : p) + 5 * wn + 4) * 8;
    }
  }

  f32x4 acc[MT][4];
#pragma unroll
  for (int m = 0; m < MT; ++m)
#pragma unroll
    for (int j = 0; j < 4; ++j) acc[m][j] = f32x4{0.f, 0.f, 0.f, 0.f};
  bf16x8 fa0[MT], fa1[MT];
  s16x4 fb0[8], fb1[8];

  STAMPAT(1);
  WAITN_BAR(13);
  STAMPAT(2);
  unsigned wpk[8];
  if (!GU) {
    asm volatile("" : "+v"(wraw[0]), "+v"(wraw[1]), "+v"(wraw[2]), "+v"(wraw[3]), "+v"(wraw[4]), "+v"(wraw[5]), "+v"(wraw[6]), "+v"(wraw[7]),
                 "+v"(wraw[8]), "+v"(wraw[9]), "+v"(wraw[10]), "+v"(wraw[11]), "+v"(wraw[12]), "+v"(wraw[13]), "+v"(wraw[14]), "+v"(wraw[15]));
#pragma unroll
    for (int i = 0; i < 8; ++i) {
      wpk[i] = wraw[2 * i] | (wraw[2 * i + 1] << 16);
      if (i >= 4 && (fr & 3) != 0) wpk[i] = 0u;
    }
    asm volatile("" : "+v"(wpk[0]), "+v"(wpk[1]), "+v"(wpk[2]), "+v"(wpk[3]), "+v"(wpk[4]), "+v"(wpk[5]), "+v"(wpk[6]), "+v"(wpk[7]));
  }
#pragma unroll
  for (int m = 0; m < MT; ++m) fa0[m] = ld128(sm + aoff0 + m * 2048);
#pragma unroll
  for (int j = 0; j < 8; ++j) fb0[j] = tr1(sm + boff[j >> 1] + (j & 1) * TSTEP);
  MEMFENCE();

  constexpr int NM = MT * 4;
  constexpr int DS = (MT == 6) ? 6 : 5;
#define MMA(i, FA, FB) acc[(i) >> 2][(i) & 3] = mfma32(cat(FB[2 * ((i) & 3)], FB[2 * ((i) & 3) + 1]), FA[(i) >> 2], acc[(i) >> 2][(i) & 3])
#define BADDR(sl, i) (sm + (sl) * B_SLOT + boff[(i) >> 1] + ((i) & 1) * TSTEP)
  u32x4 ackv, stv[MT];
  const char* ackp = ws + WS_ACK + bid * 64;
#define STP(m) (ws + WS_STAT + (size_t)(r0 + strow(mh * 96 + (m) * 16 + fr, nrow)) * 16)
#pragma unroll
  for (int t = 0; t < 10; ++t) {
    SCHED0();
    WAITN_BAR(t < 8 ? 11 : (t == 8 ? 7 : 1 + MT));
    SCHED0();
#pragma unroll
    for (int i = 0; i < NM; ++i) {
      MMA(i, fa0, fb0);
      SCHED0();
      if (i < 8) fb1[i] = tr1(BADDR((2 * t + 1) % 5, i));
      else if (i < 8 + MT) fa1[i - 8] = ld128(sm + (t % 3) * A_SLOT + aoff1 + (i - 8) * 2048);
      if (t < 8) {
        if (i == 2) { if (MT == 6) DMA_A2((t + 2) * 128, adst0 + 16384 + ((t + 2) % 3) * A_SLOT); else DMA_A2((t + 2) * 128, a2base + a2mul * (((t + 2) % 3) * A_SLOT)); }
        if (i == 2 + DS) DMA_B0((2 * t + 5) * SSTEP, bdst0 + ((2 * t) % 5) * B_SLOT);
        if (i == 2 + 2 * DS) DMA_B1((2 * t + 5) * SSTEP, bdst0 + ((2 * t) % 5) * B_SLOT + 8192);
      }
      SCHED0();
    }
    if (t < 9) WAITN_BAR(t < 8 ? 9 : 2); else LGKM_BAR();
    SCHED0();
#pragma unroll
    for (int i = 0; i < NM; ++i) {
      MMA(i, fa1, fb1);
      SCHED0();
      if (t < 9) {
        if (i < 8) fb0[i] = tr1(BADDR((2 * t + 2) % 5, i));
        else if (i < 8 + MT) fa0[i - 8] = ld128(sm + ((t + 1) % 3) * A_SLOT + aoff0 + (i - 8) * 2048);
      }
      if (t < 7) {
        if (i == 1) DMA_B0((2 * t + 6) * SSTEP, bdst0 + ((2 * t + 1) % 5) * B_SLOT);
        if (i == 1 + DS) DMA_B1((2 * t + 6) * SSTEP, bdst0 + ((2 * t + 1) % 5) * B_SLOT + 8192);
        if (i == 1 + 2 * DS) DMA_A0((t + 3) * 128, adst0 + (t % 3) * A_SLOT);
        if (i == 1 + 3 * DS) DMA_A1((t + 3) * 128, adst0 + (t % 3) * A_SLOT + 8192);
      }
      if (i == 4 && t == 8) {
        asm volatile("global_load_dwordx4 %0, %1, off sc1" : "=&v"(ackv) : "v"(ackp) : "memory");
#pragma unroll
        for (int m = 0; m < MT; ++m) asm volatile("global_load_dwordx4 %0, %1, off sc1" : "=&v"(stv[m]) : "v"(STP(m)) : "memory");
      }
      SCHED0();
    }
  }
  SCHED0();
  STAMPAT(3);

  if (fr == 15) {
#pragma unroll
    for (int j = 0; j < 4; ++j) *(LAS f32x4*)(sm + L_CS + w * 256 + j * 64 + g * 16) = acc[MT - 1][j];
  }
  if (MT == 6) asm volatile("s_waitcnt vmcnt(0) lgkmcnt(0)" : "+v"(ackv), "+v"(stv[0]), "+v"(stv[1]), "+v"(stv[2]), "+v"(stv[3]), "+v"(stv[4]), "+v"(stv[MT - 1]) :: "memory");
  else asm volatile("s_waitcnt vmcnt(0) lgkmcnt(0)" : "+v"(ackv), "+v"(stv[0]), "+v"(stv[1]), "+v"(stv[2]), "+v"(stv[3]), "+v"(stv[4]) :: "memory");
  f32x4 cs[4];
#pragma unroll
  for (int j = 0; j < 4; ++j) cs[j] = *(const LAS f32x4*)(sm + L_CS + w * 256 + j * 64 + g * 16);
  {
    const u32x2 nv = *(const LAS u32x2*)(sm + L_NONCE);
    for (unsigned spins = 0; spins < SPIN_MAX; ++spins) {
      bool ok = (ackv[0] == nv[0]) && (ackv[1] == nv[1]);
#pragma unroll
      for (int m = 0; m < MT; ++m) ok = ok && (stv[m][2] == ackv[2]) && (stv[m][3] == ackv[3]);
      if (all_ok(ok)) break;
      __builtin_amdgcn_s_sleep(2);
#pragma unroll
      for (int m = 0; m < MT; ++m) asm volatile("global_load_dwordx4 %0, %1, off sc1" : "=&v"(stv[m]) : "v"(STP(m)) : "memory");
      ackv = ld128_sc1(ackp);
#pragma unroll
      for (int m = 0; m < MT; ++m) asm volatile("" : "+v"(stv[m]) :: "memory");
    }
  }
  const u32x2 ep = u32x2{ackv[2], ackv[3]};
  if (w == 0 && lane == 0) st64_sc1(ws + WS_DONE + bid * 64, ep);
  if (GU) {
    const unsigned vB = p2_vB(w, lane);
    p2_issueB_down(srd_wd, vB, c0p2, w, smb, 0, rot);
    p2_issueB_down(srd_wd, vB, c0p2, w, smb, 1, rot);
    p2_issueB_down(srd_wd, vB, c0p2, w, smb, 2, rot);
  }
  float rstd[MT];
#pragma unroll
  for (int m = 0; m < MT; ++m) {
    const f32x2 nm = sp2(opq(-u2f(stv[m][0])));
    rstd[m] = opq(u2f(stv[m][1]));
#pragma unroll
    for (int j = 0; j < 4; ++j) {
      f32x2 l = fma2(nm, lo2(cs[j]), lo2(acc[m][j])), h = fma2(nm, hi2(cs[j]), hi2(acc[m][j]));
      acc[m][j] = f32x4{l[0], l[1], h[0], h[1]};
    }
  }
  STAMPAT(4);

  if (GU) {
    const float K0 = -2.0f * 0.7978845608028654f * 1.4426950408889634f;
    const float K1c = K0 * 0.044715f;
    ldsp stg = sm + w * 7680;
    char* hg = ws + WS_H + ct * 256 + wn * 64 + (lane & 3) * 16;
#pragma unroll
    for (int m = 0; m <= MT; ++m) {
      if (m < MT) {
        const float r = rstd[m], r2 = r * r;
        const f32x2 c0 = sp2(opq(K0 * r)), c1 = sp2(opq(K1c * r2 * r)), ir2 = sp2(opq(__builtin_amdgcn_rcpf(r2)));
        u32x2 hp[2];
#pragma unroll
        for (int j = 0; j < 2; ++j) {
#pragma unroll
          for (int q = 0; q < 2; ++q) {
            const f32x2 a = q ? hi2(acc[m][j]) : lo2(acc[m][j]), b = q ? hi2(acc[m][j + 2]) : lo2(acc[m][j + 2]);
            const f32x2 z = a * fma2(a * a, c1, c0);
            const f32x2 e = f32x2{__builtin_amdgcn_exp2f(z[0]), __builtin_amdgcn_exp2f(z[1])};
            const f32x2 d = fma2(e, ir2, ir2);
            const f32x2 hv = (a * b) * f32x2{__builtin_amdgcn_rcpf(d[0]), __builtin_amdgcn_rcpf(d[1])};
            hp[j][q] = pk2(hv[0], hv[1]);
          }
        }
        *(LAS u32x2*)(stg + (m * 16 + fr) * 80 + g * 8) = hp[0];
        *(LAS u32x2*)(stg + (m * 16 + fr) * 80 + 32 + g * 8) = hp[1];
      }
      if (m >= 1) {
        const int rl = (m - 1) * 16 + (lane >> 2);
        u32x4 v = *(const LAS u32x4*)(stg + rl * 80 + (lane & 3) * 16);
        const int sl = mh * 96 + rl;
        const int ri = sl < 95 ? sl : sl - 1;
        if (sl != 95 && ri < nrow) st128_sc1(hg + (size_t)(r0 + ri) * 3072, v);
      }
    }
    STAMPAT(5);
    WAITN_BAR(0);
    if (w == 0 && lane == 0) st64_sc1(ws + WS_HF + rg * 128 + ct * 8, ep);
    STAMPAT(6);
  } else {
    const int head = ct * 4 + wn;
    bf16x8 wq[2], wk[2];
#pragma unroll
    for (int ft = 0; ft < 2; ++ft) {
      wq[ft] = asb(u32x4{wpk[4 * ft], wpk[4 * ft + 1], g == 0 ? wpk[4 * ft + 2] : 0u, g == 0 ? wpk[4 * ft + 3] : 0u});
      wk[ft] = asb(u32x4{wpk[4 * ft], wpk[4 * ft + 1], g == 1 ? wpk[4 * ft + 2] : 0u, g == 1 ? wpk[4 * ft + 3] : 0u});
    }
    const unsigned tag = ep[0] ^ (ep[1] * 0x9E3779B1u);
    const float L2E = 1.4426950408889634f;
    const f32x4 z4 = f32x4{0.f, 0.f, 0.f, 0.f};
    bool live[6];
    u32x2 rem[6];
#pragma unroll
    for (int m = 0; m < 6; ++m) {
      int s = mh * 96 + m * 16 + fr;
      int ri = s < 95 ? s : s - 1;
      live[m] = (m < MT) && (s != 95) && (ri < nrow);
      if (m < MT) rem[m] = u32x2{pk2(acc[m][3][0], acc[m][3][1]), pk2(acc[m][3][2], acc[m][3][3])};
    }
    ldsp pk_img = sm + w * 17408;
    ldsp v_img = pk_img + 96 * 104;
    {
      unsigned kf[6][5];
      u32x2 vv[6], ve[6];
#pragma unroll
      for (int mp = 0; mp < 3; ++mp) {
        f32x2 y01[2], y23[2], y4;
#pragma unroll
        for (int hh = 0; hh < 2; ++hh) {
          const int m = 2 * mp + hh;
          if (m < MT) {
            const f32x2 r = sp2(rstd[m]);
            const float c = opq(rstd[m] * L2E);
            u32x4 kb = u32x4{pk2(acc[m][1][0], acc[m][1][1]), pk2(acc[m][1][2], acc[m][1][3]), rem[m][0], rem[m][1]};
            f32x4 k0 = mfma32(wk[0], asb(kb), z4);
            f32x4 k1 = mfma32(wk[1], asb(kb), z4);
            y01[hh] = lo2(k0) * sp2(c); y23[hh] = hi2(k0) * sp2(c); y4[hh] = k1[0] * c;
            vv[m] = u32x2{pk2v(lo2(acc[m][2]) * r), pk2v(hi2(acc[m][2]) * r)};
            u32x2 vr = u32x2{pk2v(lo2(acc[m][3]) * r), pk2v(hi2(acc[m][3]) * r)};
            ve[m] = (g == 2) ? vr : u32x2{0x3F803F80u, 0x3F803F80u};
          } else {
            y01[hh] = sp2(0.f); y23[hh] = sp2(0.f); y4[hh] = 0.f;
            vv[m] = u32x2{0u, 0u}; ve[m] = vv[m];
          }
        }
        featmap2(y01[0], y23[0], y01[1], y23[1], y4, live[2 * mp], live[2 * mp + 1], g, kf[2 * mp], kf[2 * mp + 1]);
      }
      SCHED0();
#pragma unroll
      for (int m = 0; m < 6; ++m) {
        int tok = m * 16 + fr;
        *(LAS u32x2*)(pk_img + tok * 104 + g * 8) = u32x2{kf[m][0], kf[m][1]};
        *(LAS u32x2*)(pk_img + tok * 104 + 32 + g * 8) = u32x2{kf[m][2], kf[m][3]};
        *(LAS u32x2*)(pk_img + tok * 104 + 64 + g * 8) = u32x2{kf[m][4], 0u};
        *(LAS u32x2*)(v_img + tok * 72 + g * 8) = vv[m];
        *(LAS u32x2*)(v_img + tok * 72 + 32 + (g ^ 2) * 8) = ve[m];
      }
      LGKM0();
      SCHED0();
    }
    f32x4 kv[3][2];
#pragma unroll
    for (int ft = 0; ft < 3; ++ft)
#pragma unroll
      for (int dt = 0; dt < 2; ++dt) kv[ft][dt] = z4;
    {
      const int q = fr >> 2, p = lane & 3;
#pragma unroll
      for (int s = 0; s < 3; ++s) {
        int row = 32 * s + 8 * g + q;
        bf16x8 a[3], b[2];
#pragma unroll
        for (int ft = 0; ft < 3; ++ft) a[ft] = tr2(pk_img + row * 104 + ft * 32 + p * 8, pk_img + (row + 4) * 104 + ft * 32 + p * 8);
#pragma unroll
        for (int dt = 0; dt < 2; ++dt) b[dt] = tr2(v_img + row * 72 + dt * 32 + p * 8, v_img + (row + 4) * 72 + dt * 32 + p * 8);
#pragma unroll
        for (int ft = 0; ft < 3; ++ft)
#pragma unroll
          for (int dt = 0; dt < 2; ++dt) kv[ft][dt] = mfma32(a[ft], b[dt], kv[ft][dt]);
      }
    }
    {
      f32x4 o5[5] = {kv[0][0], kv[1][0], kv[0][1], kv[1][1], f32x4{kv[2][0][0], kv[2][0][1], kv[2][1][0], kv[2][1][1]}};
      ldsp ex = sm + (4 + wn) * 17408;
      if (mh == 1) {
        LGKM0();
#pragma unroll
        for (int i = 0; i < 5; ++i) *(LAS f32x4*)(ex + i * 1024 + lane * 16) = o5[i];
      }
      LGKM_BAR();
      if (mh == 0) {
        unsigned pp[10];
#pragma unroll
        for (int i = 0; i < 5; ++i) {
          f32x4 e = *(const LAS f32x4*)(ex + i * 1024 + lane * 16);
          pp[2 * i] = pk2(o5[i][0] + e[0], o5[i][1] + e[1]);
          pp[2 * i + 1] = pk2(o5[i][2] + e[2], o5[i][3] + e[3]);
        }
        char* kp = ws + WS_KV + (size_t)(rg * NH + head) * 4096 + lane * 16;
        st128_sc1(kp, u32x4{pp[0], pp[1], pp[2], tag});
        st128_sc1(kp + 1024, u32x4{pp[3], pp[4], pp[5], tag});
        st128_sc1(kp + 2048, u32x4{pp[6], pp[7], pp[8], tag});
        st128_sc1(kp + 3072, u32x4{pp[9], 0u, 0u, tag});
      }
    }
    STAMPAT(5);
    unsigned qf[6][5];
#pragma unroll
    for (int mp = 0; mp < 3; ++mp) {
      f32x2 y01[2], y23[2], y4;
#pragma unroll
      for (int hh = 0; hh < 2; ++hh) {
        const int m = 2 * mp + hh;
        if (m < MT) {
          const float c = opq(rstd[m] * L2E);
          u32x4 qb = u32x4{pk2(acc[m][0][0], acc[m][0][1]), pk2(acc[m][0][2], acc[m][0][3]), rem[m][0], rem[m][1]};
          f32x4 q0 = mfma32(wq[0], asb(qb), z4);
          f32x4 q1 = mfma32(wq[1], asb(qb), z4);
          y01[hh] = lo2(q0) * sp2(c); y23[hh] = hi2(q0) * sp2(c); y4[hh] = q1[0] * c;
        } else {
          y01[hh] = sp2(0.f); y23[hh] = sp2(0.f); y4[hh] = 0.f;
        }
      }
      featmap2(y01[0], y23[0], y01[1], y23[1], y4, true, true, g, qf[2 * mp], qf[2 * mp + 1]);
    }
    STAMPAT(6);
    float ks[20];
    {
      {
        const char* p1 = ws + WS_KV + ((size_t)(mh * 6 + (lane < 6 ? lane : 0)) * NH + head) * 4096 + 3072 + 1008;
        for (unsigned spins = 0; spins < SPIN_MAX; ++spins) {
          u32x4 v = ld128_sc1(p1);
          if (all_ok(v[3] == tag)) break;
          __builtin_amdgcn_s_sleep(POLLS);
        }
      }
      u32x4 pv[6][4];
      const char* kp = ws + WS_KV + (size_t)((mh * 6) * NH + head) * 4096 + lane * 16;
      for (unsigned spins = 0; spins < SPIN_MAX; ++spins) {
#pragma unroll
        for (int c = 0; c < 6; ++c) {
          const char* p = kp + (size_t)c * NH * 4096;
          asm volatile("global_load_dwordx4 %0, %4, off sc1\n\tglobal_load_dwordx4 %1, %4, off offset:1024 sc1\n\t"
                       "global_load_dwordx4 %2, %4, off offset:2048 sc1\n\tglobal_load_dwordx4 %3, %4, off offset:3072 sc1"
                       : "=&v"(pv[c][0]), "=&v"(pv[c][1]), "=&v"(pv[c][2]), "=&v"(pv[c][3]) : "v"(p) : "memory");
        }
        asm volatile("s_waitcnt vmcnt(0)" : "+v"(pv[0][0]), "+v"(pv[0][1]), "+v"(pv[0][2]), "+v"(pv[0][3]), "+v"(pv[1][0]), "+v"(pv[1][1]), "+v"(pv[1][2]), "+v"(pv[1][3]),
                     "+v"(pv[2][0]), "+v"(pv[2][1]), "+v"(pv[2][2]), "+v"(pv[2][3]), "+v"(pv[3][0]), "+v"(pv[3][1]), "+v"(pv[3][2]), "+v"(pv[3][3]),
                     "+v"(pv[4][0]), "+v"(pv[4][1]), "+v"(pv[4][2]), "+v"(pv[4][3]), "+v"(pv[5][0]), "+v"(pv[5][1]), "+v"(pv[5][2]), "+v"(pv[5][3]) :: "memory");
        bool ok = true;
#pragma unroll
        for (int c = 0; c < 6; ++c)
#pragma unroll
          for (int i = 0; i < 4; ++i) ok = ok && (pv[c][i][3] == tag);
        if (all_ok(ok)) break;
        __builtin_amdgcn_s_sleep(2);
      }
      STAMPAT(7);
#pragma unroll
      for (int i = 0; i < 10; ++i) {
        float lo = 0.f, hi = 0.f;
#pragma unroll
        for (int c = 0; c < 6; ++c) {
          unsigned u = pv[c][i / 3][i % 3];
          lo += u2f(u << 16); hi += u2f(u & 0xffff0000u);
        }
        ks[2 * i] = lo; ks[2 * i + 1] = hi;
      }
      ldsp mine = sm + w * 17408 + 12288, other = sm + (w ^ 4) * 17408 + 12288;
      SCHED0();
#pragma unroll
      for (int i = 0; i < 5; ++i) *(LAS f32x4*)(mine + i * 1024 + lane * 16) = f32x4{ks[4 * i], ks[4 * i + 1], ks[4 * i + 2], ks[4 * i + 3]};
      LGKM_BAR();
#pragma unroll
      for (int i = 0; i < 5; ++i) {
        f32x4 e = *(const LAS f32x4*)(other + i * 1024 + lane * 16);
#pragma unroll
        for (int q = 0; q < 4; ++q) ks[4 * i + q] = (mh == 0) ? ks[4 * i + q] + e[q] : e[q] + ks[4 * i + q];
      }
    }
    const bf16x8 ka0 = asb(u32x4{pk2(ks[0], ks[1]), pk2(ks[2], ks[3]), pk2(ks[4], ks[5]), pk2(ks[6], ks[7])});
    const bf16x8 ka1 = asb(u32x4{pk2(ks[8], ks[9]), pk2(ks[10], ks[11]), pk2(ks[12], ks[13]), pk2(ks[14], ks[15])});
    const bf16x8 kb0 = asb(u32x4{pk2(ks[16], ks[17]), 0u, 0u, 0u});
    const bf16x8 kb1 = asb(u32x4{pk2(ks[18], ks[19]), 0u, 0u, 0u});
    {
      u32x2 om_[MT], or_[MT];
#pragma unroll
      for (int m = 0; m < MT; ++m) {
        bf16x8 b0 = asb(u32x4{qf[m][0], qf[m][1], qf[m][2], qf[m][3]});
        bf16x8 b1 = asb(u32x4{qf[m][4], 0u, 0u, 0u});
        f32x4 n0 = mfma32(kb0, b1, mfma32(ka0, b0, z4));
        f32x4 n1 = mfma32(kb1, b1, mfma32(ka1, b0, z4));
        float da = n1[0], db = n1[0];
        swap16(da, db);
        const f32x2 inv = sp2(__builtin_amdgcn_rcpf(db + 1e-6f));
        om_[m] = u32x2{pk2v(lo2(n0) * inv), pk2v(hi2(n0) * inv)};
        or_[m] = u32x2{pk2v(lo2(n1) * inv), pk2v(hi2(n1) * inv)};
      }
      SCHED0();
      ldsp stg = sm + w * 17408 + 8192;
#pragma unroll
      for (int m = 0; m < MT; ++m) {
        *(LAS u32x2*)(stg + (m * 16 + fr) * 32 + g * 8) = om_[m];
        if (g == 0) *(LAS u32x2*)(stg + 3072 + (m * 16 + fr) * 8) = or_[m];
      }
      LGKM0();
      const int tok0 = r0 + mh * 95;
      const int ntok = mh == 0 ? 95 : nrow - 95;
      char* om = ws + WS_OM + ((size_t)head * S_ + tok0) * 32;
      char* orr = ws + WS_OR + ((size_t)head * S_ + tok0) * 8;
      u32x4 tv[3];
      u32x2 tr[2];
#pragma unroll
      for (int i = 0; i < 3; ++i) tv[i] = *(const LAS u32x4*)(stg + i * 1024 + lane * 16);
#pragma unroll
      for (int i = 0; i < 2; ++i) tr[i] = *(const LAS u32x2*)(stg + 3072 + ((i * 64 + lane) < 96 ? (i * 64 + lane) : 0) * 8);
#pragma unroll
      for (int i = 0; i < 3; ++i)
        if (i * 32 + (lane >> 1) < ntok) st128_sc1(om + i * 1024 + lane * 16, tv[i]);
#pragma unroll
      for (int i = 0; i < 2; ++i)
        if (i * 64 + lane < ntok) st64_sc1(orr + (i * 64 + lane) * 8, tr[i]);
    }
    WAITN_BAR(0);
    if (w == 0 && lane == 0) st64_sc1(ws + WS_OF + rg * 128 + ct * 8, ep);
    STAMPAT(8);
  }
  return ep;
}

DI u32x2 service(ldsp sm, const char* x, char* ws, int sid, int w, int lane) {
  const int bid = __builtin_amdgcn_workgroup_id_x();
  const int g = lane >> 4, fr = lane & 15;
  STAMPAT(0);
  if (w == 0) {
    unsigned long long nn = __builtin_amdgcn_s_memrealtime();
    u32x2 nv = u32x2{(unsigned)nn, (unsigned)(nn >> 32)};
    if (lane == 0) {
      if (sid != 15) st64_sc1(ws + WS_REQ + bid * 64, nv);
      *(LAS u32x2*)(sm + L_NONCE) = nv;
    }
  }
  LGKM_BAR();
  const u32x2 nv = *(const LAS u32x2*)(sm + L_NONCE);
  u32x2 ep = u32x2{nv[0] ^ 0x5bd1e995u, nv[1] ^ 0x80000000u};
  if (sid == 15 && w < 4) {
    const int cb = w * 64 + lane;
    const bool me = (cb == bid);
    u32x2 lastq = u32x2{0u, 0u};
    for (unsigned spins = 0; spins < SPIN_MAX; ++spins) {
      u32x2 rq, dn;
      asm volatile("global_load_dwordx2 %0, %2, off sc1\n\tglobal_load_dwordx2 %1, %3, off sc1\n\ts_waitcnt vmcnt(0)"
                   : "=&v"(rq), "=&v"(dn) : "v"(ws + WS_REQ + cb * 64), "v"(ws + WS_DONE + cb * 64) : "memory");
      if (all_ok(me || (dn[0] == ep[0] && dn[1] == ep[1]))) break;
      if (spins == 0 || rq[0] != lastq[0] || rq[1] != lastq[1]) st128_sc1(ws + WS_ACK + cb * 64, u32x4{rq[0], rq[1], ep[0], ep[1]});
      lastq = rq;
      if (spins > 12) __builtin_amdgcn_s_sleep(16);
    }
  } else {
    const int npass = sid == 15 ? 2 : 1;
    bool have = (sid == 15);
#pragma unroll 1
    for (int ps = 0; ps < npass; ++ps) {
      const int rbase = sid * 128 + (sid == 15 ? (w - 4) * 32 + ps * 16 : w * 16) + g;
      u32x4 v[4][5];
#pragma unroll
      for (int i = 0; i < 4; ++i)
#pragma unroll
        for (int c = 0; c < 5; ++c) v[i][c] = *(const u32x4*)(x + (size_t)(rbase + 4 * i) * 1280 + fr * 16 + c * 256);
      float mean[4], rstd[4];
#pragma unroll
      for (int i = 0; i < 4; ++i) {
        f32x2 a = sp2(0.f);
#pragma unroll
        for (int c = 0; c < 5; ++c)
#pragma unroll
          for (int e = 0; e < 4; ++e) a += f32x2{u2f(v[i][c][e] << 16), u2f(v[i][c][e] & 0xffff0000u)};
        float s = a[0] + a[1];
        s += dpp<0xB1>(s); s += dpp<0x4E>(s); s += dpp<0x141>(s); s += dpp<0x140>(s);
        mean[i] = s * (1.0f / 640.0f);
        const f32x2 m2 = sp2(opq(mean[i]));
        f32x2 b = sp2(0.f);
#pragma unroll
        for (int c = 0; c < 5; ++c)
#pragma unroll
          for (int e = 0; e < 4; ++e) {
            const f32x2 d = f32x2{u2f(v[i][c][e] << 16), u2f(v[i][c][e] & 0xffff0000u)} - m2;
            b = fma2(d, d, b);
          }
        float t = b[0] + b[1];
        t += dpp<0xB1>(t); t += dpp<0x4E>(t); t += dpp<0x141>(t); t += dpp<0x140>(t);
        rstd[i] = __builtin_amdgcn_rsqf(t * (1.0f / 640.0f) + 1e-5f);
      }
      if (!have) {
        u32x4 ack;
        for (unsigned spins = 0; spins < SPIN_MAX; ++spins) {
          ack = ld128_sc1(ws + WS_ACK + bid * 64);
          if (all_ok(ack[0] == nv[0] && ack[1] == nv[1])) break;
          __builtin_amdgcn_s_sleep(2);
        }
        ep = u32x2{ack[2], ack[3]};
        have = true;
        if (w == 0 && lane == 0) st64_sc1(ws + WS_DONE + bid * 64, ep);
      }
#pragma unroll
      for (int i = 0; i < 4; ++i)
        if (fr == 0) st128_sc1(ws + WS_STAT + (size_t)(rbase + 4 * i) * 16, u32x4{f2u(mean[i]), f2u(rstd[i]), ep[0], ep[1]});
    }
  }
  STAMPAT(1);
  return ep;
}

__global__ __launch_bounds__(512, 2) void fused(const char* __restrict__ x, const char* __restrict__ wqkv,
                                                const char* __restrict__ wphi, const char* __restrict__ wo,
                                                const char* __restrict__ wg, const char* __restrict__ wu,
                                                const char* __restrict__ wd, char* __restrict__ ws, char* __restrict__ out) {
  extern __shared__ __attribute__((aligned(16))) char smem[];
  ldsp sm = (ldsp)smem;
  const int tid = __builtin_amdgcn_workitem_id_x();
  const int bp = __builtin_amdgcn_workgroup_id_x(), xcd = bp & 7, ip = bp >> 3;
  const int idx = ip < 2 ? 30 + ip : ip - 2;
  const int b = idx * 8 + xcd;
  const int w = __builtin_amdgcn_readfirstlane(tid >> 6), lane = tid & 63;
  const u32x4 srd_wd = make_srd(wd, DF_ * D_ * 2);
  const int rgl = idx / 5, c5 = idx % 5;
  const int rg = (xcd >> 2) * 6 + rgl, cq = xcd & 3;
  if (idx < 30 && c5 >= 3) {
    const int ct = cq * 2 + (c5 - 3);
    if (w < 4) phase1<6, false>(sm, x, wqkv, wphi, wg, wu, srd_wd, ws, rg, ct, 0, 0, w, lane);
    else phase1<5, false>(sm, x, wqkv, wphi, wg, wu, srd_wd, ws, rg, ct, 0, 0, w, lane);
    return;
  }
  const int p2i = idx < 30 ? rgl * 3 + c5 : 18 + (idx - 30);
  const int rt = (xcd >> 1) * 4 + p2i / 5, ct2 = (xcd & 1) * 5 + p2i % 5;
#define ROTF ((((p2i % 5) * 12) / 5 + (p2i / 5) * 3) % 12)
  const int rot = ROTF;
  u32x2 ep;
  if (idx < 30) {
    const int ct = cq * 3 + c5;
    if (w < 4) ep = phase1<6, true>(sm, x, wqkv, wphi, wg, wu, srd_wd, ws, rg, ct, ct2 * 64, rot, w, lane);
    else ep = phase1<5, true>(sm, x, wqkv, wphi, wg, wu, srd_wd, ws, rg, ct, ct2 * 64, rot, w, lane);
  } else {
    ep = service(sm, x, ws, b - 240, w, lane);
    {
      const u32x4 srd_wo = make_srd(wo, D_ * D_ * 2);
      const unsigned vp = (lane >> 3) * 1280 + (lane & 7) * 16 + (xcd & 1) * 640;
      const unsigned dst = (unsigned)(size_t)sm + w * 1024;
      const int w16 = (idx - 30) * 8 + w;
      if (!(b == 255 && w < 4)) {
#pragma unroll 1
        for (int p = w16; p < 192; p += 16) {
#pragma unroll
          for (int c = 0; c < 5; ++c) dma_b(srd_wd, vp, p * 8 * 1280 + c * 128, dst);
          asm volatile("s_waitcnt vmcnt(30)" ::: "memory");
        }
#pragma unroll 1
        for (int p = w16; p < 80; p += 16) {
#pragma unroll
          for (int c = 0; c < 5; ++c) dma_b(srd_wo, vp, p * 8 * 1280 + c * 128, dst);
          asm volatile("s_waitcnt vmcnt(30)" ::: "memory");
        }
      }
      WAITN_BAR(0);
    }
    const unsigned vB = p2_vB(w, lane);
    const unsigned smb = (unsigned)(size_t)sm;
    p2_issueB_down(srd_wd, vB, ct2 * 64, w, smb, 0, rot);
    p2_issueB_down(srd_wd, vB, ct2 * 64, w, smb, 1, rot);
    p2_issueB_down(srd_wd, vB, ct2 * 64, w, smb, 2, rot);
  }
  phase2(sm, x, wo, srd_wd, ws, out, rt, ct2, rot, w, lane, ep);
}

#if defined(__HIP_DEVICE_COMPILE__)

#endif

extern "C" void kernel_launch(void* const* d_in, const int* in_sizes, int n_in, void* d_out, int out_size, void* d_ws,
                              size_t ws_size, hipStream_t stream) {
  (void)hipFuncSetAttribute((const void*)fused, hipFuncAttributeMaxDynamicSharedMemorySize, K_LDS);
  fused<<<256, 512, K_LDS, stream>>>((const char*)d_in[0], (const char*)d_in[1], (const char*)d_in[2], (const char*)d_in[3],
                                     (const char*)d_in[5], (const char*)d_in[6], (const char*)d_in[7], (char*)d_ws, (char*)d_out);
}
```

```cpp
#define ORDER 2
#define POLLS 6
#include <hip/hip_runtime.h>
#include <hip/hip_bf16.h>
#include <stdint.h>

typedef __attribute__((ext_vector_type(8))) __bf16 bf16x8;
typedef __attribute__((ext_vector_type(2))) __bf16 bf16x2;
typedef __attribute__((ext_vector_type(4))) short s16x4;
typedef __attribute__((ext_vector_type(8))) short s16x8;
typedef __attribute__((ext_vector_type(4))) float f32x4;
typedef __attribute__((ext_vector_type(2))) float f32x2;
typedef __attribute__((ext_vector_type(4))) unsigned u32x4;
typedef __attribute__((ext_vector_type(2))) unsigned u32x2;
#define LAS __attribute__((address_space(3)))
#define DI __device__ __forceinline__
typedef LAS char* ldsp;

constexpr int S_ = 2048, D_ = 640, DF_ = 1536, NH = 32;
constexpr int NRG = 12;
constexpr size_t WS_H = 0;
constexpr size_t WS_KV = WS_H + (size_t)S_ * DF_ * 2;
constexpr size_t WS_OM = WS_KV + (size_t)NRG * NH * 4096;
constexpr size_t WS_OR = WS_OM + (size_t)NH * S_ * 32;
constexpr size_t WS_REQ = WS_OR + (size_t)NH * S_ * 8;
constexpr size_t WS_ACK = WS_REQ + 256 * 64;
constexpr size_t WS_DONE = WS_ACK + 256 * 64;
constexpr size_t WS_STAT = WS_DONE + 256 * 64;
constexpr size_t WS_HF = WS_STAT + 2048 * 16;
constexpr size_t WS_KF = WS_HF + 12 * 128;
constexpr size_t WS_OF = WS_KF + 8 * 128;
constexpr size_t WS_DBG = 64u << 20;

constexpr int A_SLOT = 22528, B_SLOT = 16384;
constexpr int L_A = 0;
constexpr int L_B = 3 * A_SLOT;
constexpr int L_MISC = L_B + 5 * B_SLOT;
constexpr int L_DUMMY = L_MISC;
constexpr int L_NONCE = L_MISC + 1024;
constexpr int L_CS = L_NONCE + 64;
constexpr int L_ST = L_CS + 2048;
constexpr int K_LDS = L_ST + 4096;
constexpr int P2_A = 0, P2_B = 98304;
constexpr unsigned SPIN_MAX = 1u << 20;

#if defined(__HIP_DEVICE_COMPILE__)

#endif

#define STAMPAT(i)

DI float u2f(unsigned u) { return __builtin_bit_cast(float, u); }
DI unsigned f2u(float f) { return __builtin_bit_cast(unsigned, f); }
DI u32x4 make_srd(const void* p, unsigned bytes) {
  uint64_t a = (uint64_t)p;
  u32x4 r;
  r[0] = (unsigned)a;
  r[1] = (unsigned)(a >> 32) & 0xffffu;
  r[2] = bytes;
  r[3] = 0x00020000u;
  return r;
}
DI void dma_b(u32x4 srd, unsigned voff, unsigned soff, unsigned m0v) {
  asm volatile("s_mov_b32 m0, %3\n\ts_nop 0\n\tbuffer_load_dwordx4 %0, %1, %2 offen lds" ::"v"(voff), "s"(srd), "s"(soff), "s"(m0v) : "memory");
}
DI void dma_b_mask(u32x4 srd, unsigned voff, unsigned soff, unsigned m0v, unsigned long long mask) {
  asm volatile("s_mov_b32 m0, %3\n\ts_mov_b64 exec, %4\n\tbuffer_load_dwordx4 %0, %1, %2 offen lds\n\ts_mov_b64 exec, -1" ::"v"(voff), "s"(srd), "s"(soff), "s"(m0v), "s"(mask) : "memory");
}
DI void dma_b_sc1(u32x4 srd, unsigned voff, unsigned soff, unsigned m0v) {
  asm volatile("s_mov_b32 m0, %3\n\ts_nop 0\n\tbuffer_load_dwordx4 %0, %1, %2 offen sc1 lds" ::"v"(voff), "s"(srd), "s"(soff), "s"(m0v) : "memory");
}
DI u32x4 ld128_sc1(const char* p) { u32x4 v; asm volatile("global_load_dwordx4 %0, %1, off sc1\n\ts_waitcnt vmcnt(0)" : "=&v"(v) : "v"(p) : "memory"); return v; }
DI u32x2 ld64_sc1(const char* p) { u32x2 v; asm volatile("global_load_dwordx2 %0, %1, off sc1\n\ts_waitcnt vmcnt(0)" : "=&v"(v) : "v"(p) : "memory"); return v; }
DI void st128_sc1(char* p, u32x4 v) { asm volatile("global_store_dwordx4 %0, %1, off sc1\n\ts_nop 1" ::"v"(p), "v"(v) : "memory"); }
DI void st64_sc1(char* p, u32x2 v) { asm volatile("global_store_dwordx2 %0, %1, off sc1" ::"v"(p), "v"(v) : "memory"); }
#define WAITN_BAR(N) asm volatile("s_waitcnt vmcnt(%0) lgkmcnt(0)\n\ts_barrier" ::"n"(N) : "memory")
#define LGKM_BAR() asm volatile("s_waitcnt lgkmcnt(0)\n\ts_barrier" ::: "memory")
#define LGKM0() asm volatile("s_waitcnt lgkmcnt(0)" ::: "memory")
#define MEMFENCE() asm volatile("" ::: "memory")
#define SCHED0() __builtin_amdgcn_sched_barrier(0)

DI bf16x8 ld128(ldsp p) { return *(const LAS bf16x8*)p; }
DI s16x4 tr1(ldsp p) { return __builtin_amdgcn_ds_read_tr16_b64_v4i16((LAS s16x4*)p); }
DI bf16x8 cat(s16x4 a, s16x4 b) { return __builtin_bit_cast(bf16x8, __builtin_shufflevector(a, b, 0, 1, 2, 3, 4, 5, 6, 7)); }
DI bf16x8 tr2(ldsp p0, ldsp p1) { return cat(tr1(p0), tr1(p1)); }
DI f32x4 mfma32(bf16x8 a, bf16x8 b, f32x4 c) { return __builtin_amdgcn_mfma_f32_16x16x32_bf16(a, b, c, 0, 0, 0); }
DI bf16x8 asb(u32x4 v) { return __builtin_bit_cast(bf16x8, v); }
DI unsigned pk2(float a, float b) {
  bf16x2 v;
  v[0] = (__bf16)a;
  v[1] = (__bf16)b;
  return __builtin_bit_cast(unsigned, v);
}
DI void swap16(float& a, float& b) {
  auto r = __builtin_amdgcn_permlane16_swap(f2u(a), f2u(b), false, false);
  a = u2f(r[0]); b = u2f(r[1]);
}
DI void swap32(float& a, float& b) {
  auto r = __builtin_amdgcn_permlane32_swap(f2u(a), f2u(b), false, false);
  a = u2f(r[0]); b = u2f(r[1]);
}
template <int CTRL> DI float dpp(float x) { return u2f(__builtin_amdgcn_update_dpp(0u, f2u(x), CTRL, 0xf, 0xf, false)); }
DI float fmx(float a, float b) { return __builtin_fmaxf(a, b); }
DI float fmn(float a, float b) { return __builtin_fminf(a, b); }
DI float rawmax(float a, float b) { float r; asm("v_max_f32 %0, %1, %2" : "=v"(r) : "v"(a), "v"(b)); return r; }
DI float opq(float x) { asm("" : "+v"(x)); return x; }
DI f32x2 lo2(f32x4 v) { return __builtin_shufflevector(v, v, 0, 1); }
DI f32x2 hi2(f32x4 v) { return __builtin_shufflevector(v, v, 2, 3); }
DI f32x2 sp2(float x) { return f32x2{x, x}; }
DI f32x2 fma2(f32x2 a, f32x2 b, f32x2 c) { return __builtin_elementwise_fma(a, b, c); }
DI int strow(int s, int nrow) { int ri = s < 95 ? s : s - 1; return ri < nrow ? ri : nrow - 1; }
DI int rs_of(int rg) { return (512 * rg) / 3; }
DI int rg_of(int row) { return (3 * row + 2) >> 9; }
DI bool all_ok(bool ok) { return __builtin_amdgcn_ballot_w64(ok) == ~0ull; }

DI void wait_flags(const char* p, bool need, u32x2 ep) {
  for (unsigned spins = 0; spins < SPIN_MAX; ++spins) {
    u32x2 v = ld64_sc1(p);
    if (all_ok(!need || (v[0] == ep[0] && v[1] == ep[1]))) break;
    __builtin_amdgcn_s_sleep(POLLS);
  }
}

DI f32x2 ex2(f32x2 v) { return f32x2{__builtin_amdgcn_exp2f(v[0]), __builtin_amdgcn_exp2f(v[1])}; }
DI unsigned pk2v(f32x2 v) { return pk2(v[0], v[1]); }
DI void featmap2(f32x2 a01, f32x2 a23, f32x2 b01, f32x2 b23, f32x2 e4, bool livea, bool liveb, int g, unsigned (&oa)[5], unsigned (&ob)[5]) {
  float a0 = fmx(fmx(fmx(a01[0], a01[1]), a23[0]), fmx(a23[1], e4[0]));
  float a1 = -fmn(fmn(fmn(a01[0], a01[1]), a23[0]), fmn(a23[1], e4[0]));
  float b0 = fmx(fmx(fmx(b01[0], b01[1]), b23[0]), fmx(b23[1], e4[1]));
  float b1 = -fmn(fmn(fmn(b01[0], b01[1]), b23[0]), fmn(b23[1], e4[1]));
  swap16(a0, a1); swap16(b0, b1);
  float ca = rawmax(a0, a1), cb = rawmax(b0, b1);
  swap32(ca, cb);
  float d0 = rawmax(ca, cb), d1 = d0;
  swap32(d0, d1);
  float Ma = d0, Na = d0, Mb = d1, Nb = d1;
  swap16(Ma, Na); swap16(Mb, Nb);
  const f32x2 pa01 = ex2(a01 - sp2(Ma)), pa23 = ex2(a23 - sp2(Ma)), na01 = ex2(-a01 - sp2(Na)), na23 = ex2(-a23 - sp2(Na));
  const f32x2 pb01 = ex2(b01 - sp2(Mb)), pb23 = ex2(b23 - sp2(Mb)), nb01 = ex2(-b01 - sp2(Nb)), nb23 = ex2(-b23 - sp2(Nb));
  const float pa4 = __builtin_amdgcn_exp2f(e4[0] - Ma), na4 = __builtin_amdgcn_exp2f(-e4[0] - Na);
  const float pb4 = __builtin_amdgcn_exp2f(e4[1] - Mb), nb4 = __builtin_amdgcn_exp2f(-e4[1] - Nb);
  f32x2 t;
  t = pa01 + pa23; a0 = (t[0] + t[1]) + pa4;
  t = na01 + na23; a1 = (t[0] + t[1]) + na4;
  t = pb01 + pb23; b0 = (t[0] + t[1]) + pb4;
  t = nb01 + nb23; b1 = (t[0] + t[1]) + nb4;
  swap16(a0, a1); swap16(b0, b1);
  ca = a0 + a1; cb = b0 + b1;
  swap32(ca, cb);
  d0 = __builtin_amdgcn_rcpf(ca + cb);
  if (!(g < 2 ? livea : liveb)) d0 = 0.f;
  d1 = d0;
  swap32(d0, d1);
  Ma = d0; Na = d0; Mb = d1; Nb = d1;
  swap16(Ma, Na); swap16(Mb, Nb);
  oa[0] = pk2v(pa01 * sp2(Ma)); oa[1] = pk2v(pa23 * sp2(Ma));
  oa[2] = pk2v(na01 * sp2(Na)); oa[3] = pk2v(na23 * sp2(Na));
  oa[4] = pk2(pa4 * Ma, na4 * Na);
  ob[0] = pk2v(pb01 * sp2(Mb)); ob[1] = pk2v(pb23 * sp2(Mb));
  ob[2] = pk2v(nb01 * sp2(Nb)); ob[3] = pk2v(nb23 * sp2(Nb));
  ob[4] = pk2(pb4 * Mb, nb4 * Nb);
}

struct P2Ctx { unsigned vB, vR; int c0; };
DI unsigned p2_vB(int w, int lane) {
  const int l3 = lane >> 3, l7 = lane & 7;
  int fb = ((l3 >> 1) & 1) | ((w & 1) << 1);
  int ch = (((l7 >> 1) ^ fb) << 1) | (l7 & 1);
  return l3 * 1280 + ch * 16;
}
DI int p2_rot(int s, int rot) { int r = s + rot; return r >= 12 ? r - 12 : r; }
DI void p2_issueB_down(u32x4 srd_wd, unsigned vB, int c0, int w, unsigned smb, int s, int rot) {
  const int sr = p2_rot(s, rot);
  dma_b(srd_wd, vB, (128 * sr + 8 * w) * 1280 + c0 * 2, smb + P2_B + (s % 3) * 16384 + w * 1024);
  dma_b(srd_wd, vB, (128 * sr + 8 * w + 64) * 1280 + c0 * 2, smb + P2_B + (s % 3) * 16384 + w * 1024 + 8192);
}

DI void phase2(ldsp sm, const char* x, const char* wo, const u32x4 srd_wd, char* ws, char* out, int rt, int ct, int rot, int w, int lane, u32x2 ep) {
  const int r0 = rt * 128, c0 = ct * 64;
  const int g = lane >> 4, fr = lane & 15, l3 = lane >> 3, l7 = lane & 7;
  const int kh = w & 1, nh = (w >> 1) & 1, mh = w >> 2;
  const unsigned smb = (unsigned)(size_t)sm;
  const u32x4 srd_h = make_srd(ws + WS_H, S_ * DF_ * 2);
  const u32x4 srd_wo = make_srd(wo, D_ * D_ * 2);
  const u32x4 srd_om = make_srd(ws + WS_OM, NH * S_ * 32);
  const u32x4 srd_or = make_srd(ws + WS_OR, NH * S_ * 8);
  const unsigned vB = p2_vB(w, lane);
  const unsigned vA = l3 * 3072 + ((l7 ^ ((4 * (w & 1) + (l3 >> 1)) & 7)) * 16);
  const unsigned vR = vB - l3 * 1280 + (20 * (lane >> 5) + 16 + (l3 & 3)) * 1280;
  const unsigned vL = lane * 16;
  const int rgl = rg_of(r0), rgh = rg_of(r0 + 127);

#define P2_PIECE(s, p) do { \
    constexpr int i_ = (p) < 2 ? (p) : (p) - 2; \
    if ((s) < 12) { \
      if ((p) < 2) dma_b(srd_wd, vB, (128 * p2_rot((s), rot) + 8 * w + 64 * i_) * 1280 + c0 * 2, smb + P2_B + ((s) % 3) * 16384 + i_ * 8192 + w * 1024); \
      else dma_b_sc1(srd_h, vA, (r0 + 8 * w + 64 * (i_ & 1)) * 3072 + p2_rot((s), rot) * 256 + (i_ >> 1) * 128, smb + P2_A + ((s) % 3) * 32768 + (i_ >> 1) * 16384 + (i_ & 1) * 8192 + w * 1024); \
    } else if ((s) < 16) { \
      if ((p) < 2) dma_b(srd_wo, vB, (160 * ((s) - 12) + 20 * ((w + 8 * i_) >> 1) + 8 * (w & 1)) * 1280 + c0 * 2, smb + P2_B + ((s) % 3) * 16384 + i_ * 8192 + w * 1024); \
      else dma_b_sc1(srd_om, vL, ((8 * ((s) - 12) + 2 * i_ + (w >> 2)) * S_ + r0) * 32 + (w & 3) * 1024, smb + P2_A + ((s) % 3) * 32768 + i_ * 8192 + w * 1024); \
    } else { \
      if ((p) < 2) dma_b(srd_wo, vR, (40 * (w + 8 * i_)) * 1280 + c0 * 2, smb + P2_B + ((s) % 3) * 16384 + i_ * 8192 + w * 1024); \
      else dma_b_sc1(srd_or, vL, ((8 * i_ + w) * S_ + r0) * 8, smb + P2_A + ((s) % 3) * 32768 + i_ * 8192 + w * 1024); \
    } } while (0)

  const int aD = P2_A + kh * 16384 + (64 * mh + fr) * 128 + ((g ^ ((fr >> 1) & 7)) * 16);
  const int aM = P2_A + (4 * kh + (g >> 1)) * 4096 + (64 * mh + fr) * 32 + (g & 1) * 16;
  const int aR = P2_A + (16 * kh + 2 * g) * 1024 + (64 * mh + fr) * 8;
  int bO[2];
  {
    const int q = fr >> 2, p = lane & 3;
    const int f = ((q >> 1) & 1) | ((g & 1) << 1);
#pragma unroll
    for (int j = 0; j < 2; ++j) bO[j] = P2_B + (64 * kh + 8 * g + q) * 128 + (((2 * nh + j) ^ f) * 32) + p * 8;
  }
  f32x4 acc[4][2];
#pragma unroll
  for (int m = 0; m < 4; ++m)
#pragma unroll
    for (int j = 0; j < 2; ++j) acc[m][j] = f32x4{0.f, 0.f, 0.f, 0.f};

  STAMPAT(8);
  if (w == 0) wait_flags(ws + WS_HF + (size_t)(lane < 16 ? rgl : rgh) * 128 + (lane & 15) * 8, lane < 32 && (lane & 15) < 12, ep);
  WAITN_BAR(0);
  STAMPAT(9);
#define P2_A3(s) do { P2_PIECE(s, 2); P2_PIECE(s, 3); P2_PIECE(s, 4); P2_PIECE(s, 5); } while (0)
  P2_A3(0); P2_A3(1); P2_A3(2);
  u32x4 xr[2];
  bf16x8 fa[2][8];
  s16x4 fb[2][8];
#define P2_RD(s, i) do { \
    if ((i) < 8) fb[(s) & 1][i] = tr1(sm + ((s) % 3) * 16384 + bO[((i) >> 1) & 1] + ((i) >> 2) * 4096 + ((i) & 1) * 512); \
    else if ((i) < 16) { \
      constexpr int sl_ = ((i) - 8) >> 2, m_ = ((i) - 8) & 3; \
      if ((s) < 12) fa[(s) & 1][((i) - 8) & 7] = ld128(sm + ((s) % 3) * 32768 + (sl_ ? (aD ^ 64) : aD) + m_ * 2048); \
      else if ((s) < 16) fa[(s) & 1][((i) - 8) & 7] = ld128(sm + ((s) % 3) * 32768 + aM + sl_ * 8192 + m_ * 512); \
      else fa[(s) & 1][((i) - 8) & 7] = cat(*(const LAS s16x4*)(sm + ((s) % 3) * 32768 + aR + sl_ * 8192 + m_ * 128), *(const LAS s16x4*)(sm + ((s) % 3) * 32768 + aR + sl_ * 8192 + 1024 + m_ * 128)); } } while (0)
#define P2_MM(s, i) acc[((i) >> 1) & 3][(i) & 1] = mfma32(cat(fb[(s) & 1][((i) >> 3) * 4 + 2 * ((i) & 1)], fb[(s) & 1][((i) >> 3) * 4 + 2 * ((i) & 1) + 1]), fa[(s) & 1][((i) >> 3) * 4 + (((i) >> 1) & 3)], acc[((i) >> 1) & 3][(i) & 1])
#define P2_GROUP(s, q) do { \
    if ((s) >= 1 && (s) + 2 < 17 && (s) != 12 && !(((s) == 10 || (s) == 11) && (q) >= 2)) P2_PIECE((s) + 2, q); \
    SCHED0(); \
    if ((s) < 17) { P2_RD(s, 3 * (q)); P2_RD(s, 3 * (q) + 1); P2_RD(s, 3 * (q) + 2); } \
    SCHED0(); \
    if ((s) >= 1 && (s) != 12) { P2_MM((s) - 1, ((q) * 8) / 3); P2_MM((s) - 1, ((q) * 8) / 3 + 1); if (((q) * 8) / 3 + 2 < (((q) + 1) * 8) / 3) P2_MM((s) - 1, ((q) * 8) / 3 + 2); } \
    SCHED0(); } while (0)
#define P2_STAGE(s) do { \
    SCHED0(); \
    if ((s) < 17) WAITN_BAR(((s) == 0 || (s) == 12) ? 8 : (((s) == 1 || (s) == 13) ? 4 : ((s) == 11 ? 2 : ((s) < 16 ? 6 : 0)))); \
    SCHED0(); \
    P2_GROUP(s, 0); P2_GROUP(s, 1); P2_GROUP(s, 2); P2_GROUP(s, 3); P2_GROUP(s, 4); P2_GROUP(s, 5); } while (0)
  P2_STAGE(0); P2_STAGE(1); P2_STAGE(2); P2_STAGE(3); P2_STAGE(4); P2_STAGE(5); P2_STAGE(6); P2_STAGE(7); P2_STAGE(8);
  P2_STAGE(9); P2_STAGE(10); P2_STAGE(11);
  SCHED0();
#pragma unroll
  for (int i = 0; i < 16; ++i) P2_MM(11, i);
  SCHED0();
  STAMPAT(10);
  if (w == 0) wait_flags(ws + WS_OF + (size_t)(lane < 16 ? rgl : rgh) * 128 + (lane & 15) * 8, lane < 32 && (lane & 15) < 8, ep);
  LGKM_BAR();
  STAMPAT(11);
  P2_PIECE(14, 0); P2_PIECE(14, 1);
  P2_A3(12); P2_A3(13); P2_A3(14);
  P2_STAGE(12); P2_STAGE(13); P2_STAGE(14); P2_STAGE(15);
  const int orow = 64 * mh + 32 * kh + (lane >> 2), ocol = c0 * 2 + nh * 64 + (lane & 3) * 16;
#pragma unroll
  for (int i = 0; i < 2; ++i)
    asm volatile("global_load_dwordx4 %0, %1, off" : "=&v"(xr[i]) : "v"(x + (size_t)(r0 + orow + 16 * i) * 1280 + ocol) : "memory");
  P2_STAGE(16); P2_STAGE(17);
  SCHED0();
  STAMPAT(12);
  f32x4 keep[2][2];
  {
    ldsp ex = sm + w * 4096 + lane * 16;
#pragma unroll
    for (int i = 0; i < 2; ++i)
#pragma unroll
      for (int j = 0; j < 2; ++j) {
        f32x4 snd;
#pragma unroll
        for (int e = 0; e < 4; ++e) {
          const float lo_ = acc[i][j][e], hi_ = acc[2 + i][j][e];
          snd[e] = kh ? lo_ : hi_;
          keep[i][j][e] = kh ? hi_ : lo_;
        }
        *(LAS f32x4*)(ex + (i * 2 + j) * 1024) = snd;
      }
  }
  LGKM_BAR();
  {
    ldsp ex = sm + (w ^ 1) * 4096 + lane * 16;
    ldsp tp = sm + 65536 + w * 4608;
#pragma unroll
    for (int i = 0; i < 2; ++i)
#pragma unroll
      for (int j = 0; j < 2; ++j) {
        const f32x4 e = *(const LAS f32x4*)(ex + (i * 2 + j) * 1024);
        *(LAS f32x4*)(tp + (i * 16 + fr) * 144 + j * 64 + g * 16) = keep[i][j] + e;
      }
    asm volatile("" : "+v"(xr[0]), "+v"(xr[1]));
#pragma unroll
    for (int i = 0; i < 2; ++i) {
      const f32x4 s0 = *(const LAS f32x4*)(tp + (i * 16 + (lane >> 2)) * 144 + (lane & 3) * 32);
      const f32x4 s1 = *(const LAS f32x4*)(tp + (i * 16 + (lane >> 2)) * 144 + (lane & 3) * 32 + 16);
      u32x4 o;
#pragma unroll
      for (int e = 0; e < 2; ++e) {
        o[e] = pk2(s0[2 * e] + u2f(xr[i][e] << 16), s0[2 * e + 1] + u2f(xr[i][e] & 0xffff0000u));
        o[2 + e] = pk2(s1[2 * e] + u2f(xr[i][2 + e] << 16), s1[2 * e + 1] + u2f(xr[i][2 + e] & 0xffff0000u));
      }
      *(u32x4*)(out + (size_t)(r0 + orow + 16 * i) * 1280 + ocol) = o;
    }
  }
}

template <int MT, bool GU>
DI u32x2 phase1(ldsp sm, const char* x, const char* wqkv, const char* wphi, const char* wg, const char* wu, const u32x4 srd_wd,
                char* ws, int rg, int ct, int c0p2, int rot, int w, int lane) {
  STAMPAT(0);
  constexpr int mh = (MT == 6) ? 0 : 1;
  const int wn = w & 3;
  const int r0 = rs_of(rg), nrow = rs_of(rg + 1) - r0;
  const unsigned smb = (unsigned)(size_t)sm;
  const int g = lane >> 4, fr = lane & 15;

  const u32x4 srdx = make_srd(x, S_ * D_ * 2);
  unsigned voffA[3];
#pragma unroll
  for (int c = 0; c < 3; ++c) {
    int slot = 8 * (w + 8 * c) + (lane >> 3);
    int ch = (lane & 7) ^ ((slot >> 1) & 7);
    int ri = slot < 95 ? slot : slot - 1;
    ri = ri < nrow ? ri : nrow - 1;
    voffA[c] = (unsigned)((r0 + ri) * 1280 + ch * 16);
  }
  if (w >= 6) voffA[2] = 0x40000000u;
  const unsigned long long FULL = ~0ull, PART = 0x00FFFFFFFFFFFFFFull;
  const unsigned long long mask1 = (w == 3) ? PART : FULL, mask2 = (w == 5) ? PART : FULL;
  const unsigned adst0 = smb + L_A + w * 1024;
  u32x4 srd0, srd1;
  unsigned voff0, voff1;
  constexpr unsigned SSTEP = GU ? 32 * 3072 : 32 * 3840;
  if (GU) {
    srd0 = make_srd(wg, D_ * DF_ * 2);
    srd1 = make_srd(wu, D_ * DF_ * 2);
    int krow = 4 * w + (lane >> 4);
    int chp = lane & 15;
    int f = (krow & 3) | (((krow >> 3) & 1) << 2);
    int ch = (((chp >> 1) ^ f) << 1) | (chp & 1);
    voff0 = voff1 = krow * 3072 + ct * 256 + ch * 16;
  } else {
    srd0 = srd1 = make_srd(wqkv, D_ * 3 * D_ * 2);
    unsigned vo[2];
#pragma unroll
    for (int c = 0; c < 2; ++c) {
      int cidx = (w + 8 * c) * 64 + lane;
      int rho = cidx / 30, cc = cidx % 30;
      int seg = cc / 10, ch = cc % 10;
      int k = (rho & 16) | ((rho & 4) << 1) | ((rho & 8) >> 1) | (rho & 3);
      vo[c] = (cidx < 960) ? (unsigned)(k * 3840 + seg * 1280 + ct * 160 + ch * 16) : 0x40000000u;
    }
    voff0 = vo[0];
    voff1 = vo[1];
  }
  const unsigned bdst0 = smb + L_B + w * 1024;
#define KROT (((rg%6)*5/3+ct*2)%10)
  const unsigned krot = KROT;
#define RA(so) ((so) + krot * 128u >= 1280u ? (so) + krot * 128u - 1280u : (so) + krot * 128u)
#define RB(so) ((so) + krot * 2u * SSTEP >= 20u * SSTEP ? (so) + krot * 2u * SSTEP - 20u * SSTEP : (so) + krot * 2u * SSTEP)
#define DMA_B0(soff, dst) dma_b(srd0, voff0, RB(soff), (dst))
#define DMA_B1(soff, dst) dma_b(srd1, voff1, RB(soff), (dst))
#define DMA_A0(soff, dst) dma_b(srdx, voffA[0], RA(soff), (dst))
#define DMA_A1(soff, dst) dma_b_mask(srdx, voffA[1], RA(soff), (dst), mask1)
#define DMA_A2(soff, dst) dma_b_mask(srdx, voffA[2], RA(soff), (dst), mask2)
  const unsigned a2base = (w >= 6) ? smb + L_DUMMY : adst0 + 16384;
  const unsigned a2mul = (w >= 6) ? 0u : 1u;
  unsigned wraw[16];
  if (!GU) {
    const char* wp = wphi + (ct * 4 + wn) * 800;
    const char* p0 = wp + (5 * (fr >> 2) + (fr & 3)) * 2;
    const char* p1 = wp + (5 * (fr >> 2) + 4) * 2;
    const char* q0 = p0 + g * 160;
    const char* q1 = p1 + g * 160;
#pragma unroll
    for (int j = 0; j < 4; ++j) {
      asm volatile("global_load_ushort %0, %1, off offset:%2" : "=&v"(wraw[j]) : "v"(q0), "n"(j * 40) : "memory");
      asm volatile("global_load_ushort %0, %1, off offset:%2" : "=&v"(wraw[4 + j]) : "v"(p0), "n"(640 + j * 40) : "memory");
      asm volatile("global_load_ushort %0, %1, off offset:%2" : "=&v"(wraw[8 + j]) : "v"(q1), "n"(j * 40) : "memory");
      asm volatile("global_load_ushort %0, %1, off offset:%2" : "=&v"(wraw[12 + j]) : "v"(p1), "n"(640 + j * 40) : "memory");
    }
  }
  DMA_B0(0, bdst0); DMA_B1(0, bdst0 + 8192);
  DMA_A0(0, adst0); DMA_A1(0, adst0 + 8192);
  DMA_A2(0, a2base);
  DMA_B0(SSTEP, bdst0 + B_SLOT); DMA_B1(SSTEP, bdst0 + B_SLOT + 8192);
  DMA_B0(2 * SSTEP, bdst0 + 2 * B_SLOT); DMA_B1(2 * SSTEP, bdst0 + 2 * B_SLOT + 8192);
  DMA_A0(128, adst0 + A_SLOT); DMA_A1(128, adst0 + A_SLOT + 8192);
  DMA_A2(128, a2base + a2mul * A_SLOT);
  DMA_B0(3 * SSTEP, bdst0 + 3 * B_SLOT); DMA_B1(3 * SSTEP, bdst0 + 3 * B_SLOT + 8192);
  DMA_B0(4 * SSTEP, bdst0 + 4 * B_SLOT); DMA_B1(4 * SSTEP, bdst0 + 4 * B_SLOT + 8192);
  DMA_A0(256, adst0 + 2 * A_SLOT); DMA_A1(256, adst0 + 2 * A_SLOT + 8192);
  if (w == 0 && lane < 48) {
    int rr = lane >> 3;
    *(LAS u32x4*)(sm + L_A + (rr >> 1) * A_SLOT + ((rr & 1) ? 175 : 95) * 128 + (lane & 7) * 16) =
        u32x4{0x3F803F80u, 0x3F803F80u, 0x3F803F80u, 0x3F803F80u};
  }
  const int bid = __builtin_amdgcn_workgroup_id_x();
  if (w == 1) {
    unsigned long long nn = __builtin_amdgcn_s_memrealtime();
    u32x2 nv = u32x2{(unsigned)nn, (unsigned)(nn >> 32)};
    if (lane == 0) {
      st64_sc1(ws + WS_REQ + bid * 64, nv);
      *(LAS u32x2*)(sm + L_NONCE) = nv;
    }
  }

  const int aoff0 = L_A + mh * 6 * 2048 + fr * 128 + ((g ^ ((fr >> 1) & 7)) * 16);
  const int aoff1 = aoff0 ^ 64;
  int boff[4];
  constexpr int TSTEP = GU ? 1024 : 3840;
  {
    const int q = fr >> 2, p = lane & 3;
    if (GU) {
      int f = q | ((g & 1) << 2);
#pragma unroll
      for (int j = 0; j < 4; ++j)
        boff[j] = L_B + (j >> 1) * 8192 + (8 * g + q) * 256 + (((2 * wn + (j & 1)) ^ f) * 32) + p * 8;
    } else {
      int rho = 16 * (g >> 1) + 4 * (g & 1) + q;
#pragma unroll
      for (int j = 0; j < 3; ++j) boff[j] = L_B + rho * 480 + (20 * j + 5 * wn + p) * 8;
      boff[3] = L_B + rho * 480 + (20 * (p == 3 ? 0 : p) + 5 * wn + 4) * 8;
    }
  }

  f32x4 acc[MT][4];
#pragma unroll
  for (int m = 0; m < MT; ++m)
#pragma unroll
    for (int j = 0; j < 4; ++j) acc[m][j] = f32x4{0.f, 0.f, 0.f, 0.f};
  bf16x8 fa0[MT], fa1[MT];
  s16x4 fb0[8], fb1[8];

  STAMPAT(1);
  WAITN_BAR(13);
  STAMPAT(2);
  unsigned wpk[8];
  if (!GU) {
    asm volatile("" : "+v"(wraw[0]), "+v"(wraw[1]), "+v"(wraw[2]), "+v"(wraw[3]), "+v"(wraw[4]), "+v"(wraw[5]), "+v"(wraw[6]), "+v"(wraw[7]),
                 "+v"(wraw[8]), "+v"(wraw[9]), "+v"(wraw[10]), "+v"(wraw[11]), "+v"(wraw[12]), "+v"(wraw[13]), "+v"(wraw[14]), "+v"(wraw[15]));
#pragma unroll
    for (int i = 0; i < 8; ++i) {
      wpk[i] = wraw[2 * i] | (wraw[2 * i + 1] << 16);
      if (i >= 4 && (fr & 3) != 0) wpk[i] = 0u;
    }
    asm volatile("" : "+v"(wpk[0]), "+v"(wpk[1]), "+v"(wpk[2]), "+v"(wpk[3]), "+v"(wpk[4]), "+v"(wpk[5]), "+v"(wpk[6]), "+v"(wpk[7]));
  }
#pragma unroll
  for (int m = 0; m < MT; ++m) fa0[m] = ld128(sm + aoff0 + m * 2048);
#pragma unroll
  for (int j = 0; j < 8; ++j) fb0[j] = tr1(sm + boff[j >> 1] + (j & 1) * TSTEP);
  MEMFENCE();

  constexpr int NM = MT * 4;
  constexpr int DS = (MT == 6) ? 6 : 5;
#define MMA(i, FA, FB) acc[(i) >> 2][(i) & 3] = mfma32(cat(FB[2 * ((i) & 3)], FB[2 * ((i) & 3) + 1]), FA[(i) >> 2], acc[(i) >> 2][(i) & 3])
#define BADDR(sl, i) (sm + (sl) * B_SLOT + boff[(i) >> 1] + ((i) & 1) * TSTEP)
  u32x4 ackv, stv[MT];
  const char* ackp = ws + WS_ACK + bid * 64;
#define STP(m) (ws + WS_STAT + (size_t)(r0 + strow(mh * 96 + (m) * 16 + fr, nrow)) * 16)
#pragma unroll
  for (int t = 0; t < 10; ++t) {
    SCHED0();
    WAITN_BAR(t < 8 ? 11 : (t == 8 ? 9 : 2));
    SCHED0();
#pragma unroll
    for (int i = 0; i < NM; ++i) {
      MMA(i, fa0, fb0);
      SCHED0();
      if (i < 8) fb1[i] = tr1(BADDR((2 * t + 1) % 5, i));
      else if (i < 8 + MT) fa1[i - 8] = ld128(sm + (t % 3) * A_SLOT + aoff1 + (i - 8) * 2048);
      if (t < 8) {
        if (i == 2) { if (MT == 6) DMA_A2((t + 2) * 128, adst0 + 16384 + ((t + 2) % 3) * A_SLOT); else DMA_A2((t + 2) * 128, a2base + a2mul * (((t + 2) % 3) * A_SLOT)); }
        if (i == 2 + DS) DMA_B0((2 * t + 5) * SSTEP, bdst0 + ((2 * t) % 5) * B_SLOT);
        if (i == 2 + 2 * DS) DMA_B1((2 * t + 5) * SSTEP, bdst0 + ((2 * t) % 5) * B_SLOT + 8192);
      }
      if (i == NM - 1 && t == 7) {
        const char* g0_ = ws + WS_STAT + (size_t)(r0 + strow(mh * 96 + lane, nrow)) * 16;
        const char* g1_ = lane < 32 ? ws + WS_STAT + (size_t)(r0 + strow(mh * 96 + 64 + lane, nrow)) * 16 : ackp;
        if (wn == 0) {
          asm volatile("s_mov_b32 m0, %1\n\ts_nop 0\n\tglobal_load_lds_dwordx4 %0, off sc1" ::"v"(g0_), "s"(smb + L_ST + mh * 2048) : "memory");
          asm volatile("s_mov_b32 m0, %1\n\ts_nop 0\n\tglobal_load_lds_dwordx4 %0, off sc1" ::"v"(g1_), "s"(smb + L_ST + mh * 2048 + 1024) : "memory");
        } else {
          dma_b(srdx, 0x40000000u, 0, smb + L_DUMMY);
          dma_b(srdx, 0x40000000u, 0, smb + L_DUMMY);
        }
      }
      SCHED0();
    }
    if (t < 9) WAITN_BAR(t < 7 ? 9 : (t == 7 ? 11 : 4)); else WAITN_BAR(0);
    SCHED0();
#pragma unroll
    for (int i = 0; i < NM; ++i) {
      MMA(i, fa1, fb1);
      SCHED0();
      if (t < 9) {
        if (i < 8) fb0[i] = tr1(BADDR((2 * t + 2) % 5, i));
        else if (i < 8 + MT) fa0[i - 8] = ld128(sm + ((t + 1) % 3) * A_SLOT + aoff0 + (i - 8) * 2048);
      }
      if (t < 7) {
        if (i == 1) DMA_B0((2 * t + 6) * SSTEP, bdst0 + ((2 * t + 1) % 5) * B_SLOT);
        if (i == 1 + DS) DMA_B1((2 * t + 6) * SSTEP, bdst0 + ((2 * t + 1) % 5) * B_SLOT + 8192);
        if (i == 1 + 2 * DS) DMA_A0((t + 3) * 128, adst0 + (t % 3) * A_SLOT);
        if (i == 1 + 3 * DS) DMA_A1((t + 3) * 128, adst0 + (t % 3) * A_SLOT + 8192);
      }
      SCHED0();
    }
  }
  SCHED0();
  STAMPAT(3);

  if (fr == 15) {
#pragma unroll
    for (int j = 0; j < 4; ++j) *(LAS f32x4*)(sm + L_CS + w * 256 + j * 64 + g * 16) = acc[MT - 1][j];
  }
  asm volatile("s_waitcnt vmcnt(0) lgkmcnt(0)" ::: "memory");
  ackv = *(const LAS u32x4*)(sm + L_ST + mh * 2048 + 1536);
#pragma unroll
  for (int m = 0; m < MT; ++m) stv[m] = *(const LAS u32x4*)(sm + L_ST + mh * 2048 + (m * 16 + fr) * 16);
  f32x4 cs[4];
#pragma unroll
  for (int j = 0; j < 4; ++j) cs[j] = *(const LAS f32x4*)(sm + L_CS + w * 256 + j * 64 + g * 16);
  {
    const u32x2 nv = *(const LAS u32x2*)(sm + L_NONCE);
    for (unsigned spins = 0; spins < SPIN_MAX; ++spins) {
      bool ok = (ackv[0] == nv[0]) && (ackv[1] == nv[1]);
#pragma unroll
      for (int m = 0; m < MT; ++m) ok = ok && (stv[m][2] == ackv[2]) && (stv[m][3] == ackv[3]);
      if (all_ok(ok)) break;
      __builtin_amdgcn_s_sleep(2);
#pragma unroll
      for (int m = 0; m < MT; ++m) asm volatile("global_load_dwordx4 %0, %1, off sc1" : "=&v"(stv[m]) : "v"(STP(m)) : "memory");
      ackv = ld128_sc1(ackp);
#pragma unroll
      for (int m = 0; m < MT; ++m) asm volatile("" : "+v"(stv[m]) :: "memory");
    }
  }
  const u32x2 ep = u32x2{ackv[2], ackv[3]};
  if (w == 0 && lane == 0) st64_sc1(ws + WS_DONE + bid * 64, ep);
  if (GU) {
    const unsigned vB = p2_vB(w, lane);
    p2_issueB_down(srd_wd, vB, c0p2, w, smb, 0, rot);
    p2_issueB_down(srd_wd, vB, c0p2, w, smb, 1, rot);
    p2_issueB_down(srd_wd, vB, c0p2, w, smb, 2, rot);
  }
  float rstd[MT];
#pragma unroll
  for (int m = 0; m < MT; ++m) {
    const f32x2 nm = sp2(opq(-u2f(stv[m][0])));
    rstd[m] = opq(u2f(stv[m][1]));
#pragma unroll
    for (int j = 0; j < 4; ++j) {
      f32x2 l = fma2(nm, lo2(cs[j]), lo2(acc[m][j])), h = fma2(nm, hi2(cs[j]), hi2(acc[m][j]));
      acc[m][j] = f32x4{l[0], l[1], h[0], h[1]};
    }
  }
  STAMPAT(4);

  if (GU) {
    const float K0 = -2.0f * 0.7978845608028654f * 1.4426950408889634f;
    const float K1c = K0 * 0.044715f;
    ldsp stg = sm + w * 7680;
    char* hg = ws + WS_H + ct * 256 + wn * 64 + (lane & 3) * 16;
#pragma unroll
    for (int m = 0; m <= MT; ++m) {
      if (m < MT) {
        const float r = rstd[m], r2 = r * r;
        const f32x2 c0 = sp2(opq(K0 * r)), c1 = sp2(opq(K1c * r2 * r)), ir2 = sp2(opq(__builtin_amdgcn_rcpf(r2)));
        u32x2 hp[2];
#pragma unroll
        for (int j = 0; j < 2; ++j) {
#pragma unroll
          for (int q = 0; q < 2; ++q) {
            const f32x2 a = q ? hi2(acc[m][j]) : lo2(acc[m][j]), b = q ? hi2(acc[m][j + 2]) : lo2(acc[m][j + 2]);
            const f32x2 z = a * fma2(a * a, c1, c0);
            const f32x2 e = f32x2{__builtin_amdgcn_exp2f(z[0]), __builtin_amdgcn_exp2f(z[1])};
            const f32x2 d = fma2(e, ir2, ir2);
            const f32x2 hv = (a * b) * f32x2{__builtin_amdgcn_rcpf(d[0]), __builtin_amdgcn_rcpf(d[1])};
            hp[j][q] = pk2(hv[0], hv[1]);
          }
        }
        *(LAS u32x2*)(stg + (m * 16 + fr) * 80 + g * 8) = hp[0];
        *(LAS u32x2*)(stg + (m * 16 + fr) * 80 + 32 + g * 8) = hp[1];
      }
      if (m >= 1) {
        const int rl = (m - 1) * 16 + (lane >> 2);
        u32x4 v = *(const LAS u32x4*)(stg + rl * 80 + (lane & 3) * 16);
        const int sl = mh * 96 + rl;
        const int ri = sl < 95 ? sl : sl - 1;
        if (sl != 95 && ri < nrow) st128_sc1(hg + (size_t)(r0 + ri) * 3072, v);
      }
    }
    STAMPAT(5);
    WAITN_BAR(0);
    if (w == 0 && lane == 0) st64_sc1(ws + WS_HF + rg * 128 + ct * 8, ep);
    STAMPAT(6);
  } else {
    const int head = ct * 4 + wn;
    bf16x8 wq[2], wk[2];
#pragma unroll
    for (int ft = 0; ft < 2; ++ft) {
      wq[ft] = asb(u32x4{wpk[4 * ft], wpk[4 * ft + 1], g == 0 ? wpk[4 * ft + 2] : 0u, g == 0 ? wpk[4 * ft + 3] : 0u});
      wk[ft] = asb(u32x4{wpk[4 * ft], wpk[4 * ft + 1], g == 1 ? wpk[4 * ft + 2] : 0u, g == 1 ? wpk[4 * ft + 3] : 0u});
    }
    const unsigned tag = ep[0] ^ (ep[1] * 0x9E3779B1u);
    const float L2E = 1.4426950408889634f;
    const f32x4 z4 = f32x4{0.f, 0.f, 0.f, 0.f};
    bool live[6];
    u32x2 rem[6];
#pragma unroll
    for (int m = 0; m < 6; ++m) {
      int s = mh * 96 + m * 16 + fr;
      int ri = s < 95 ? s : s - 1;
      live[m] = (m < MT) && (s != 95) && (ri < nrow);
      if (m < MT) rem[m] = u32x2{pk2(acc[m][3][0], acc[m][3][1]), pk2(acc[m][3][2], acc[m][3][3])};
    }
    ldsp pk_img = sm + w * 17408;
    ldsp v_img = pk_img + 96 * 104;
    {
      unsigned kf[6][5];
      u32x2 vv[6], ve[6];
#pragma unroll
      for (int mp = 0; mp < 3; ++mp) {
        f32x2 y01[2], y23[2], y4;
#pragma unroll
        for (int hh = 0; hh < 2; ++hh) {
          const int m = 2 * mp + hh;
          if (m < MT) {
            const f32x2 r = sp2(rstd[m]);
            const float c = opq(rstd[m] * L2E);
            u32x4 kb = u32x4{pk2(acc[m][1][0], acc[m][1][1]), pk2(acc[m][1][2], acc[m][1][3]), rem[m][0], rem[m][1]};
            f32x4 k0 = mfma32(wk[0], asb(kb), z4);
            f32x4 k1 = mfma32(wk[1], asb(kb), z4);
            y01[hh] = lo2(k0) * sp2(c); y23[hh] = hi2(k0) * sp2(c); y4[hh] = k1[0] * c;
            vv[m] = u32x2{pk2v(lo2(acc[m][2]) * r), pk2v(hi2(acc[m][2]) * r)};
            u32x2 vr = u32x2{pk2v(lo2(acc[m][3]) * r), pk2v(hi2(acc[m][3]) * r)};
            ve[m] = (g == 2) ? vr : u32x2{0x3F803F80u, 0x3F803F80u};
          } else {
            y01[hh] = sp2(0.f); y23[hh] = sp2(0.f); y4[hh] = 0.f;
            vv[m] = u32x2{0u, 0u}; ve[m] = vv[m];
          }
        }
        featmap2(y01[0], y23[0], y01[1], y23[1], y4, live[2 * mp], live[2 * mp + 1], g, kf[2 * mp], kf[2 * mp + 1]);
      }
      SCHED0();
#pragma unroll
      for (int m = 0; m < 6; ++m) {
        int tok = m * 16 + fr;
        *(LAS u32x2*)(pk_img + tok * 104 + g * 8) = u32x2{kf[m][0], kf[m][1]};
        *(LAS u32x2*)(pk_img + tok * 104 + 32 + g * 8) = u32x2{kf[m][2], kf[m][3]};
        *(LAS u32x2*)(pk_img + tok * 104 + 64 + g * 8) = u32x2{kf[m][4], 0u};
        *(LAS u32x2*)(v_img + tok * 72 + g * 8) = vv[m];
        *(LAS u32x2*)(v_img + tok * 72 + 32 + (g ^ 2) * 8) = ve[m];
      }
      LGKM0();
      SCHED0();
    }
    f32x4 kv[3][2];
#pragma unroll
    for (int ft = 0; ft < 3; ++ft)
#pragma unroll
      for (int dt = 0; dt < 2; ++dt) kv[ft][dt] = z4;
    {
      const int q = fr >> 2, p = lane & 3;
#pragma unroll
      for (int s = 0; s < 3; ++s) {
        int row = 32 * s + 8 * g + q;
        bf16x8 a[3], b[2];
#pragma unroll
        for (int ft = 0; ft < 3; ++ft) a[ft] = tr2(pk_img + row * 104 + ft * 32 + p * 8, pk_img + (row + 4) * 104 + ft * 32 + p * 8);
#pragma unroll
        for (int dt = 0; dt < 2; ++dt) b[dt] = tr2(v_img + row * 72 + dt * 32 + p * 8, v_img + (row + 4) * 72 + dt * 32 + p * 8);
#pragma unroll
        for (int ft = 0; ft < 3; ++ft)
#pragma unroll
          for (int dt = 0; dt < 2; ++dt) kv[ft][dt] = mfma32(a[ft], b[dt], kv[ft][dt]);
      }
    }
    {
      f32x4 o5[5] = {kv[0][0], kv[1][0], kv[0][1], kv[1][1], f32x4{kv[2][0][0], kv[2][0][1], kv[2][1][0], kv[2][1][1]}};
      ldsp ex = sm + (4 + wn) * 17408;
      if (mh == 1) {
        LGKM0();
#pragma unroll
        for (int i = 0; i < 5; ++i) *(LAS f32x4*)(ex + i * 1024 + lane * 16) = o5[i];
      }
      LGKM_BAR();
      if (mh == 0) {
        unsigned pp[10];
#pragma unroll
        for (int i = 0; i < 5; ++i) {
          f32x4 e = *(const LAS f32x4*)(ex + i * 1024 + lane * 16);
          pp[2 * i] = pk2(o5[i][0] + e[0], o5[i][1] + e[1]);
          pp[2 * i + 1] = pk2(o5[i][2] + e[2], o5[i][3] + e[3]);
        }
        char* kp = ws + WS_KV + (size_t)(rg * NH + head) * 4096 + lane * 16;
        st128_sc1(kp, u32x4{pp[0], pp[1], pp[2], tag});
        st128_sc1(kp + 1024, u32x4{pp[3], pp[4], pp[5], tag});
        st128_sc1(kp + 2048, u32x4{pp[6], pp[7], pp[8], tag});
        st128_sc1(kp + 3072, u32x4{pp[9], 0u, 0u, tag});
      }
    }
    STAMPAT(5);
    unsigned qf[6][5];
#pragma unroll
    for (int mp = 0; mp < 3; ++mp) {
      f32x2 y01[2], y23[2], y4;
#pragma unroll
      for (int hh = 0; hh < 2; ++hh) {
        const int m = 2 * mp + hh;
        if (m < MT) {
          const float c = opq(rstd[m] * L2E);
          u32x4 qb = u32x4{pk2(acc[m][0][0], acc[m][0][1]), pk2(acc[m][0][2], acc[m][0][3]), rem[m][0], rem[m][1]};
          f32x4 q0 = mfma32(wq[0], asb(qb), z4);
          f32x4 q1 = mfma32(wq[1], asb(qb), z4);
          y01[hh] = lo2(q0) * sp2(c); y23[hh] = hi2(q0) * sp2(c); y4[hh] = q1[0] * c;
        } else {
          y01[hh] = sp2(0.f); y23[hh] = sp2(0.f); y4[hh] = 0.f;
        }
      }
      featmap2(y01[0], y23[0], y01[1], y23[1], y4, true, true, g, qf[2 * mp], qf[2 * mp + 1]);
    }
    STAMPAT(6);
    float ks[20];
    {
      {
        const char* p1 = ws + WS_KV + ((size_t)(mh * 6 + (lane < 6 ? lane : 0)) * NH + head) * 4096 + 3072 + 1008;
        for (unsigned spins = 0; spins < SPIN_MAX; ++spins) {
          u32x4 v = ld128_sc1(p1);
          if (all_ok(v[3] == tag)) break;
          __builtin_amdgcn_s_sleep(POLLS);
        }
      }
      u32x4 pv[6][4];
      const char* kp = ws + WS_KV + (size_t)((mh * 6) * NH + head) * 4096 + lane * 16;
      for (unsigned spins = 0; spins < SPIN_MAX; ++spins) {
#pragma unroll
        for (int c = 0; c < 6; ++c) {
          const char* p = kp + (size_t)c * NH * 4096;
          asm volatile("global_load_dwordx4 %0, %4, off sc1\n\tglobal_load_dwordx4 %1, %4, off offset:1024 sc1\n\t"
                       "global_load_dwordx4 %2, %4, off offset:2048 sc1\n\tglobal_load_dwordx4 %3, %4, off offset:3072 sc1"
                       : "=&v"(pv[c][0]), "=&v"(pv[c][1]), "=&v"(pv[c][2]), "=&v"(pv[c][3]) : "v"(p) : "memory");
        }
        asm volatile("s_waitcnt vmcnt(0)" : "+v"(pv[0][0]), "+v"(pv[0][1]), "+v"(pv[0][2]), "+v"(pv[0][3]), "+v"(pv[1][0]), "+v"(pv[1][1]), "+v"(pv[1][2]), "+v"(pv[1][3]),
                     "+v"(pv[2][0]), "+v"(pv[2][1]), "+v"(pv[2][2]), "+v"(pv[2][3]), "+v"(pv[3][0]), "+v"(pv[3][1]), "+v"(pv[3][2]), "+v"(pv[3][3]),
                     "+v"(pv[4][0]), "+v"(pv[4][1]), "+v"(pv[4][2]), "+v"(pv[4][3]), "+v"(pv[5][0]), "+v"(pv[5][1]), "+v"(pv[5][2]), "+v"(pv[5][3]) :: "memory");
        bool ok = true;
#pragma unroll
        for (int c = 0; c < 6; ++c)
#pragma unroll
          for (int i = 0; i < 4; ++i) ok = ok && (pv[c][i][3] == tag);
        if (all_ok(ok)) break;
        __builtin_amdgcn_s_sleep(2);
      }
      STAMPAT(7);
#pragma unroll
      for (int i = 0; i < 10; ++i) {
        float lo = 0.f, hi = 0.f;
#pragma unroll
        for (int c = 0; c < 6; ++c) {
          unsigned u = pv[c][i / 3][i % 3];
          lo += u2f(u << 16); hi += u2f(u & 0xffff0000u);
        }
        ks[2 * i] = lo; ks[2 * i + 1] = hi;
      }
      ldsp mine = sm + w * 17408 + 12288, other = sm + (w ^ 4) * 17408 + 12288;
      SCHED0();
#pragma unroll
      for (int i = 0; i < 5; ++i) *(LAS f32x4*)(mine + i * 1024 + lane * 16) = f32x4{ks[4 * i], ks[4 * i + 1], ks[4 * i + 2], ks[4 * i + 3]};
      LGKM_BAR();
#pragma unroll
      for (int i = 0; i < 5; ++i) {
        f32x4 e = *(const LAS f32x4*)(other + i * 1024 + lane * 16);
#pragma unroll
        for (int q = 0; q < 4; ++q) ks[4 * i + q] = (mh == 0) ? ks[4 * i + q] + e[q] : e[q] + ks[4 * i + q];
      }
    }
    const bf16x8 ka0 = asb(u32x4{pk2(ks[0], ks[1]), pk2(ks[2], ks[3]), pk2(ks[4], ks[5]), pk2(ks[6], ks[7])});
    const bf16x8 ka1 = asb(u32x4{pk2(ks[8], ks[9]), pk2(ks[10], ks[11]), pk2(ks[12], ks[13]), pk2(ks[14], ks[15])});
    const bf16x8 kb0 = asb(u32x4{pk2(ks[16], ks[17]), 0u, 0u, 0u});
    const bf16x8 kb1 = asb(u32x4{pk2(ks[18], ks[19]), 0u, 0u, 0u});
    {
      u32x2 om_[MT], or_[MT];
#pragma unroll
      for (int m = 0; m < MT; ++m) {
        bf16x8 b0 = asb(u32x4{qf[m][0], qf[m][1], qf[m][2], qf[m][3]});
        bf16x8 b1 = asb(u32x4{qf[m][4], 0u, 0u, 0u});
        f32x4 n0 = mfma32(kb0, b1, mfma32(ka0, b0, z4));
        f32x4 n1 = mfma32(kb1, b1, mfma32(ka1, b0, z4));
        float da = n1[0], db = n1[0];
        swap16(da, db);
        const f32x2 inv = sp2(__builtin_amdgcn_rcpf(db + 1e-6f));
        om_[m] = u32x2{pk2v(lo2(n0) * inv), pk2v(hi2(n0) * inv)};
        or_[m] = u32x2{pk2v(lo2(n1) * inv), pk2v(hi2(n1) * inv)};
      }
      SCHED0();
      ldsp stg = sm + w * 17408 + 8192;
#pragma unroll
      for (int m = 0; m < MT; ++m) {
        *(LAS u32x2*)(stg + (m * 16 + fr) * 32 + g * 8) = om_[m];
        if (g == 0) *(LAS u32x2*)(stg + 3072 + (m * 16 + fr) * 8) = or_[m];
      }
      LGKM0();
      const int tok0 = r0 + mh * 95;
      const int ntok = mh == 0 ? 95 : nrow - 95;
      char* om = ws + WS_OM + ((size_t)head * S_ + tok0) * 32;
      char* orr = ws + WS_OR + ((size_t)head * S_ + tok0) * 8;
      u32x4 tv[3];
      u32x2 tr[2];
#pragma unroll
      for (int i = 0; i < 3; ++i) tv[i] = *(const LAS u32x4*)(stg + i * 1024 + lane * 16);
#pragma unroll
      for (int i = 0; i < 2; ++i) tr[i] = *(const LAS u32x2*)(stg + 3072 + ((i * 64 + lane) < 96 ? (i * 64 + lane) : 0) * 8);
#pragma unroll
      for (int i = 0; i < 3; ++i)
        if (i * 32 + (lane >> 1) < ntok) st128_sc1(om + i * 1024 + lane * 16, tv[i]);
#pragma unroll
      for (int i = 0; i < 2; ++i)
        if (i * 64 + lane < ntok) st64_sc1(orr + (i * 64 + lane) * 8, tr[i]);
    }
    WAITN_BAR(0);
    if (w == 0 && lane == 0) st64_sc1(ws + WS_OF + rg * 128 + ct * 8, ep);
    STAMPAT(8);
  }
  return ep;
}

DI u32x2 service(ldsp sm, const char* x, char* ws, int sid, int w, int lane) {
  const int bid = __builtin_amdgcn_workgroup_id_x();
  const int g = lane >> 4, fr = lane & 15;
  STAMPAT(0);
  if (w == 0) {
    unsigned long long nn = __builtin_amdgcn_s_memrealtime();
    u32x2 nv = u32x2{(unsigned)nn, (unsigned)(nn >> 32)};
    if (lane == 0) {
      if (sid != 15) st64_sc1(ws + WS_REQ + bid * 64, nv);
      *(LAS u32x2*)(sm + L_NONCE) = nv;
    }
  }
  LGKM_BAR();
  const u32x2 nv = *(const LAS u32x2*)(sm + L_NONCE);
  u32x2 ep = u32x2{nv[0] ^ 0x5bd1e995u, nv[1] ^ 0x80000000u};
  if (sid == 15 && w < 4) {
    const int cb = w * 64 + lane;
    const bool me = (cb == bid);
    u32x2 lastq = u32x2{0u, 0u};
    for (unsigned spins = 0; spins < SPIN_MAX; ++spins) {
      u32x2 rq, dn;
      asm volatile("global_load_dwordx2 %0, %2, off sc1\n\tglobal_load_dwordx2 %1, %3, off sc1\n\ts_waitcnt vmcnt(0)"
                   : "=&v"(rq), "=&v"(dn) : "v"(ws + WS_REQ + cb * 64), "v"(ws + WS_DONE + cb * 64) : "memory");
      if (all_ok(me || (dn[0] == ep[0] && dn[1] == ep[1]))) break;
      if (spins == 0 || rq[0] != lastq[0] || rq[1] != lastq[1]) st128_sc1(ws + WS_ACK + cb * 64, u32x4{rq[0], rq[1], ep[0], ep[1]});
      lastq = rq;
      if (spins > 12) __builtin_amdgcn_s_sleep(16);
    }
  } else {
    const int npass = sid == 15 ? 2 : 1;
    bool have = (sid == 15);
#pragma unroll 1
    for (int ps = 0; ps < npass; ++ps) {
      const int rbase = sid * 128 + (sid == 15 ? (w - 4) * 32 + ps * 16 : w * 16) + g;
      u32x4 v[4][5];
#pragma unroll
      for (int i = 0; i < 4; ++i)
#pragma unroll
        for (int c = 0; c < 5; ++c) v[i][c] = *(const u32x4*)(x + (size_t)(rbase + 4 * i) * 1280 + fr * 16 + c * 256);
      float mean[4], rstd[4];
#pragma unroll
      for (int i = 0; i < 4; ++i) {
        f32x2 a = sp2(0.f);
#pragma unroll
        for (int c = 0; c < 5; ++c)
#pragma unroll
          for (int e = 0; e < 4; ++e) a += f32x2{u2f(v[i][c][e] << 16), u2f(v[i][c][e] & 0xffff0000u)};
        float s = a[0] + a[1];
        s += dpp<0xB1>(s); s += dpp<0x4E>(s); s += dpp<0x141>(s); s += dpp<0x140>(s);
        mean[i] = s * (1.0f / 640.0f);
        const f32x2 m2 = sp2(opq(mean[i]));
        f32x2 b = sp2(0.f);
#pragma unroll
        for (int c = 0; c < 5; ++c)
#pragma unroll
          for (int e = 0; e < 4; ++e) {
            const f32x2 d = f32x2{u2f(v[i][c][e] << 16), u2f(v[i][c][e] & 0xffff0000u)} - m2;
            b = fma2(d, d, b);
          }
        float t = b[0] + b[1];
        t += dpp<0xB1>(t); t += dpp<0x4E>(t); t += dpp<0x141>(t); t += dpp<0x140>(t);
        rstd[i] = __builtin_amdgcn_rsqf(t * (1.0f / 640.0f) + 1e-5f);
      }
      if (!have) {
        u32x4 ack;
        for (unsigned spins = 0; spins < SPIN_MAX; ++spins) {
          ack = ld128_sc1(ws + WS_ACK + bid * 64);
          if (all_ok(ack[0] == nv[0] && ack[1] == nv[1])) break;
          __builtin_amdgcn_s_sleep(2);
        }
        ep = u32x2{ack[2], ack[3]};
        have = true;
        if (w == 0 && lane == 0) st64_sc1(ws + WS_DONE + bid * 64, ep);
      }
#pragma unroll
      for (int i = 0; i < 4; ++i)
        if (fr == 0) st128_sc1(ws + WS_STAT + (size_t)(rbase + 4 * i) * 16, u32x4{f2u(mean[i]), f2u(rstd[i]), ep[0], ep[1]});
    }
  }
  STAMPAT(1);
  return ep;
}

__global__ __launch_bounds__(512, 2) void fused(const char* __restrict__ x, const char* __restrict__ wqkv,
                                                const char* __restrict__ wphi, const char* __restrict__ wo,
                                                const char* __restrict__ wg, const char* __restrict__ wu,
                                                const char* __restrict__ wd, char* __restrict__ ws, char* __restrict__ out) {
  extern __shared__ __attribute__((aligned(16))) char smem[];
  ldsp sm = (ldsp)smem;
  const int tid = __builtin_amdgcn_workitem_id_x();
  const int bp = __builtin_amdgcn_workgroup_id_x(), xcd = bp & 7, ip = bp >> 3;
  const int idx = ip < 2 ? 30 + ip : ip - 2;
  const int b = idx * 8 + xcd;
  const int w = __builtin_amdgcn_readfirstlane(tid >> 6), lane = tid & 63;
  const u32x4 srd_wd = make_srd(wd, DF_ * D_ * 2);
  const int rgl = idx / 5, c5 = idx % 5;
  const int rg = (xcd >> 2) * 6 + rgl, cq = xcd & 3;
  if (idx < 30 && c5 >= 3) {
    const int ct = cq * 2 + (c5 - 3);
    if (w < 4) phase1<6, false>(sm, x, wqkv, wphi, wg, wu, srd_wd, ws, rg, ct, 0, 0, w, lane);
    else phase1<5, false>(sm, x, wqkv, wphi, wg, wu, srd_wd, ws, rg, ct, 0, 0, w, lane);
    return;
  }
  const int p2i = idx < 30 ? rgl * 3 + c5 : 18 + (idx - 30);
  const int rt = (xcd >> 1) * 4 + p2i / 5, ct2 = (xcd & 1) * 5 + p2i % 5;
#define ROTF ((((p2i % 5) * 12) / 5 + (p2i / 5) * 3) % 12)
  const int rot = ROTF;
  u32x2 ep;
  if (idx < 30) {
    const int ct = cq * 3 + c5;
    if (w < 4) ep = phase1<6, true>(sm, x, wqkv, wphi, wg, wu, srd_wd, ws, rg, ct, ct2 * 64, rot, w, lane);
    else ep = phase1<5, true>(sm, x, wqkv, wphi, wg, wu, srd_wd, ws, rg, ct, ct2 * 64, rot, w, lane);
  } else {
    ep = service(sm, x, ws, b - 240, w, lane);
    {
      const u32x4 srd_wo = make_srd(wo, D_ * D_ * 2);
      const unsigned vp = (lane >> 3) * 1280 + (lane & 7) * 16 + (xcd & 1) * 640;
      const unsigned dst = (unsigned)(size_t)sm + w * 1024;
      const int w16 = (idx - 30) * 8 + w;
      if (!(b == 255 && w < 4)) {
#pragma unroll 1
        for (int p = w16; p < 192; p += 16) {
#pragma unroll
          for (int c = 0; c < 5; ++c) dma_b(srd_wd, vp, p * 8 * 1280 + c * 128, dst);
          asm volatile("s_waitcnt vmcnt(30)" ::: "memory");
        }
#pragma unroll 1
        for (int p = w16; p < 80; p += 16) {
#pragma unroll
          for (int c = 0; c < 5; ++c) dma_b(srd_wo, vp, p * 8 * 1280 + c * 128, dst);
          asm volatile("s_waitcnt vmcnt(30)" ::: "memory");
        }
      }
      WAITN_BAR(0);
    }
    const unsigned vB = p2_vB(w, lane);
    const unsigned smb = (unsigned)(size_t)sm;
    p2_issueB_down(srd_wd, vB, ct2 * 64, w, smb, 0, rot);
    p2_issueB_down(srd_wd, vB, ct2 * 64, w, smb, 1, rot);
    p2_issueB_down(srd_wd, vB, ct2 * 64, w, smb, 2, rot);
  }
  phase2(sm, x, wo, srd_wd, ws, out, rt, ct2, rot, w, lane, ep);
}

#if defined(__HIP_DEVICE_COMPILE__)

#endif

extern "C" void kernel_launch(void* const* d_in, const int* in_sizes, int n_in, void* d_out, int out_size, void* d_ws,
                              size_t ws_size, hipStream_t stream) {
  (void)hipFuncSetAttribute((const void*)fused, hipFuncAttributeMaxDynamicSharedMemorySize, K_LDS);
  fused<<<256, 512, K_LDS, stream>>>((const char*)d_in[0], (const char*)d_in[1], (const char*)d_in[2], (const char*)d_in[3],
                                     (const char*)d_in[5], (const char*)d_in[6], (const char*)d_in[7], (char*)d_ws, (char*)d_out);
}
```

```cpp
#define ORDER 2
#define POLLS 6
#include <hip/hip_runtime.h>
#include <hip/hip_bf16.h>
#include <stdint.h>

typedef __attribute__((ext_vector_type(8))) __bf16 bf16x8;
typedef __attribute__((ext_vector_type(2))) __bf16 bf16x2;
typedef __attribute__((ext_vector_type(4))) short s16x4;
typedef __attribute__((ext_vector_type(8))) short s16x8;
typedef __attribute__((ext_vector_type(4))) float f32x4;
typedef __attribute__((ext_vector_type(2))) float f32x2;
typedef __attribute__((ext_vector_type(4))) unsigned u32x4;
typedef __attribute__((ext_vector_type(2))) unsigned u32x2;
#define LAS __attribute__((address_space(3)))
#define DI __device__ __forceinline__
typedef LAS char* ldsp;

constexpr int S_ = 2048, D_ = 640, DF_ = 1536, NH = 32;
constexpr int NRG = 12;
constexpr size_t WS_H = 0;
constexpr size_t WS_KV = WS_H + (size_t)S_ * DF_ * 2;
constexpr size_t WS_OM = WS_KV + (size_t)NRG * NH * 4096;
constexpr size_t WS_OR = WS_OM + (size_t)NH * S_ * 32;
constexpr size_t WS_REQ = WS_OR + (size_t)NH * S_ * 8;
constexpr size_t WS_ACK = WS_REQ + 256 * 64;
constexpr size_t WS_DONE = WS_ACK + 256 * 64;
constexpr size_t WS_STAT = WS_DONE + 256 * 64;
constexpr size_t WS_HF = WS_STAT + 2048 * 16;
constexpr size_t WS_KF = WS_HF + 12 * 128;
constexpr size_t WS_OF = WS_KF + 8 * 128;
constexpr size_t WS_DBG = 64u << 20;

constexpr int A_SLOT = 22528, B_SLOT = 16384;
constexpr int L_A = 0;
constexpr int L_B = 3 * A_SLOT;
constexpr int L_MISC = L_B + 5 * B_SLOT;
constexpr int L_DUMMY = L_MISC;
constexpr int L_NONCE = L_MISC + 1024;
constexpr int L_CS = L_NONCE + 64;
constexpr int K_LDS = L_CS + 2048;
constexpr int P2_A = 0, P2_B = 98304;
constexpr unsigned SPIN_MAX = 1u << 20;

#if defined(__HIP_DEVICE_COMPILE__)

#endif

#define STAMPAT(i)

DI float u2f(unsigned u) { return __builtin_bit_cast(float, u); }
DI unsigned f2u(float f) { return __builtin_bit_cast(unsigned, f); }
DI u32x4 make_srd(const void* p, unsigned bytes) {
  uint64_t a = (uint64_t)p;
  u32x4 r;
  r[0] = (unsigned)a;
  r[1] = (unsigned)(a >> 32) & 0xffffu;
  r[2] = bytes;
  r[3] = 0x00020000u;
  return r;
}
DI void dma_b(u32x4 srd, unsigned voff, unsigned soff, unsigned m0v) {
  asm volatile("s_mov_b32 m0, %3\n\ts_nop 0\n\tbuffer_load_dwordx4 %0, %1, %2 offen lds" ::"v"(voff), "s"(srd), "s"(soff), "s"(m0v) : "memory");
}
DI void dma_b_mask(u32x4 srd, unsigned voff, unsigned soff, unsigned m0v, unsigned long long mask) {
  asm volatile("s_mov_b32 m0, %3\n\ts_mov_b64 exec, %4\n\tbuffer_load_dwordx4 %0, %1, %2 offen lds\n\ts_mov_b64 exec, -1" ::"v"(voff), "s"(srd), "s"(soff), "s"(m0v), "s"(mask) : "memory");
}
DI void dma_b_sc1(u32x4 srd, unsigned voff, unsigned soff, unsigned m0v) {
  asm volatile("s_mov_b32 m0, %3\n\ts_nop 0\n\tbuffer_load_dwordx4 %0, %1, %2 offen sc1 lds" ::"v"(voff), "s"(srd), "s"(soff), "s"(m0v) : "memory");
}
DI u32x4 ld128_sc1(const char* p) { u32x4 v; asm volatile("global_load_dwordx4 %0, %1, off sc1\n\ts_waitcnt vmcnt(0)" : "=&v"(v) : "v"(p) : "memory"); return v; }
DI u32x2 ld64_sc1(const char* p) { u32x2 v; asm volatile("global_load_dwordx2 %0, %1, off sc1\n\ts_waitcnt vmcnt(0)" : "=&v"(v) : "v"(p) : "memory"); return v; }
DI void st128_sc1(char* p, u32x4 v) { asm volatile("global_store_dwordx4 %0, %1, off sc1\n\ts_nop 1" ::"v"(p), "v"(v) : "memory"); }
DI void st64_sc1(char* p, u32x2 v) { asm volatile("global_store_dwordx2 %0, %1, off sc1" ::"v"(p), "v"(v) : "memory"); }
#define WAITN_BAR(N) asm volatile("s_waitcnt vmcnt(%0) lgkmcnt(0)\n\ts_barrier" ::"n"(N) : "memory")
#define LGKM_BAR() asm volatile("s_waitcnt lgkmcnt(0)\n\ts_barrier" ::: "memory")
#define LGKM0() asm volatile("s_waitcnt lgkmcnt(0)" ::: "memory")
#define MEMFENCE() asm volatile("" ::: "memory")
#define SCHED0() __builtin_amdgcn_sched_barrier(0)

DI bf16x8 ld128(ldsp p) { return *(const LAS bf16x8*)p; }
DI s16x4 tr1(ldsp p) { return __builtin_amdgcn_ds_read_tr16_b64_v4i16((LAS s16x4*)p); }
DI bf16x8 cat(s16x4 a, s16x4 b) { return __builtin_bit_cast(bf16x8, __builtin_shufflevector(a, b, 0, 1, 2, 3, 4, 5, 6, 7)); }
DI bf16x8 tr2(ldsp p0, ldsp p1) { return cat(tr1(p0), tr1(p1)); }
DI f32x4 mfma32(bf16x8 a, bf16x8 b, f32x4 c) { return __builtin_amdgcn_mfma_f32_16x16x32_bf16(a, b, c, 0, 0, 0); }
DI f32x4 mfma16(s16x4 a, s16x4 b, f32x4 c) { return __builtin_amdgcn_mfma_f32_16x16x16bf16_1k(a, b, c, 0, 0, 0); }
DI bf16x8 asb(u32x4 v) { return __builtin_bit_cast(bf16x8, v); }
DI unsigned pk2(float a, float b) {
  bf16x2 v;
  v[0] = (__bf16)a;
  v[1] = (__bf16)b;
  return __builtin_bit_cast(unsigned, v);
}
DI void swap16(float& a, float& b) {
  auto r = __builtin_amdgcn_permlane16_swap(f2u(a), f2u(b), false, false);
  a = u2f(r[0]); b = u2f(r[1]);
}
DI void swap32(float& a, float& b) {
  auto r = __builtin_amdgcn_permlane32_swap(f2u(a), f2u(b), false, false);
  a = u2f(r[0]); b = u2f(r[1]);
}
template <int CTRL> DI float dpp(float x) { return u2f(__builtin_amdgcn_update_dpp(0u, f2u(x), CTRL, 0xf, 0xf, false)); }
DI float fmx(float a, float b) { return __builtin_fmaxf(a, b); }
DI float fmn(float a, float b) { return __builtin_fminf(a, b); }
DI float rawmax(float a, float b) { float r; asm("v_max_f32 %0, %1, %2" : "=v"(r) : "v"(a), "v"(b)); return r; }
DI float opq(float x) { asm("" : "+v"(x)); return x; }
DI f32x2 lo2(f32x4 v) { return __builtin_shufflevector(v, v, 0, 1); }
DI f32x2 hi2(f32x4 v) { return __builtin_shufflevector(v, v, 2, 3); }
DI f32x2 sp2(float x) { return f32x2{x, x}; }
DI f32x2 fma2(f32x2 a, f32x2 b, f32x2 c) { return __builtin_elementwise_fma(a, b, c); }
DI int strow(int s, int nrow) { int ri = s < 95 ? s : s - 1; return ri < nrow ? ri : nrow - 1; }
DI int rs_of(int rg) { return (512 * rg) / 3; }
DI int rg_of(int row) { return (3 * row + 2) >> 9; }
DI bool all_ok(bool ok) { return __builtin_amdgcn_ballot_w64(ok) == ~0ull; }

DI void wait_flags(const char* p, bool need, u32x2 ep) {
  for (unsigned spins = 0; spins < SPIN_MAX; ++spins) {
    u32x2 v = ld64_sc1(p);
    if (all_ok(!need || (v[0] == ep[0] && v[1] == ep[1]))) break;
    __builtin_amdgcn_s_sleep(POLLS);
  }
}

DI f32x2 ex2(f32x2 v) { return f32x2{__builtin_amdgcn_exp2f(v[0]), __builtin_amdgcn_exp2f(v[1])}; }
DI unsigned pk2v(f32x2 v) { return pk2(v[0], v[1]); }
DI void featmap2(f32x2 a01, f32x2 a23, f32x2 b01, f32x2 b23, f32x2 e4, bool livea, bool liveb, int g, unsigned (&oa)[5], unsigned (&ob)[5]) {
  float a0 = fmx(fmx(fmx(a01[0], a01[1]), a23[0]), fmx(a23[1], e4[0]));
  float a1 = -fmn(fmn(fmn(a01[0], a01[1]), a23[0]), fmn(a23[1], e4[0]));
  float b0 = fmx(fmx(fmx(b01[0], b01[1]), b23[0]), fmx(b23[1], e4[1]));
  float b1 = -fmn(fmn(fmn(b01[0], b01[1]), b23[0]), fmn(b23[1], e4[1]));
  swap16(a0, a1); swap16(b0, b1);
  float ca = rawmax(a0, a1), cb = rawmax(b0, b1);
  swap32(ca, cb);
  float d0 = rawmax(ca, cb), d1 = d0;
  swap32(d0, d1);
  float Ma = d0, Na = d0, Mb = d1, Nb = d1;
  swap16(Ma, Na); swap16(Mb, Nb);
  const f32x2 pa01 = ex2(a01 - sp2(Ma)), pa23 = ex2(a23 - sp2(Ma)), na01 = ex2(-a01 - sp2(Na)), na23 = ex2(-a23 - sp2(Na));
  const f32x2 pb01 = ex2(b01 - sp2(Mb)), pb23 = ex2(b23 - sp2(Mb)), nb01 = ex2(-b01 - sp2(Nb)), nb23 = ex2(-b23 - sp2(Nb));
  const float pa4 = __builtin_amdgcn_exp2f(e4[0] - Ma), na4 = __builtin_amdgcn_exp2f(-e4[0] - Na);
  const float pb4 = __builtin_amdgcn_exp2f(e4[1] - Mb), nb4 = __builtin_amdgcn_exp2f(-e4[1] - Nb);
  f32x2 t;
  t = pa01 + pa23; a0 = (t[0] + t[1]) + pa4;
  t = na01 + na23; a1 = (t[0] + t[1]) + na4;
  t = pb01 + pb23; b0 = (t[0] + t[1]) + pb4;
  t = nb01 + nb23; b1 = (t[0] + t[1]) + nb4;
  swap16(a0, a1); swap16(b0, b1);
  ca = a0 + a1; cb = b0 + b1;
  swap32(ca, cb);
  d0 = __builtin_amdgcn_rcpf(ca + cb);
  if (!(g < 2 ? livea : liveb)) d0 = 0.f;
  d1 = d0;
  swap32(d0, d1);
  Ma = d0; Na = d0; Mb = d1; Nb = d1;
  swap16(Ma, Na); swap16(Mb, Nb);
  oa[0] = pk2v(pa01 * sp2(Ma)); oa[1] = pk2v(pa23 * sp2(Ma));
  oa[2] = pk2v(na01 * sp2(Na)); oa[3] = pk2v(na23 * sp2(Na));
  oa[4] = pk2(pa4 * Ma, na4 * Na);
  ob[0] = pk2v(pb01 * sp2(Mb)); ob[1] = pk2v(pb23 * sp2(Mb));
  ob[2] = pk2v(nb01 * sp2(Nb)); ob[3] = pk2v(nb23 * sp2(Nb));
  ob[4] = pk2(pb4 * Mb, nb4 * Nb);
}

struct P2Ctx { unsigned vB, vR; int c0; };
DI unsigned p2_vB(int w, int lane) {
  const int l3 = lane >> 3, l7 = lane & 7;
  int fb = ((l3 >> 1) & 1) | ((w & 1) << 1);
  int ch = (((l7 >> 1) ^ fb) << 1) | (l7 & 1);
  return l3 * 1280 + ch * 16;
}
DI int p2_rot(int s, int rot) { int r = s + rot; return r >= 12 ? r - 12 : r; }
DI void p2_issueB_down(u32x4 srd_wd, unsigned vB, int c0, int w, unsigned smb, int s, int rot) {
  const int sr = p2_rot(s, rot);
  dma_b(srd_wd, vB, (128 * sr + 8 * w) * 1280 + c0 * 2, smb + P2_B + (s % 3) * 16384 + w * 1024);
  dma_b(srd_wd, vB, (128 * sr + 8 * w + 64) * 1280 + c0 * 2, smb + P2_B + (s % 3) * 16384 + w * 1024 + 8192);
}

DI void phase2(ldsp sm, const char* x, const char* wo, const u32x4 srd_wd, char* ws, char* out, int rt, int ct, int rot, int w, int lane, u32x2 ep) {
  const int r0 = rt * 128, c0 = ct * 64;
  const int g = lane >> 4, fr = lane & 15, l3 = lane >> 3, l7 = lane & 7;
  const int kh = w & 1, nh = (w >> 1) & 1, mh = w >> 2;
  const unsigned smb = (unsigned)(size_t)sm;
  const u32x4 srd_h = make_srd(ws + WS_H, S_ * DF_ * 2);
  const u32x4 srd_wo = make_srd(wo, D_ * D_ * 2);
  const u32x4 srd_om = make_srd(ws + WS_OM, NH * S_ * 32);
  const u32x4 srd_or = make_srd(ws + WS_OR, NH * S_ * 8);
  const unsigned vB = p2_vB(w, lane);
  const unsigned vA = l3 * 3072 + ((l7 ^ ((4 * (w & 1) + (l3 >> 1)) & 7)) * 16);
  const unsigned vR = vB - l3 * 1280 + (20 * (lane >> 5) + 16 + (l3 & 3)) * 1280;
  const unsigned vL = lane * 16;
  const int rgl = rg_of(r0), rgh = rg_of(r0 + 127);

#define P2_PIECE(s, p) do { \
    constexpr int i_ = (p) < 2 ? (p) : (p) - 2; \
    if ((s) < 12) { \
      if ((p) < 2) dma_b(srd_wd, vB, (128 * p2_rot((s), rot) + 8 * w + 64 * i_) * 1280 + c0 * 2, smb + P2_B + ((s) % 3) * 16384 + i_ * 8192 + w * 1024); \
      else dma_b_sc1(srd_h, vA, (r0 + 8 * w + 64 * (i_ & 1)) * 3072 + p2_rot((s), rot) * 256 + (i_ >> 1) * 128, smb + P2_A + ((s) % 3) * 32768 + (i_ >> 1) * 16384 + (i_ & 1) * 8192 + w * 1024); \
    } else if ((s) < 16) { \
      if ((p) < 2) dma_b(srd_wo, vB, (160 * ((s) - 12) + 20 * ((w + 8 * i_) >> 1) + 8 * (w & 1)) * 1280 + c0 * 2, smb + P2_B + ((s) % 3) * 16384 + i_ * 8192 + w * 1024); \
      else dma_b_sc1(srd_om, vL, ((8 * ((s) - 12) + 2 * i_ + (w >> 2)) * S_ + r0) * 32 + (w & 3) * 1024, smb + P2_A + ((s) % 3) * 32768 + i_ * 8192 + w * 1024); \
    } else { \
      if ((p) < 2) dma_b(srd_wo, vR, (40 * (w + 8 * i_)) * 1280 + c0 * 2, smb + P2_B + ((s) % 3) * 16384 + i_ * 8192 + w * 1024); \
      else dma_b_sc1(srd_or, vL, ((8 * i_ + w) * S_ + r0) * 8, smb + P2_A + ((s) % 3) * 32768 + i_ * 8192 + w * 1024); \
    } } while (0)

  const int aD = P2_A + kh * 16384 + (64 * mh + fr) * 128 + ((g ^ ((fr >> 1) & 7)) * 16);
  const int aM = P2_A + (4 * kh + (g >> 1)) * 4096 + (64 * mh + fr) * 32 + (g & 1) * 16;
  const int aR = P2_A + (16 * kh + 2 * g) * 1024 + (64 * mh + fr) * 8;
  int bO[2];
  {
    const int q = fr >> 2, p = lane & 3;
    const int f = ((q >> 1) & 1) | ((g & 1) << 1);
#pragma unroll
    for (int j = 0; j < 2; ++j) bO[j] = P2_B + (64 * kh + 8 * g + q) * 128 + (((2 * nh + j) ^ f) * 32) + p * 8;
  }
  f32x4 acc[4][2];
#pragma unroll
  for (int m = 0; m < 4; ++m)
#pragma unroll
    for (int j = 0; j < 2; ++j) acc[m][j] = f32x4{0.f, 0.f, 0.f, 0.f};

  STAMPAT(8);
  if (w == 0) wait_flags(ws + WS_HF + (size_t)(lane < 16 ? rgl : rgh) * 128 + (lane & 15) * 8, lane < 32 && (lane & 15) < 12, ep);
  WAITN_BAR(0);
  STAMPAT(9);
#define P2_A3(s) do { P2_PIECE(s, 2); P2_PIECE(s, 3); P2_PIECE(s, 4); P2_PIECE(s, 5); } while (0)
  P2_A3(0); P2_A3(1); P2_A3(2);
  u32x4 xr[2];
  bf16x8 fa[2][8];
  s16x4 fb[2][8];
#define P2_RD(s, i) do { \
    if ((i) < 8) fb[(s) & 1][i] = tr1(sm + ((s) % 3) * 16384 + bO[((i) >> 1) & 1] + ((i) >> 2) * 4096 + ((i) & 1) * 512); \
    else if ((i) < 16) { \
      constexpr int sl_ = ((i) - 8) >> 2, m_ = ((i) - 8) & 3; \
      if ((s) < 12) fa[(s) & 1][((i) - 8) & 7] = ld128(sm + ((s) % 3) * 32768 + (sl_ ? (aD ^ 64) : aD) + m_ * 2048); \
      else if ((s) < 16) fa[(s) & 1][((i) - 8) & 7] = ld128(sm + ((s) % 3) * 32768 + aM + sl_ * 8192 + m_ * 512); \
      else fa[(s) & 1][((i) - 8) & 7] = cat(*(const LAS s16x4*)(sm + ((s) % 3) * 32768 + aR + sl_ * 8192 + m_ * 128), *(const LAS s16x4*)(sm + ((s) % 3) * 32768 + aR + sl_ * 8192 + 1024 + m_ * 128)); } } while (0)
#define P2_MM(s, i) acc[((i) >> 1) & 3][(i) & 1] = mfma32(cat(fb[(s) & 1][((i) >> 3) * 4 + 2 * ((i) & 1)], fb[(s) & 1][((i) >> 3) * 4 + 2 * ((i) & 1) + 1]), fa[(s) & 1][((i) >> 3) * 4 + (((i) >> 1) & 3)], acc[((i) >> 1) & 3][(i) & 1])
#define P2_GROUP(s, q) do { \
    if ((s) >= 1 && (s) + 2 < 17 && (s) != 12 && !(((s) == 10 || (s) == 11) && (q) >= 2)) P2_PIECE((s) + 2, q); \
    SCHED0(); \
    if ((s) < 17) { P2_RD(s, 3 * (q)); P2_RD(s, 3 * (q) + 1); P2_RD(s, 3 * (q) + 2); } \
    SCHED0(); \
    if ((s) >= 1 && (s) != 12) { P2_MM((s) - 1, ((q) * 8) / 3); P2_MM((s) - 1, ((q) * 8) / 3 + 1); if (((q) * 8) / 3 + 2 < (((q) + 1) * 8) / 3) P2_MM((s) - 1, ((q) * 8) / 3 + 2); } \
    SCHED0(); } while (0)
#define P2_STAGE(s) do { \
    SCHED0(); \
    if ((s) < 17) WAITN_BAR(((s) == 0 || (s) == 12) ? 8 : (((s) == 1 || (s) == 13) ? 4 : ((s) == 11 ? 2 : ((s) < 16 ? 6 : 0)))); \
    SCHED0(); \
    P2_GROUP(s, 0); P2_GROUP(s, 1); P2_GROUP(s, 2); P2_GROUP(s, 3); P2_GROUP(s, 4); P2_GROUP(s, 5); } while (0)
  P2_STAGE(0); P2_STAGE(1); P2_STAGE(2); P2_STAGE(3); P2_STAGE(4); P2_STAGE(5); P2_STAGE(6); P2_STAGE(7); P2_STAGE(8);
  P2_STAGE(9); P2_STAGE(10); P2_STAGE(11);
  SCHED0();
#pragma unroll
  for (int i = 0; i < 16; ++i) P2_MM(11, i);
  SCHED0();
  STAMPAT(10);
  if (w == 0) wait_flags(ws + WS_OF + (size_t)(lane < 16 ? rgl : rgh) * 128 + (lane & 15) * 8, lane < 32 && (lane & 15) < 8, ep);
  LGKM_BAR();
  STAMPAT(11);
  P2_PIECE(14, 0); P2_PIECE(14, 1);
  P2_A3(12); P2_A3(13); P2_A3(14);
  P2_STAGE(12); P2_STAGE(13); P2_STAGE(14); P2_STAGE(15);
  const int orow = 64 * mh + 32 * kh + (lane >> 2), ocol = c0 * 2 + nh * 64 + (lane & 3) * 16;
#pragma unroll
  for (int i = 0; i < 2; ++i)
    asm volatile("global_load_dwordx4 %0, %1, off" : "=&v"(xr[i]) : "v"(x + (size_t)(r0 + orow + 16 * i) * 1280 + ocol) : "memory");
  P2_STAGE(16); P2_STAGE(17);
  SCHED0();
  STAMPAT(12);
  f32x4 keep[2][2];
  {
    ldsp ex = sm + w * 4096 + lane * 16;
#pragma unroll
    for (int i = 0; i < 2; ++i)
#pragma unroll
      for (int j = 0; j < 2; ++j) {
        f32x4 snd;
#pragma unroll
        for (int e = 0; e < 4; ++e) {
          const float lo_ = acc[i][j][e], hi_ = acc[2 + i][j][e];
          snd[e] = kh ? lo_ : hi_;
          keep[i][j][e] = kh ? hi_ : lo_;
        }
        *(LAS f32x4*)(ex + (i * 2 + j) * 1024) = snd;
      }
  }
  LGKM_BAR();
  {
    ldsp ex = sm + (w ^ 1) * 4096 + lane * 16;
    ldsp tp = sm + 65536 + w * 4608;
#pragma unroll
    for (int i = 0; i < 2; ++i)
#pragma unroll
      for (int j = 0; j < 2; ++j) {
        const f32x4 e = *(const LAS f32x4*)(ex + (i * 2 + j) * 1024);
        *(LAS f32x4*)(tp + (i * 16 + fr) * 144 + j * 64 + g * 16) = keep[i][j] + e;
      }
    asm volatile("" : "+v"(xr[0]), "+v"(xr[1]));
#pragma unroll
    for (int i = 0; i < 2; ++i) {
      const f32x4 s0 = *(const LAS f32x4*)(tp + (i * 16 + (lane >> 2)) * 144 + (lane & 3) * 32);
      const f32x4 s1 = *(const LAS f32x4*)(tp + (i * 16 + (lane >> 2)) * 144 + (lane & 3) * 32 + 16);
      u32x4 o;
#pragma unroll
      for (int e = 0; e < 2; ++e) {
        o[e] = pk2(s0[2 * e] + u2f(xr[i][e] << 16), s0[2 * e + 1] + u2f(xr[i][e] & 0xffff0000u));
        o[2 + e] = pk2(s1[2 * e] + u2f(xr[i][2 + e] << 16), s1[2 * e + 1] + u2f(xr[i][2 + e] & 0xffff0000u));
      }
      st128_sc1(out + (size_t)(r0 + orow + 16 * i) * 1280 + ocol, o);
    }
  }
}

template <int MT, bool GU>
DI u32x2 phase1(ldsp sm, const char* x, const char* wqkv, const char* wphi, const char* wg, const char* wu, const u32x4 srd_wd,
                char* ws, int rg, int ct, int c0p2, int rot, int w, int lane) {
  STAMPAT(0);
  constexpr int mh = (MT == 6) ? 0 : 1;
  const int wn = w & 3;
  const int r0 = rs_of(rg), nrow = rs_of(rg + 1) - r0;
  const unsigned smb = (unsigned)(size_t)sm;
  const int g = lane >> 4, fr = lane & 15;

  const u32x4 srdx = make_srd(x, S_ * D_ * 2);
  unsigned voffA[3];
#pragma unroll
  for (int c = 0; c < 3; ++c) {
    int slot = 8 * (w + 8 * c) + (lane >> 3);
    int ch = (lane & 7) ^ ((slot >> 1) & 7);
    int ri = slot < 95 ? slot : slot - 1;
    ri = ri < nrow ? ri : nrow - 1;
    voffA[c] = (unsigned)((r0 + ri) * 1280 + ch * 16);
  }
  if (w >= 6) voffA[2] = 0x40000000u;
  const unsigned long long FULL = ~0ull, PART = 0x00FFFFFFFFFFFFFFull;
  const unsigned long long mask1 = (w == 3) ? PART : FULL, mask2 = (w == 5) ? PART : FULL;
  const unsigned adst0 = smb + L_A + w * 1024;
  u32x4 srd0, srd1;
  unsigned voff0, voff1;
  constexpr unsigned SSTEP = GU ? 32 * 3072 : 32 * 3840;
  if (GU) {
    srd0 = make_srd(wg, D_ * DF_ * 2);
    srd1 = make_srd(wu, D_ * DF_ * 2);
    int krow = 4 * w + (lane >> 4);
    int chp = lane & 15;
    int f = (krow & 3) | (((krow >> 3) & 1) << 2);
    int ch = (((chp >> 1) ^ f) << 1) | (chp & 1);
    voff0 = voff1 = krow * 3072 + ct * 256 + ch * 16;
  } else {
    srd0 = srd1 = make_srd(wqkv, D_ * 3 * D_ * 2);
    unsigned vo[2];
#pragma unroll
    for (int c = 0; c < 2; ++c) {
      int cidx = (w + 8 * c) * 64 + lane;
      int rho = cidx / 30, cc = cidx % 30;
      int seg = cc / 10, ch = cc % 10;
      int k = (rho & 16) | ((rho & 4) << 1) | ((rho & 8) >> 1) | (rho & 3);
      vo[c] = (cidx < 960) ? (unsigned)(k * 3840 + seg * 1280 + ct * 160 + ch * 16) : 0x40000000u;
    }
    voff0 = vo[0];
    voff1 = vo[1];
  }
  const unsigned bdst0 = smb + L_B + w * 1024;
#define KROT (((rg%6)*5/3+ct*2)%10)
  const unsigned krot = KROT;
#define RA(so) ((so) + krot * 128u >= 1280u ? (so) + krot * 128u - 1280u : (so) + krot * 128u)
#define RB(so) ((so) + krot * 2u * SSTEP >= 20u * SSTEP ? (so) + krot * 2u * SSTEP - 20u * SSTEP : (so) + krot * 2u * SSTEP)
#define DMA_B0(soff, dst) dma_b(srd0, voff0, RB(soff), (dst))
#define DMA_B1(soff, dst) dma_b(srd1, voff1, RB(soff), (dst))
#define DMA_A0(soff, dst) dma_b(srdx, voffA[0], RA(soff), (dst))
#define DMA_A1(soff, dst) dma_b_mask(srdx, voffA[1], RA(soff), (dst), mask1)
#define DMA_A2(soff, dst) dma_b_mask(srdx, voffA[2], RA(soff), (dst), mask2)
  const unsigned a2base = (w >= 6) ? smb + L_DUMMY : adst0 + 16384;
  const unsigned a2mul = (w >= 6) ? 0u : 1u;
  unsigned wraw[16];
  if (!GU) {
    const char* wp = wphi + (ct * 4 + wn) * 800;
    const char* p0 = wp + (5 * (fr >> 2) + (fr & 3)) * 2;
    const char* p1 = wp + (5 * (fr >> 2) + 4) * 2;
    const char* q0 = p0 + g * 160;
    const char* q1 = p1 + g * 160;
#pragma unroll
    for (int j = 0; j < 4; ++j) {
      asm volatile("global_load_ushort %0, %1, off offset:%2" : "=&v"(wraw[j]) : "v"(q0), "n"(j * 40) : "memory");
      asm volatile("global_load_ushort %0, %1, off offset:%2" : "=&v"(wraw[4 + j]) : "v"(p0), "n"(640 + j * 40) : "memory");
      asm volatile("global_load_ushort %0, %1, off offset:%2" : "=&v"(wraw[8 + j]) : "v"(q1), "n"(j * 40) : "memory");
      asm volatile("global_load_ushort %0, %1, off offset:%2" : "=&v"(wraw[12 + j]) : "v"(p1), "n"(640 + j * 40) : "memory");
    }
  }
  DMA_B0(0, bdst0); DMA_B1(0, bdst0 + 8192);
  DMA_A0(0, adst0); DMA_A1(0, adst0 + 8192);
  DMA_A2(0, a2base);
  DMA_B0(SSTEP, bdst0 + B_SLOT); DMA_B1(SSTEP, bdst0 + B_SLOT + 8192);
  DMA_B0(2 * SSTEP, bdst0 + 2 * B_SLOT); DMA_B1(2 * SSTEP, bdst0 + 2 * B_SLOT + 8192);
  DMA_A0(128, adst0 + A_SLOT); DMA_A1(128, adst0 + A_SLOT + 8192);
  DMA_A2(128, a2base + a2mul * A_SLOT);
  DMA_B0(3 * SSTEP, bdst0 + 3 * B_SLOT); DMA_B1(3 * SSTEP, bdst0 + 3 * B_SLOT + 8192);
  DMA_B0(4 * SSTEP, bdst0 + 4 * B_SLOT); DMA_B1(4 * SSTEP, bdst0 + 4 * B_SLOT + 8192);
  DMA_A0(256, adst0 + 2 * A_SLOT); DMA_A1(256, adst0 + 2 * A_SLOT + 8192);
  if (w == 0 && lane < 48) {
    int rr = lane >> 3;
    *(LAS u32x4*)(sm + L_A + (rr >> 1) * A_SLOT + ((rr & 1) ? 175 : 95) * 128 + (lane & 7) * 16) =
        u32x4{0x3F803F80u, 0x3F803F80u, 0x3F803F80u, 0x3F803F80u};
  }
  const int bid = __builtin_amdgcn_workgroup_id_x();
  if (w == 1) {
    unsigned long long nn = __builtin_amdgcn_s_memrealtime();
    u32x2 nv = u32x2{(unsigned)nn, (unsigned)(nn >> 32)};
    if (lane == 0) {
      st64_sc1(ws + WS_REQ + bid * 64, nv);
      *(LAS u32x2*)(sm + L_NONCE) = nv;
    }
  }

  const int aoff0 = L_A + mh * 6 * 2048 + fr * 128 + ((g ^ ((fr >> 1) & 7)) * 16);
  const int aoff1 = aoff0 ^ 64;
  int boff[4];
  constexpr int TSTEP = GU ? 1024 : 3840;
  {
    const int q = fr >> 2, p = lane & 3;
    if (GU) {
      int f = q | ((g & 1) << 2);
#pragma unroll
      for (int j = 0; j < 4; ++j)
        boff[j] = L_B + (j >> 1) * 8192 + (8 * g + q) * 256 + (((2 * wn + (j & 1)) ^ f) * 32) + p * 8;
    } else {
      int rho = 16 * (g >> 1) + 4 * (g & 1) + q;
#pragma unroll
      for (int j = 0; j < 3; ++j) boff[j] = L_B + rho * 480 + (20 * j + 5 * wn + p) * 8;
      boff[3] = L_B + rho * 480 + (20 * (p == 3 ? 0 : p) + 5 * wn + 4) * 8;
    }
  }

  f32x4 acc[MT][4];
#pragma unroll
  for (int m = 0; m < MT; ++m)
#pragma unroll
    for (int j = 0; j < 4; ++j) acc[m][j] = f32x4{0.f, 0.f, 0.f, 0.f};
  bf16x8 fa0[MT], fa1[MT];
  s16x4 fb0[8], fb1[8];

  STAMPAT(1);
  WAITN_BAR(13);
  STAMPAT(2);
  unsigned wpk[8];
  if (!GU) {
    asm volatile("" : "+v"(wraw[0]), "+v"(wraw[1]), "+v"(wraw[2]), "+v"(wraw[3]), "+v"(wraw[4]), "+v"(wraw[5]), "+v"(wraw[6]), "+v"(wraw[7]),
                 "+v"(wraw[8]), "+v"(wraw[9]), "+v"(wraw[10]), "+v"(wraw[11]), "+v"(wraw[12]), "+v"(wraw[13]), "+v"(wraw[14]), "+v"(wraw[15]));
#pragma unroll
    for (int i = 0; i < 8; ++i) {
      wpk[i] = wraw[2 * i] | (wraw[2 * i + 1] << 16);
      if (i >= 4 && (fr & 3) != 0) wpk[i] = 0u;
    }
    asm volatile("" : "+v"(wpk[0]), "+v"(wpk[1]), "+v"(wpk[2]), "+v"(wpk[3]), "+v"(wpk[4]), "+v"(wpk[5]), "+v"(wpk[6]), "+v"(wpk[7]));
  }
#pragma unroll
  for (int m = 0; m < MT; ++m) fa0[m] = ld128(sm + aoff0 + m * 2048);
#pragma unroll
  for (int j = 0; j < 8; ++j) fb0[j] = tr1(sm + boff[j >> 1] + (j & 1) * TSTEP);
  MEMFENCE();

  constexpr int NM = MT * 4;
  constexpr int DS = (MT == 6) ? 6 : 5;
#define MMA(i, FA, FB) acc[(i) >> 2][(i) & 3] = mfma32(cat(FB[2 * ((i) & 3)], FB[2 * ((i) & 3) + 1]), FA[(i) >> 2], acc[(i) >> 2][(i) & 3])
#define BADDR(sl, i) (sm + (sl) * B_SLOT + boff[(i) >> 1] + ((i) & 1) * TSTEP)
  u32x4 ackv, stv[MT];
  const char* ackp = ws + WS_ACK + bid * 64;
#define STP(m) (ws + WS_STAT + (size_t)(r0 + strow(mh * 96 + (m) * 16 + fr, nrow)) * 16)
#pragma unroll
  for (int t = 0; t < 10; ++t) {
    SCHED0();
    WAITN_BAR(t < 8 ? 11 : (t == 8 ? 7 : 1 + MT));
    SCHED0();
#pragma unroll
    for (int i = 0; i < NM; ++i) {
      MMA(i, fa0, fb0);
      SCHED0();
      if (i < 8) fb1[i] = tr1(BADDR((2 * t + 1) % 5, i));
      else if (i < 8 + MT) fa1[i - 8] = ld128(sm + (t % 3) * A_SLOT + aoff1 + (i - 8) * 2048);
      if (t < 8) {
        if (i == 2) { if (MT == 6) DMA_A2((t + 2) * 128, adst0 + 16384 + ((t + 2) % 3) * A_SLOT); else DMA_A2((t + 2) * 128, a2base + a2mul * (((t + 2) % 3) * A_SLOT)); }
        if (i == 2 + DS) DMA_B0((2 * t + 5) * SSTEP, bdst0 + ((2 * t) % 5) * B_SLOT);
        if (i == 2 + 2 * DS) DMA_B1((2 * t + 5) * SSTEP, bdst0 + ((2 * t) % 5) * B_SLOT + 8192);
      }
      SCHED0();
    }
    if (t < 9) WAITN_BAR(t < 8 ? 9 : 2); else LGKM_BAR();
    SCHED0();
#pragma unroll
    for (int i = 0; i < NM; ++i) {
      MMA(i, fa1, fb1);
      SCHED0();
      if (t < 9) {
        if (i < 8) fb0[i] = tr1(BADDR((2 * t + 2) % 5, i));
        else if (i < 8 + MT) fa0[i - 8] = ld128(sm + ((t + 1) % 3) * A_SLOT + aoff0 + (i - 8) * 2048);
      }
      if (t < 7) {
        if (i == 1) DMA_B0((2 * t + 6) * SSTEP, bdst0 + ((2 * t + 1) % 5) * B_SLOT);
        if (i == 1 + DS) DMA_B1((2 * t + 6) * SSTEP, bdst0 + ((2 * t + 1) % 5) * B_SLOT + 8192);
        if (i == 1 + 2 * DS) DMA_A0((t + 3) * 128, adst0 + (t % 3) * A_SLOT);
        if (i == 1 + 3 * DS) DMA_A1((t + 3) * 128, adst0 + (t % 3) * A_SLOT + 8192);
      }
      if (i == 4 && t == 8) {
        asm volatile("global_load_dwordx4 %0, %1, off sc1" : "=&v"(ackv) : "v"(ackp) : "memory");
#pragma unroll
        for (int m = 0; m < MT; ++m) asm volatile("global_load_dwordx4 %0, %1, off sc1" : "=&v"(stv[m]) : "v"(STP(m)) : "memory");
      }
      SCHED0();
    }
  }
  SCHED0();
  STAMPAT(3);

  if (fr == 15) {
#pragma unroll
    for (int j = 0; j < 4; ++j) *(LAS f32x4*)(sm + L_CS + w * 256 + j * 64 + g * 16) = acc[MT - 1][j];
  }
  if (MT == 6) asm volatile("s_waitcnt vmcnt(0) lgkmcnt(0)" : "+v"(ackv), "+v"(stv[0]), "+v"(stv[1]), "+v"(stv[2]), "+v"(stv[3]), "+v"(stv[4]), "+v"(stv[MT - 1]) :: "memory");
  else asm volatile("s_waitcnt vmcnt(0) lgkmcnt(0)" : "+v"(ackv), "+v"(stv[0]), "+v"(stv[1]), "+v"(stv[2]), "+v"(stv[3]), "+v"(stv[4]) :: "memory");
  f32x4 cs[4];
#pragma unroll
  for (int j = 0; j < 4; ++j) cs[j] = *(const LAS f32x4*)(sm + L_CS + w * 256 + j * 64 + g * 16);
  {
    const u32x2 nv = *(const LAS u32x2*)(sm + L_NONCE);
    for (unsigned spins = 0; spins < SPIN_MAX; ++spins) {
      bool ok = (ackv[0] == nv[0]) && (ackv[1] == nv[1]);
#pragma unroll
      for (int m = 0; m < MT; ++m) ok = ok && (stv[m][2] == ackv[2]) && (stv[m][3] == ackv[3]);
      if (all_ok(ok)) break;
      __builtin_amdgcn_s_sleep(2);
#pragma unroll
      for (int m = 0; m < MT; ++m) asm volatile("global_load_dwordx4 %0, %1, off sc1" : "=&v"(stv[m]) : "v"(STP(m)) : "memory");
      ackv = ld128_sc1(ackp);
#pragma unroll
      for (int m = 0; m < MT; ++m) asm volatile("" : "+v"(stv[m]) :: "memory");
    }
  }
  const u32x2 ep = u32x2{ackv[2], ackv[3]};
  if (w == 0 && lane == 0) st64_sc1(ws + WS_DONE + bid * 64, ep);
  if (GU) {
    const unsigned vB = p2_vB(w, lane);
    p2_issueB_down(srd_wd, vB, c0p2, w, smb, 0, rot);
    p2_issueB_down(srd_wd, vB, c0p2, w, smb, 1, rot);
    p2_issueB_down(srd_wd, vB, c0p2, w, smb, 2, rot);
  }
  float rstd[MT];
#pragma unroll
  for (int m = 0; m < MT; ++m) {
    const f32x2 nm = sp2(opq(-u2f(stv[m][0])));
    rstd[m] = opq(u2f(stv[m][1]));
#pragma unroll
    for (int j = 0; j < 4; ++j) {
      f32x2 l = fma2(nm, lo2(cs[j]), lo2(acc[m][j])), h = fma2(nm, hi2(cs[j]), hi2(acc[m][j]));
      acc[m][j] = f32x4{l[0], l[1], h[0], h[1]};
    }
  }
  STAMPAT(4);

  if (GU) {
    const float K0 = -2.0f * 0.7978845608028654f * 1.4426950408889634f;
    const float K1c = K0 * 0.044715f;
    ldsp stg = sm + w * 7680;
    char* hg = ws + WS_H + ct * 256 + wn * 64 + (lane & 3) * 16;
#pragma unroll
    for (int m = 0; m <= MT; ++m) {
      if (m < MT) {
        const float r = rstd[m], r2 = r * r;
        const f32x2 c0 = sp2(opq(K0 * r)), c1 = sp2(opq(K1c * r2 * r)), ir2 = sp2(opq(__builtin_amdgcn_rcpf(r2)));
        u32x2 hp[2];
#pragma unroll
        for (int j = 0; j < 2; ++j) {
#pragma unroll
          for (int q = 0; q < 2; ++q) {
            const f32x2 a = q ? hi2(acc[m][j]) : lo2(acc[m][j]), b = q ? hi2(acc[m][j + 2]) : lo2(acc[m][j + 2]);
            const f32x2 z = a * fma2(a * a, c1, c0);
            const f32x2 e = f32x2{__builtin_amdgcn_exp2f(z[0]), __builtin_amdgcn_exp2f(z[1])};
            const f32x2 d = fma2(e, ir2, ir2);
            const f32x2 hv = (a * b) * f32x2{__builtin_amdgcn_rcpf(d[0]), __builtin_amdgcn_rcpf(d[1])};
            hp[j][q] = pk2(hv[0], hv[1]);
          }
        }
        *(LAS u32x2*)(stg + (m * 16 + fr) * 80 + g * 8) = hp[0];
        *(LAS u32x2*)(stg + (m * 16 + fr) * 80 + 32 + g * 8) = hp[1];
      }
      if (m >= 1) {
        const int rl = (m - 1) * 16 + (lane >> 2);
        u32x4 v = *(const LAS u32x4*)(stg + rl * 80 + (lane & 3) * 16);
        const int sl = mh * 96 + rl;
        const int ri = sl < 95 ? sl : sl - 1;
        if (sl != 95 && ri < nrow) st128_sc1(hg + (size_t)(r0 + ri) * 3072, v);
      }
    }
    STAMPAT(5);
    WAITN_BAR(0);
    if (w == 0 && lane == 0) st64_sc1(ws + WS_HF + rg * 128 + ct * 8, ep);
    STAMPAT(6);
  } else {
    const int head = ct * 4 + wn;
    bf16x8 wq[2], wk[2];
#pragma unroll
    for (int ft = 0; ft < 2; ++ft) {
      wq[ft] = asb(u32x4{wpk[4 * ft], wpk[4 * ft + 1], g == 0 ? wpk[4 * ft + 2] : 0u, g == 0 ? wpk[4 * ft + 3] : 0u});
      wk[ft] = asb(u32x4{wpk[4 * ft], wpk[4 * ft + 1], g == 1 ? wpk[4 * ft + 2] : 0u, g == 1 ? wpk[4 * ft + 3] : 0u});
    }
    const unsigned tag = (ep[0] ^ (ep[1] * 0x9E3779B1u)) & 0xBFFFBFFFu;
    const float L2E = 1.4426950408889634f;
    const f32x4 z4 = f32x4{0.f, 0.f, 0.f, 0.f};
    bool live[6];
    u32x2 rem[6];
#pragma unroll
    for (int m = 0; m < 6; ++m) {
      int s = mh * 96 + m * 16 + fr;
      int ri = s < 95 ? s : s - 1;
      live[m] = (m < MT) && (s != 95) && (ri < nrow);
      if (m < MT) rem[m] = u32x2{pk2(acc[m][3][0], acc[m][3][1]), pk2(acc[m][3][2], acc[m][3][3])};
    }
    ldsp pk_img = sm + w * 17408;
    ldsp v_img = pk_img + 96 * 104;
    {
      unsigned kf[6][5];
      u32x2 vv[6], ve[6];
#pragma unroll
      for (int mp = 0; mp < 3; ++mp) {
        f32x2 y01[2], y23[2], y4;
#pragma unroll
        for (int hh = 0; hh < 2; ++hh) {
          const int m = 2 * mp + hh;
          if (m < MT) {
            const f32x2 r = sp2(rstd[m]);
            const float c = opq(rstd[m] * L2E);
            u32x4 kb = u32x4{pk2(acc[m][1][0], acc[m][1][1]), pk2(acc[m][1][2], acc[m][1][3]), rem[m][0], rem[m][1]};
            f32x4 k0 = mfma32(wk[0], asb(kb), z4);
            f32x4 k1 = mfma32(wk[1], asb(kb), z4);
            y01[hh] = lo2(k0) * sp2(c); y23[hh] = hi2(k0) * sp2(c); y4[hh] = k1[0] * c;
            vv[m] = u32x2{pk2v(lo2(acc[m][2]) * r), pk2v(hi2(acc[m][2]) * r)};
            u32x2 vr = u32x2{pk2v(lo2(acc[m][3]) * r), pk2v(hi2(acc[m][3]) * r)};
            ve[m] = (g == 2) ? vr : u32x2{0x3F803F80u, 0x3F803F80u};
          } else {
            y01[hh] = sp2(0.f); y23[hh] = sp2(0.f); y4[hh] = 0.f;
            vv[m] = u32x2{0u, 0u}; ve[m] = vv[m];
          }
        }
        featmap2(y01[0], y23[0], y01[1], y23[1], y4, live[2 * mp], live[2 * mp + 1], g, kf[2 * mp], kf[2 * mp + 1]);
      }
      SCHED0();
#pragma unroll
      for (int m = 0; m < 6; ++m) {
        int tok = m * 16 + fr;
        *(LAS u32x2*)(pk_img + tok * 104 + g * 8) = u32x2{kf[m][0], kf[m][1]};
        *(LAS u32x2*)(pk_img + tok * 104 + 32 + g * 8) = u32x2{kf[m][2], kf[m][3]};
        *(LAS u32x2*)(pk_img + tok * 104 + 64 + g * 8) = u32x2{kf[m][4], 0u};
        *(LAS u32x2*)(v_img + tok * 72 + g * 8) = vv[m];
        *(LAS u32x2*)(v_img + tok * 72 + 32 + (g ^ 2) * 8) = ve[m];
      }
      LGKM0();
      SCHED0();
    }
    f32x4 kv[3][2];
#pragma unroll
    for (int ft = 0; ft < 3; ++ft)
#pragma unroll
      for (int dt = 0; dt < 2; ++dt) kv[ft][dt] = z4;
    {
      const int q = fr >> 2, p = lane & 3;
#pragma unroll
      for (int s = 0; s < 3; ++s) {
        int row = 32 * s + 8 * g + q;
        bf16x8 a[3], b[2];
#pragma unroll
        for (int ft = 0; ft < 3; ++ft) a[ft] = tr2(pk_img + row * 104 + ft * 32 + p * 8, pk_img + (row + 4) * 104 + ft * 32 + p * 8);
#pragma unroll
        for (int dt = 0; dt < 2; ++dt) b[dt] = tr2(v_img + row * 72 + dt * 32 + p * 8, v_img + (row + 4) * 72 + dt * 32 + p * 8);
#pragma unroll
        for (int ft = 0; ft < 3; ++ft)
#pragma unroll
          for (int dt = 0; dt < 2; ++dt) kv[ft][dt] = mfma32(a[ft], b[dt], kv[ft][dt]);
      }
    }
    {
      f32x4 o5[5] = {kv[0][0], kv[1][0], kv[0][1], kv[1][1], f32x4{kv[2][0][0], kv[2][0][1], kv[2][1][0], kv[2][1][1]}};
      ldsp ex = sm + (4 + wn) * 17408;
      if (mh == 1) {
        LGKM0();
#pragma unroll
        for (int i = 0; i < 5; ++i) *(LAS f32x4*)(ex + i * 1024 + lane * 16) = o5[i];
      }
      LGKM_BAR();
      if (mh == 0) {
        unsigned pp[10];
#pragma unroll
        for (int i = 0; i < 5; ++i) {
          f32x4 e = *(const LAS f32x4*)(ex + i * 1024 + lane * 16);
          pp[2 * i] = pk2(o5[i][0] + e[0], o5[i][1] + e[1]);
          pp[2 * i + 1] = pk2(o5[i][2] + e[2], o5[i][3] + e[3]);
        }
        char* kp = ws + WS_KV + (size_t)(rg * NH + head) * 4096 + lane * 16;
        st128_sc1(kp, u32x4{pp[0], pp[1], pp[2], tag});
        st128_sc1(kp + 1024, u32x4{pp[3], pp[4], pp[5], tag});
        st128_sc1(kp + 2048, u32x4{pp[6], pp[7], pp[8], tag});
        st128_sc1(kp + 3072, u32x4{pp[9], 0u, 0u, tag});
      }
    }
    STAMPAT(5);
    unsigned qf[6][5];
#pragma unroll
    for (int mp = 0; mp < 3; ++mp) {
      f32x2 y01[2], y23[2], y4;
#pragma unroll
      for (int hh = 0; hh < 2; ++hh) {
        const int m = 2 * mp + hh;
        if (m < MT) {
          const float c = opq(rstd[m] * L2E);
          u32x4 qb = u32x4{pk2(acc[m][0][0], acc[m][0][1]), pk2(acc[m][0][2], acc[m][0][3]), rem[m][0], rem[m][1]};
          f32x4 q0 = mfma32(wq[0], asb(qb), z4);
          f32x4 q1 = mfma32(wq[1], asb(qb), z4);
          y01[hh] = lo2(q0) * sp2(c); y23[hh] = hi2(q0) * sp2(c); y4[hh] = q1[0] * c;
        } else {
          y01[hh] = sp2(0.f); y23[hh] = sp2(0.f); y4[hh] = 0.f;
        }
      }
      featmap2(y01[0], y23[0], y01[1], y23[1], y4, true, true, g, qf[2 * mp], qf[2 * mp + 1]);
    }
    STAMPAT(6);
    float ks[20];
    {
      {
        const char* p1 = ws + WS_KV + ((size_t)(mh * 6 + (lane < 6 ? lane : 0)) * NH + head) * 4096 + 3072 + 1008;
        for (unsigned spins = 0; spins < SPIN_MAX; ++spins) {
          u32x4 v = ld128_sc1(p1);
          if (all_ok(v[3] == tag)) break;
          __builtin_amdgcn_s_sleep(POLLS);
        }
      }
      u32x4 pv[6][4];
      const char* kp = ws + WS_KV + (size_t)((mh * 6) * NH + head) * 4096 + lane * 16;
      for (unsigned spins = 0; spins < SPIN_MAX; ++spins) {
#pragma unroll
        for (int c = 0; c < 6; ++c) {
          const char* p = kp + (size_t)c * NH * 4096;
          asm volatile("global_load_dwordx4 %0, %4, off sc1\n\tglobal_load_dwordx4 %1, %4, off offset:1024 sc1\n\t"
                       "global_load_dwordx4 %2, %4, off offset:2048 sc1\n\tglobal_load_dwordx4 %3, %4, off offset:3072 sc1"
                       : "=&v"(pv[c][0]), "=&v"(pv[c][1]), "=&v"(pv[c][2]), "=&v"(pv[c][3]) : "v"(p) : "memory");
        }
        asm volatile("s_waitcnt vmcnt(0)" : "+v"(pv[0][0]), "+v"(pv[0][1]), "+v"(pv[0][2]), "+v"(pv[0][3]), "+v"(pv[1][0]), "+v"(pv[1][1]), "+v"(pv[1][2]), "+v"(pv[1][3]),
                     "+v"(pv[2][0]), "+v"(pv[2][1]), "+v"(pv[2][2]), "+v"(pv[2][3]), "+v"(pv[3][0]), "+v"(pv[3][1]), "+v"(pv[3][2]), "+v"(pv[3][3]),
                     "+v"(pv[4][0]), "+v"(pv[4][1]), "+v"(pv[4][2]), "+v"(pv[4][3]), "+v"(pv[5][0]), "+v"(pv[5][1]), "+v"(pv[5][2]), "+v"(pv[5][3]) :: "memory");
        bool ok = true;
#pragma unroll
        for (int c = 0; c < 6; ++c)
#pragma unroll
          for (int i = 0; i < 4; ++i) ok = ok && (pv[c][i][3] == tag);
        if (all_ok(ok)) break;
        __builtin_amdgcn_s_sleep(2);
      }
      STAMPAT(7);
      f32x4 kt[5];
      {
        const bool dg = (g == (fr >> 2));
        const int r = fr & 3;
        bf16x8 sel[4];
#pragma unroll
        for (int q = 0; q < 4; ++q) {
          const int j = q == 0 ? r : (q == 1 ? (r < 2 ? 4 + r : 8) : (q == 2 ? (r >= 2 ? r - 2 : 8) : 2 + r));
          const unsigned one = dg ? ((j & 1) ? 0x3F800000u : 0x00003F80u) : 0u;
          sel[q] = asb(u32x4{(j >> 1) == 0 ? one : 0u, (j >> 1) == 1 ? one : 0u, (j >> 1) == 2 ? one : 0u, 0u});
        }
#pragma unroll
        for (int t = 0; t < 5; ++t) kt[t] = z4;
#pragma unroll
        for (int c = 0; c < 6; ++c) {
          kt[0] = mfma32(sel[0], asb(pv[c][0]), kt[0]);
          kt[1] = mfma32(sel[1], asb(pv[c][0]), kt[1]);
          kt[2] = mfma32(sel[3], asb(pv[c][1]), kt[2]);
          kt[3] = mfma32(sel[0], asb(pv[c][2]), kt[3]);
          kt[4] = mfma32(sel[1], asb(pv[c][2]), kt[4]);
          kt[1] = mfma32(sel[2], asb(pv[c][1]), kt[1]);
          kt[4] = mfma32(sel[2], asb(pv[c][3]), kt[4]);
        }
      }
      ldsp mine = sm + w * 17408 + 12288, other = sm + (w ^ 4) * 17408 + 12288;
      SCHED0();
#pragma unroll
      for (int i = 0; i < 5; ++i) *(LAS f32x4*)(mine + i * 1024 + lane * 16) = kt[i];
      LGKM_BAR();
#pragma unroll
      for (int i = 0; i < 5; ++i) {
        const f32x4 e = kt[i] + *(const LAS f32x4*)(other + i * 1024 + lane * 16);
#pragma unroll
        for (int q = 0; q < 4; ++q) ks[4 * i + q] = e[q];
      }
    }
    const bf16x8 ka0 = asb(u32x4{pk2(ks[0], ks[1]), pk2(ks[2], ks[3]), pk2(ks[4], ks[5]), pk2(ks[6], ks[7])});
    const bf16x8 ka1 = asb(u32x4{pk2(ks[8], ks[9]), pk2(ks[10], ks[11]), pk2(ks[12], ks[13]), pk2(ks[14], ks[15])});
    const bf16x8 kb0 = asb(u32x4{pk2(ks[16], ks[17]), 0u, 0u, 0u});
    const bf16x8 kb1 = asb(u32x4{pk2(ks[18], ks[19]), 0u, 0u, 0u});
    {
      u32x2 om_[MT], or_[MT];
#pragma unroll
      for (int m = 0; m < MT; ++m) {
        bf16x8 b0 = asb(u32x4{qf[m][0], qf[m][1], qf[m][2], qf[m][3]});
        bf16x8 b1 = asb(u32x4{qf[m][4], 0u, 0u, 0u});
        f32x4 n0 = mfma32(kb0, b1, mfma32(ka0, b0, z4));
        f32x4 n1 = mfma32(kb1, b1, mfma32(ka1, b0, z4));
        float da = n1[0], db = n1[0];
        swap16(da, db);
        const f32x2 inv = sp2(__builtin_amdgcn_rcpf(db + 1e-6f));
        om_[m] = u32x2{pk2v(lo2(n0) * inv), pk2v(hi2(n0) * inv)};
        or_[m] = u32x2{pk2v(lo2(n1) * inv), pk2v(hi2(n1) * inv)};
      }
      SCHED0();
      ldsp stg = sm + w * 17408 + 8192;
#pragma unroll
      for (int m = 0; m < MT; ++m) {
        *(LAS u32x2*)(stg + (m * 16 + fr) * 32 + g * 8) = om_[m];
        if (g == 0) *(LAS u32x2*)(stg + 3072 + (m * 16 + fr) * 8) = or_[m];
      }
      LGKM0();
      const int tok0 = r0 + mh * 95;
      const int ntok = mh == 0 ? 95 : nrow - 95;
      char* om = ws + WS_OM + ((size_t)head * S_ + tok0) * 32;
      char* orr = ws + WS_OR + ((size_t)head * S_ + tok0) * 8;
      u32x4 tv[3];
      u32x2 tr[2];
#pragma unroll
      for (int i = 0; i < 3; ++i) tv[i] = *(const LAS u32x4*)(stg + i * 1024 + lane * 16);
#pragma unroll
      for (int i = 0; i < 2; ++i) tr[i] = *(const LAS u32x2*)(stg + 3072 + ((i * 64 + lane) < 96 ? (i * 64 + lane) : 0) * 8);
#pragma unroll
      for (int i = 0; i < 3; ++i)
        if (i * 32 + (lane >> 1) < ntok) st128_sc1(om + i * 1024 + lane * 16, tv[i]);
#pragma unroll
      for (int i = 0; i < 2; ++i)
        if (i * 64 + lane < ntok) st64_sc1(orr + (i * 64 + lane) * 8, tr[i]);
    }
    WAITN_BAR(0);
    if (w == 0 && lane == 0) st64_sc1(ws + WS_OF + rg * 128 + ct * 8, ep);
    STAMPAT(8);
  }
  return ep;
}

DI u32x2 service(ldsp sm, const char* x, char* ws, int sid, int w, int lane) {
  const int bid = __builtin_amdgcn_workgroup_id_x();
  const int g = lane >> 4, fr = lane & 15;
  STAMPAT(0);
  if (w == 0) {
    unsigned long long nn = __builtin_amdgcn_s_memrealtime();
    u32x2 nv = u32x2{(unsigned)nn, (unsigned)(nn >> 32)};
    if (lane == 0) {
      if (sid != 15) st64_sc1(ws + WS_REQ + bid * 64, nv);
      *(LAS u32x2*)(sm + L_NONCE) = nv;
    }
  }
  LGKM_BAR();
  const u32x2 nv = *(const LAS u32x2*)(sm + L_NONCE);
  u32x2 ep = u32x2{nv[0] ^ 0x5bd1e995u, nv[1] ^ 0x80000000u};
  if (sid == 15 && w < 4) {
    const int cb = w * 64 + lane;
    const bool me = (cb == bid);
    u32x2 lastq = u32x2{0u, 0u};
    for (unsigned spins = 0; spins < SPIN_MAX; ++spins) {
      u32x2 rq, dn;
      asm volatile("global_load_dwordx2 %0, %2, off sc1\n\tglobal_load_dwordx2 %1, %3, off sc1\n\ts_waitcnt vmcnt(0)"
                   : "=&v"(rq), "=&v"(dn) : "v"(ws + WS_REQ + cb * 64), "v"(ws + WS_DONE + cb * 64) : "memory");
      if (all_ok(me || (dn[0] == ep[0] && dn[1] == ep[1]))) break;
      if (spins == 0 || rq[0] != lastq[0] || rq[1] != lastq[1]) st128_sc1(ws + WS_ACK + cb * 64, u32x4{rq[0], rq[1], ep[0], ep[1]});
      lastq = rq;
      if (spins > 12) __builtin_amdgcn_s_sleep(16);
    }
  } else {
    const int npass = sid == 15 ? 2 : 1;
    bool have = (sid == 15);
#pragma unroll 1
    for (int ps = 0; ps < npass; ++ps) {
      const int rbase = sid * 128 + (sid == 15 ? (w - 4) * 32 + ps * 16 : w * 16) + g;
      u32x4 v[4][5];
#pragma unroll
      for (int i = 0; i < 4; ++i)
#pragma unroll
        for (int c = 0; c < 5; ++c) v[i][c] = *(const u32x4*)(x + (size_t)(rbase + 4 * i) * 1280 + fr * 16 + c * 256);
      float mean[4], rstd[4];
#pragma unroll
      for (int i = 0; i < 4; ++i) {
        f32x2 a = sp2(0.f);
#pragma unroll
        for (int c = 0; c < 5; ++c)
#pragma unroll
          for (int e = 0; e < 4; ++e) a += f32x2{u2f(v[i][c][e] << 16), u2f(v[i][c][e] & 0xffff0000u)};
        float s = a[0] + a[1];
        s += dpp<0xB1>(s); s += dpp<0x4E>(s); s += dpp<0x141>(s); s += dpp<0x140>(s);
        mean[i] = s * (1.0f / 640.0f);
        const f32x2 m2 = sp2(opq(mean[i]));
        f32x2 b = sp2(0.f);
#pragma unroll
        for (int c = 0; c < 5; ++c)
#pragma unroll
          for (int e = 0; e < 4; ++e) {
            const f32x2 d = f32x2{u2f(v[i][c][e] << 16), u2f(v[i][c][e] & 0xffff0000u)} - m2;
            b = fma2(d, d, b);
          }
        float t = b[0] + b[1];
        t += dpp<0xB1>(t); t += dpp<0x4E>(t); t += dpp<0x141>(t); t += dpp<0x140>(t);
        rstd[i] = __builtin_amdgcn_rsqf(t * (1.0f / 640.0f) + 1e-5f);
      }
      if (!have) {
        u32x4 ack;
        for (unsigned spins = 0; spins < SPIN_MAX; ++spins) {
          ack = ld128_sc1(ws + WS_ACK + bid * 64);
          if (all_ok(ack[0] == nv[0] && ack[1] == nv[1])) break;
          __builtin_amdgcn_s_sleep(2);
        }
        ep = u32x2{ack[2], ack[3]};
        have = true;
        if (w == 0 && lane == 0) st64_sc1(ws + WS_DONE + bid * 64, ep);
      }
#pragma unroll
      for (int i = 0; i < 4; ++i)
        if (fr == 0) st128_sc1(ws + WS_STAT + (size_t)(rbase + 4 * i) * 16, u32x4{f2u(mean[i]), f2u(rstd[i]), ep[0], ep[1]});
    }
  }
  STAMPAT(1);
  return ep;
}

__global__ __launch_bounds__(512, 2) void fused(const char* __restrict__ x, const char* __restrict__ wqkv,
                                                const char* __restrict__ wphi, const char* __restrict__ wo,
                                                const char* __restrict__ wg, const char* __restrict__ wu,
                                                const char* __restrict__ wd, char* __restrict__ ws, char* __restrict__ out) {
  extern __shared__ __attribute__((aligned(16))) char smem[];
  ldsp sm = (ldsp)smem;
  const int tid = __builtin_amdgcn_workitem_id_x();
  const int bp = __builtin_amdgcn_workgroup_id_x(), xcd = bp & 7, ip = bp >> 3;
  const int idx = ip < 2 ? 30 + ip : ip - 2;
  const int b = idx * 8 + xcd;
  const int w = __builtin_amdgcn_readfirstlane(tid >> 6), lane = tid & 63;
  const u32x4 srd_wd = make_srd(wd, DF_ * D_ * 2);
  const int rgl = idx / 5, c5 = idx % 5;
  const int rg = (xcd >> 2) * 6 + rgl, cq = xcd & 3;
  if (idx < 30 && c5 >= 3) {
    const int ct = cq * 2 + (c5 - 3);
    if (w < 4) phase1<6, false>(sm, x, wqkv, wphi, wg, wu, srd_wd, ws, rg, ct, 0, 0, w, lane);
    else phase1<5, false>(sm, x, wqkv, wphi, wg, wu, srd_wd, ws, rg, ct, 0, 0, w, lane);
    return;
  }
  const int p2i = idx < 30 ? rgl * 3 + c5 : 18 + (idx - 30);
  const int rt = (xcd >> 1) * 4 + p2i / 5, ct2 = (xcd & 1) * 5 + p2i % 5;
#define ROTF ((((p2i % 5) * 12) / 5 + (p2i / 5) * 3) % 12)
  const int rot = ROTF;
  u32x2 ep;
  if (idx < 30) {
    const int ct = cq * 3 + c5;
    if (w < 4) ep = phase1<6, true>(sm, x, wqkv, wphi, wg, wu, srd_wd, ws, rg, ct, ct2 * 64, rot, w, lane);
    else ep = phase1<5, true>(sm, x, wqkv, wphi, wg, wu, srd_wd, ws, rg, ct, ct2 * 64, rot, w, lane);
  } else {
    ep = service(sm, x, ws, b - 240, w, lane);
    {
      const u32x4 srd_wo = make_srd(wo, D_ * D_ * 2);
      const unsigned vp = (lane >> 3) * 1280 + (lane & 7) * 16 + (xcd & 1) * 640;
      const unsigned dst = (unsigned)(size_t)sm + w * 1024;
      const int w16 = (idx - 30) * 8 + w;
      if (!(b == 255 && w < 4)) {
#pragma unroll 1
        for (int p = w16; p < 192; p += 16) {
#pragma unroll
          for (int c = 0; c < 5; ++c) dma_b(srd_wd, vp, p * 8 * 1280 + c * 128, dst);
          asm volatile("s_waitcnt vmcnt(30)" ::: "memory");
        }
#pragma unroll 1
        for (int p = w16; p < 80; p += 16) {
#pragma unroll
          for (int c = 0; c < 5; ++c) dma_b(srd_wo, vp, p * 8 * 1280 + c * 128, dst);
          asm volatile("s_waitcnt vmcnt(30)" ::: "memory");
        }
      }
      WAITN_BAR(0);
    }
    const unsigned vB = p2_vB(w, lane);
    const unsigned smb = (unsigned)(size_t)sm;
    p2_issueB_down(srd_wd, vB, ct2 * 64, w, smb, 0, rot);
    p2_issueB_down(srd_wd, vB, ct2 * 64, w, smb, 1, rot);
    p2_issueB_down(srd_wd, vB, ct2 * 64, w, smb, 2, rot);
  }
  phase2(sm, x, wo, srd_wd, ws, out, rt, ct2, rot, w, lane, ep);
}

#if defined(__HIP_DEVICE_COMPILE__)

#endif

extern "C" void kernel_launch(void* const* d_in, const int* in_sizes, int n_in, void* d_out, int out_size, void* d_ws,
                              size_t ws_size, hipStream_t stream) {
  (void)hipFuncSetAttribute((const void*)fused, hipFuncAttributeMaxDynamicSharedMemorySize, K_LDS);
  fused<<<256, 512, K_LDS, stream>>>((const char*)d_in[0], (const char*)d_in[1], (const char*)d_in[2], (const char*)d_in[3],
                                     (const char*)d_in[5], (const char*)d_in[6], (const char*)d_in[7], (char*)d_ws, (char*)d_out);
}
```

```cpp
#include <hip/hip_runtime.h>
#include <hip/hip_bf16.h>
#include <stdint.h>

typedef __attribute__((ext_vector_type(8))) __bf16 bf16x8;
typedef __attribute__((ext_vector_type(2))) __bf16 bf16x2;
typedef __attribute__((ext_vector_type(4))) short s16x4;
typedef __attribute__((ext_vector_type(8))) short s16x8;
typedef __attribute__((ext_vector_type(4))) float f32x4;
typedef __attribute__((ext_vector_type(2))) float f32x2;
typedef __attribute__((ext_vector_type(4))) unsigned u32x4;
typedef __attribute__((ext_vector_type(2))) unsigned u32x2;
#define LAS __attribute__((address_space(3)))
#define DI __device__ __forceinline__
typedef LAS char* ldsp;

constexpr int S_ = 2048, D_ = 640, DF_ = 1536, NH = 32;
constexpr int NRG = 12;
constexpr size_t WS_H = 0;
constexpr size_t WS_KV = WS_H + (size_t)S_ * DF_ * 2;
constexpr size_t WS_OM = WS_KV + (size_t)NRG * NH * 4096;
constexpr size_t WS_OR = WS_OM + (size_t)NH * S_ * 32;
constexpr size_t WS_REQ = WS_OR + (size_t)NH * S_ * 8;
constexpr size_t WS_ACK = WS_REQ + 256 * 64;
constexpr size_t WS_DONE = WS_ACK + 256 * 64;
constexpr size_t WS_STAT = WS_DONE + 256 * 64;
constexpr size_t WS_HF = WS_STAT + 2048 * 16;
constexpr size_t WS_KF = WS_HF + 12 * 128;
constexpr size_t WS_OF = WS_KF + 8 * 128;

constexpr int A_SLOT = 22528, B_SLOT = 16384;
constexpr int L_A = 0;
constexpr int L_B = 3 * A_SLOT;
constexpr int L_MISC = L_B + 5 * B_SLOT;
constexpr int L_DUMMY = L_MISC;
constexpr int L_NONCE = L_MISC + 1024;
constexpr int L_CS = L_NONCE + 64;
constexpr int K_LDS = L_CS + 2048;
constexpr int P2_A = 0, P2_B = 98304;
constexpr unsigned SPIN_MAX = 1u << 20;
#define POLLS 6


DI float u2f(unsigned u) { return __builtin_bit_cast(float, u); }
DI unsigned f2u(float f) { return __builtin_bit_cast(unsigned, f); }
DI u32x4 make_srd(const void* p, unsigned bytes) {
  uint64_t a = (uint64_t)p;
  u32x4 r;
  r[0] = (unsigned)a;
  r[1] = (unsigned)(a >> 32) & 0xffffu;
  r[2] = bytes;
  r[3] = 0x00020000u;
  return r;
}
DI void dma_b(u32x4 srd, unsigned voff, unsigned soff, unsigned m0v) {
  asm volatile("s_mov_b32 m0, %3\n\ts_nop 0\n\tbuffer_load_dwordx4 %0, %1, %2 offen lds" ::"v"(voff), "s"(srd), "s"(soff), "s"(m0v) : "memory");
}
DI void dma_b_mask(u32x4 srd, unsigned voff, unsigned soff, unsigned m0v, unsigned long long mask) {
  asm volatile("s_mov_b32 m0, %3\n\ts_mov_b64 exec, %4\n\tbuffer_load_dwordx4 %0, %1, %2 offen lds\n\ts_mov_b64 exec, -1" ::"v"(voff), "s"(srd), "s"(soff), "s"(m0v), "s"(mask) : "memory");
}
DI void dma_b_sc1(u32x4 srd, unsigned voff, unsigned soff, unsigned m0v) {
  asm volatile("s_mov_b32 m0, %3\n\ts_nop 0\n\tbuffer_load_dwordx4 %0, %1, %2 offen sc1 lds" ::"v"(voff), "s"(srd), "s"(soff), "s"(m0v) : "memory");
}
DI u32x4 ld128_sc1(const char* p) { u32x4 v; asm volatile("global_load_dwordx4 %0, %1, off sc1\n\ts_waitcnt vmcnt(0)" : "=&v"(v) : "v"(p) : "memory"); return v; }
DI u32x2 ld64_sc1(const char* p) { u32x2 v; asm volatile("global_load_dwordx2 %0, %1, off sc1\n\ts_waitcnt vmcnt(0)" : "=&v"(v) : "v"(p) : "memory"); return v; }
DI void st128_sc1(char* p, u32x4 v) { asm volatile("global_store_dwordx4 %0, %1, off sc1\n\ts_nop 1" ::"v"(p), "v"(v) : "memory"); }
DI void st64_sc1(char* p, u32x2 v) { asm volatile("global_store_dwordx2 %0, %1, off sc1" ::"v"(p), "v"(v) : "memory"); }
#define WAITN_BAR(N) asm volatile("s_waitcnt vmcnt(%0) lgkmcnt(0)\n\ts_barrier" ::"n"(N) : "memory")
#define LGKM_BAR() asm volatile("s_waitcnt lgkmcnt(0)\n\ts_barrier" ::: "memory")
#define LGKM0() asm volatile("s_waitcnt lgkmcnt(0)" ::: "memory")
#define MEMFENCE() asm volatile("" ::: "memory")
#define SCHED0() __builtin_amdgcn_sched_barrier(0)

DI bf16x8 ld128(ldsp p) { return *(const LAS bf16x8*)p; }
DI s16x4 tr1(ldsp p) { return __builtin_amdgcn_ds_read_tr16_b64_v4i16((LAS s16x4*)p); }
DI bf16x8 cat(s16x4 a, s16x4 b) { return __builtin_bit_cast(bf16x8, __builtin_shufflevector(a, b, 0, 1, 2, 3, 4, 5, 6, 7)); }
DI bf16x8 tr2(ldsp p0, ldsp p1) { return cat(tr1(p0), tr1(p1)); }
DI f32x4 mfma32(bf16x8 a, bf16x8 b, f32x4 c) { return __builtin_amdgcn_mfma_f32_16x16x32_bf16(a, b, c, 0, 0, 0); }
DI f32x4 mfma16(s16x4 a, s16x4 b, f32x4 c) { return __builtin_amdgcn_mfma_f32_16x16x16bf16_1k(a, b, c, 0, 0, 0); }
DI bf16x8 asb(u32x4 v) { return __builtin_bit_cast(bf16x8, v); }
DI unsigned pk2(float a, float b) {
  bf16x2 v;
  v[0] = (__bf16)a;
  v[1] = (__bf16)b;
  return __builtin_bit_cast(unsigned, v);
}
DI void swap16(float& a, float& b) {
  auto r = __builtin_amdgcn_permlane16_swap(f2u(a), f2u(b), false, false);
  a = u2f(r[0]); b = u2f(r[1]);
}
DI void swap32(float& a, float& b) {
  auto r = __builtin_amdgcn_permlane32_swap(f2u(a), f2u(b), false, false);
  a = u2f(r[0]); b = u2f(r[1]);
}
template <int CTRL> DI float dpp(float x) { return u2f(__builtin_amdgcn_update_dpp(0u, f2u(x), CTRL, 0xf, 0xf, false)); }
DI float fmx(float a, float b) { return __builtin_fmaxf(a, b); }
DI float fmn(float a, float b) { return __builtin_fminf(a, b); }
DI float rawmax(float a, float b) { float r; asm("v_max_f32 %0, %1, %2" : "=v"(r) : "v"(a), "v"(b)); return r; }
DI float opq(float x) { asm("" : "+v"(x)); return x; }
DI f32x2 lo2(f32x4 v) { return __builtin_shufflevector(v, v, 0, 1); }
DI f32x2 hi2(f32x4 v) { return __builtin_shufflevector(v, v, 2, 3); }
DI f32x2 sp2(float x) { return f32x2{x, x}; }
DI f32x2 fma2(f32x2 a, f32x2 b, f32x2 c) { return __builtin_elementwise_fma(a, b, c); }
DI int strow(int s, int nrow) { int ri = s < 95 ? s : s - 1; return ri < nrow ? ri : nrow - 1; }
DI int rs_of(int rg) { return (512 * rg) / 3; }
DI int rg_of(int row) { return (3 * row + 2) >> 9; }
DI bool all_ok(bool ok) { return __builtin_amdgcn_ballot_w64(ok) == ~0ull; }

DI void wait_flags(const char* p, bool need, u32x2 ep) {
  for (unsigned spins = 0; spins < SPIN_MAX; ++spins) {
    u32x2 v = ld64_sc1(p);
    if (all_ok(!need || (v[0] == ep[0] && v[1] == ep[1]))) break;
    __builtin_amdgcn_s_sleep(POLLS);
  }
}

DI f32x2 ex2(f32x2 v) { return f32x2{__builtin_amdgcn_exp2f(v[0]), __builtin_amdgcn_exp2f(v[1])}; }
DI unsigned pk2v(f32x2 v) { return pk2(v[0], v[1]); }
DI void featmap2(f32x2 a01, f32x2 a23, f32x2 b01, f32x2 b23, f32x2 e4, bool livea, bool liveb, int g, unsigned (&oa)[5], unsigned (&ob)[5]) {
  float a0 = fmx(fmx(fmx(a01[0], a01[1]), a23[0]), fmx(a23[1], e4[0]));
  float a1 = -fmn(fmn(fmn(a01[0], a01[1]), a23[0]), fmn(a23[1], e4[0]));
  float b0 = fmx(fmx(fmx(b01[0], b01[1]), b23[0]), fmx(b23[1], e4[1]));
  float b1 = -fmn(fmn(fmn(b01[0], b01[1]), b23[0]), fmn(b23[1], e4[1]));
  swap16(a0, a1); swap16(b0, b1);
  float ca = rawmax(a0, a1), cb = rawmax(b0, b1);
  swap32(ca, cb);
  float d0 = rawmax(ca, cb), d1 = d0;
  swap32(d0, d1);
  float Ma = d0, Na = d0, Mb = d1, Nb = d1;
  swap16(Ma, Na); swap16(Mb, Nb);
  const f32x2 pa01 = ex2(a01 - sp2(Ma)), pa23 = ex2(a23 - sp2(Ma)), na01 = ex2(-a01 - sp2(Na)), na23 = ex2(-a23 - sp2(Na));
  const f32x2 pb01 = ex2(b01 - sp2(Mb)), pb23 = ex2(b23 - sp2(Mb)), nb01 = ex2(-b01 - sp2(Nb)), nb23 = ex2(-b23 - sp2(Nb));
  const float pa4 = __builtin_amdgcn_exp2f(e4[0] - Ma), na4 = __builtin_amdgcn_exp2f(-e4[0] - Na);
  const float pb4 = __builtin_amdgcn_exp2f(e4[1] - Mb), nb4 = __builtin_amdgcn_exp2f(-e4[1] - Nb);
  f32x2 t;
  t = pa01 + pa23; a0 = (t[0] + t[1]) + pa4;
  t = na01 + na23; a1 = (t[0] + t[1]) + na4;
  t = pb01 + pb23; b0 = (t[0] + t[1]) + pb4;
  t = nb01 + nb23; b1 = (t[0] + t[1]) + nb4;
  swap16(a0, a1); swap16(b0, b1);
  ca = a0 + a1; cb = b0 + b1;
  swap32(ca, cb);
  d0 = __builtin_amdgcn_rcpf(ca + cb);
  if (!(g < 2 ? livea : liveb)) d0 = 0.f;
  d1 = d0;
  swap32(d0, d1);
  Ma = d0; Na = d0; Mb = d1; Nb = d1;
  swap16(Ma, Na); swap16(Mb, Nb);
  oa[0] = pk2v(pa01 * sp2(Ma)); oa[1] = pk2v(pa23 * sp2(Ma));
  oa[2] = pk2v(na01 * sp2(Na)); oa[3] = pk2v(na23 * sp2(Na));
  oa[4] = pk2(pa4 * Ma, na4 * Na);
  ob[0] = pk2v(pb01 * sp2(Mb)); ob[1] = pk2v(pb23 * sp2(Mb));
  ob[2] = pk2v(nb01 * sp2(Nb)); ob[3] = pk2v(nb23 * sp2(Nb));
  ob[4] = pk2(pb4 * Mb, nb4 * Nb);
}

struct P2Ctx { unsigned vB, vR; int c0; };
DI unsigned p2_vB(int w, int lane) {
  const int l3 = lane >> 3, l7 = lane & 7;
  int fb = ((l3 >> 1) & 1) | ((w & 1) << 1);
  int ch = (((l7 >> 1) ^ fb) << 1) | (l7 & 1);
  return l3 * 1280 + ch * 16;
}
DI int p2_rot(int s, int rot) { int r = s + rot; return r >= 12 ? r - 12 : r; }
DI void p2_issueB_down(u32x4 srd_wd, unsigned vB, int c0, int w, unsigned smb, int s, int rot) {
  const int sr = p2_rot(s, rot);
  dma_b(srd_wd, vB, (128 * sr + 8 * w) * 1280 + c0 * 2, smb + P2_B + (s % 3) * 16384 + w * 1024);
  dma_b(srd_wd, vB, (128 * sr + 8 * w + 64) * 1280 + c0 * 2, smb + P2_B + (s % 3) * 16384 + w * 1024 + 8192);
}

DI void phase2(ldsp sm, const char* x, const char* wo, const u32x4 srd_wd, char* ws, char* out, int rt, int ct, int rot, int w, int lane, u32x2 ep) {
  const int r0 = rt * 128, c0 = ct * 64;
  const int g = lane >> 4, fr = lane & 15, l3 = lane >> 3, l7 = lane & 7;
  const int kh = w & 1, nh = (w >> 1) & 1, mh = w >> 2;
  const unsigned smb = (unsigned)(size_t)sm;
  const u32x4 srd_h = make_srd(ws + WS_H, S_ * DF_ * 2);
  const u32x4 srd_wo = make_srd(wo, D_ * D_ * 2);
  const u32x4 srd_om = make_srd(ws + WS_OM, NH * S_ * 32);
  const u32x4 srd_or = make_srd(ws + WS_OR, NH * S_ * 8);
  const unsigned vB = p2_vB(w, lane);
  const unsigned vA = l3 * 3072 + ((l7 ^ ((4 * (w & 1) + (l3 >> 1)) & 7)) * 16);
  const unsigned vR = vB - l3 * 1280 + (20 * (lane >> 5) + 16 + (l3 & 3)) * 1280;
  const unsigned vL = lane * 16;
  const int rgl = rg_of(r0), rgh = rg_of(r0 + 127);

#define P2_PIECE(s, p) do { \
    constexpr int i_ = (p) < 2 ? (p) : (p) - 2; \
    if ((s) < 12) { \
      if ((p) < 2) dma_b(srd_wd, vB, (128 * p2_rot((s), rot) + 8 * w + 64 * i_) * 1280 + c0 * 2, smb + P2_B + ((s) % 3) * 16384 + i_ * 8192 + w * 1024); \
      else dma_b_sc1(srd_h, vA, (r0 + 8 * w + 64 * (i_ & 1)) * 3072 + p2_rot((s), rot) * 256 + (i_ >> 1) * 128, smb + P2_A + ((s) % 3) * 32768 + (i_ >> 1) * 16384 + (i_ & 1) * 8192 + w * 1024); \
    } else if ((s) < 16) { \
      if ((p) < 2) dma_b(srd_wo, vB, (160 * ((s) - 12) + 20 * ((w + 8 * i_) >> 1) + 8 * (w & 1)) * 1280 + c0 * 2, smb + P2_B + ((s) % 3) * 16384 + i_ * 8192 + w * 1024); \
      else dma_b_sc1(srd_om, vL, ((8 * ((s) - 12) + 2 * i_ + (w >> 2)) * S_ + r0) * 32 + (w & 3) * 1024, smb + P2_A + ((s) % 3) * 32768 + i_ * 8192 + w * 1024); \
    } else { \
      if ((p) < 2) dma_b(srd_wo, vR, (40 * (w + 8 * i_)) * 1280 + c0 * 2, smb + P2_B + ((s) % 3) * 16384 + i_ * 8192 + w * 1024); \
      else dma_b_sc1(srd_or, vL, ((8 * i_ + w) * S_ + r0) * 8, smb + P2_A + ((s) % 3) * 32768 + i_ * 8192 + w * 1024); \
    } } while (0)

  const int aD = P2_A + kh * 16384 + (64 * mh + fr) * 128 + ((g ^ ((fr >> 1) & 7)) * 16);
  const int aM = P2_A + (4 * kh + (g >> 1)) * 4096 + (64 * mh + fr) * 32 + (g & 1) * 16;
  const int aR = P2_A + (16 * kh + 2 * g) * 1024 + (64 * mh + fr) * 8;
  int bO[2];
  {
    const int q = fr >> 2, p = lane & 3;
    const int f = ((q >> 1) & 1) | ((g & 1) << 1);
#pragma unroll
    for (int j = 0; j < 2; ++j) bO[j] = P2_B + (64 * kh + 8 * g + q) * 128 + (((2 * nh + j) ^ f) * 32) + p * 8;
  }
  f32x4 acc[4][2];
#pragma unroll
  for (int m = 0; m < 4; ++m)
#pragma unroll
    for (int j = 0; j < 2; ++j) acc[m][j] = f32x4{0.f, 0.f, 0.f, 0.f};

  if (w == 0) wait_flags(ws + WS_HF + (size_t)(lane < 16 ? rgl : rgh) * 128 + (lane & 15) * 8, lane < 32 && (lane & 15) < 12, ep);
  WAITN_BAR(0);
#define P2_A3(s) do { P2_PIECE(s, 2); P2_PIECE(s, 3); P2_PIECE(s, 4); P2_PIECE(s, 5); } while (0)
  P2_A3(0); P2_A3(1); P2_A3(2);
  u32x4 xr[2];
  bf16x8 fa[2][8];
  s16x4 fb[2][8];
#define P2_RD(s, i) do { \
    if ((i) < 8) fb[(s) & 1][i] = tr1(sm + ((s) % 3) * 16384 + bO[((i) >> 1) & 1] + ((i) >> 2) * 4096 + ((i) & 1) * 512); \
    else if ((i) < 16) { \
      constexpr int sl_ = ((i) - 8) >> 2, m_ = ((i) - 8) & 3; \
      if ((s) < 12) fa[(s) & 1][((i) - 8) & 7] = ld128(sm + ((s) % 3) * 32768 + (sl_ ? (aD ^ 64) : aD) + m_ * 2048); \
      else if ((s) < 16) fa[(s) & 1][((i) - 8) & 7] = ld128(sm + ((s) % 3) * 32768 + aM + sl_ * 8192 + m_ * 512); \
      else fa[(s) & 1][((i) - 8) & 7] = cat(*(const LAS s16x4*)(sm + ((s) % 3) * 32768 + aR + sl_ * 8192 + m_ * 128), *(const LAS s16x4*)(sm + ((s) % 3) * 32768 + aR + sl_ * 8192 + 1024 + m_ * 128)); } } while (0)
#define P2_MM(s, i) acc[((i) >> 1) & 3][(i) & 1] = mfma32(cat(fb[(s) & 1][((i) >> 3) * 4 + 2 * ((i) & 1)], fb[(s) & 1][((i) >> 3) * 4 + 2 * ((i) & 1) + 1]), fa[(s) & 1][((i) >> 3) * 4 + (((i) >> 1) & 3)], acc[((i) >> 1) & 3][(i) & 1])
#define P2_GROUP(s, q) do { \
    if ((s) >= 1 && (s) + 2 < 17 && (s) != 12 && !(((s) == 10 || (s) == 11) && (q) >= 2)) P2_PIECE((s) + 2, q); \
    SCHED0(); \
    if ((s) < 17) { P2_RD(s, 3 * (q)); P2_RD(s, 3 * (q) + 1); P2_RD(s, 3 * (q) + 2); } \
    SCHED0(); \
    if ((s) >= 1 && (s) != 12) { P2_MM((s) - 1, ((q) * 8) / 3); P2_MM((s) - 1, ((q) * 8) / 3 + 1); if (((q) * 8) / 3 + 2 < (((q) + 1) * 8) / 3) P2_MM((s) - 1, ((q) * 8) / 3 + 2); } \
    SCHED0(); } while (0)
#define P2_STAGE(s) do { \
    SCHED0(); \
    if ((s) < 17) WAITN_BAR(((s) == 0 || (s) == 12) ? 8 : (((s) == 1 || (s) == 13) ? 4 : ((s) == 11 ? 2 : ((s) < 16 ? 6 : 0)))); \
    SCHED0(); \
    P2_GROUP(s, 0); P2_GROUP(s, 1); P2_GROUP(s, 2); P2_GROUP(s, 3); P2_GROUP(s, 4); P2_GROUP(s, 5); } while (0)
  P2_STAGE(0); P2_STAGE(1); P2_STAGE(2); P2_STAGE(3); P2_STAGE(4); P2_STAGE(5); P2_STAGE(6); P2_STAGE(7); P2_STAGE(8);
  P2_STAGE(9); P2_STAGE(10); P2_STAGE(11);
  SCHED0();
#pragma unroll
  for (int i = 0; i < 16; ++i) P2_MM(11, i);
  SCHED0();
  if (w == 0) wait_flags(ws + WS_OF + (size_t)(lane < 16 ? rgl : rgh) * 128 + (lane & 15) * 8, lane < 32 && (lane & 15) < 8, ep);
  LGKM_BAR();
  P2_PIECE(14, 0); P2_PIECE(14, 1);
  P2_A3(12); P2_A3(13); P2_A3(14);
  P2_STAGE(12); P2_STAGE(13); P2_STAGE(14); P2_STAGE(15);
  const int orow = 64 * mh + 32 * kh + (lane >> 2), ocol = c0 * 2 + nh * 64 + (lane & 3) * 16;
#pragma unroll
  for (int i = 0; i < 2; ++i)
    asm volatile("global_load_dwordx4 %0, %1, off" : "=&v"(xr[i]) : "v"(x + (size_t)(r0 + orow + 16 * i) * 1280 + ocol) : "memory");
  P2_STAGE(16); P2_STAGE(17);
  SCHED0();
  f32x4 keep[2][2];
  {
    ldsp ex = sm + w * 4096 + lane * 16;
#pragma unroll
    for (int i = 0; i < 2; ++i)
#pragma unroll
      for (int j = 0; j < 2; ++j) {
        f32x4 snd;
#pragma unroll
        for (int e = 0; e < 4; ++e) {
          const float lo_ = acc[i][j][e], hi_ = acc[2 + i][j][e];
          snd[e] = kh ? lo_ : hi_;
          keep[i][j][e] = kh ? hi_ : lo_;
        }
        *(LAS f32x4*)(ex + (i * 2 + j) * 1024) = snd;
      }
  }
  LGKM_BAR();
  {
    ldsp ex = sm + (w ^ 1) * 4096 + lane * 16;
    ldsp tp = sm + 65536 + w * 4608;
#pragma unroll
    for (int i = 0; i < 2; ++i)
#pragma unroll
      for (int j = 0; j < 2; ++j) {
        const f32x4 e = *(const LAS f32x4*)(ex + (i * 2 + j) * 1024);
        *(LAS f32x4*)(tp + (i * 16 + fr) * 144 + j * 64 + g * 16) = keep[i][j] + e;
      }
    asm volatile("" : "+v"(xr[0]), "+v"(xr[1]));
#pragma unroll
    for (int i = 0; i < 2; ++i) {
      const f32x4 s0 = *(const LAS f32x4*)(tp + (i * 16 + (lane >> 2)) * 144 + (lane & 3) * 32);
      const f32x4 s1 = *(const LAS f32x4*)(tp + (i * 16 + (lane >> 2)) * 144 + (lane & 3) * 32 + 16);
      u32x4 o;
#pragma unroll
      for (int e = 0; e < 2; ++e) {
        o[e] = pk2(s0[2 * e] + u2f(xr[i][e] << 16), s0[2 * e + 1] + u2f(xr[i][e] & 0xffff0000u));
        o[2 + e] = pk2(s1[2 * e] + u2f(xr[i][2 + e] << 16), s1[2 * e + 1] + u2f(xr[i][2 + e] & 0xffff0000u));
      }
      *(u32x4*)(out + (size_t)(r0 + orow + 16 * i) * 1280 + ocol) = o;
    }
  }
}

template <int MT, bool GU>
DI u32x2 phase1(ldsp sm, const char* x, const char* wqkv, const char* wphi, const char* wg, const char* wu, const u32x4 srd_wd,
                char* ws, int rg, int ct, int c0p2, int rot, int w, int lane) {
  constexpr int mh = (MT == 6) ? 0 : 1;
  const int wn = w & 3;
  const int r0 = rs_of(rg), nrow = rs_of(rg + 1) - r0;
  const unsigned smb = (unsigned)(size_t)sm;
  const int g = lane >> 4, fr = lane & 15;

  const u32x4 srdx = make_srd(x, S_ * D_ * 2);
  unsigned voffA[3];
#pragma unroll
  for (int c = 0; c < 3; ++c) {
    int slot = 8 * (w + 8 * c) + (lane >> 3);
    int ch = (lane & 7) ^ ((slot >> 1) & 7);
    int ri = slot < 95 ? slot : slot - 1;
    ri = ri < nrow ? ri : nrow - 1;
    voffA[c] = (unsigned)((r0 + ri) * 1280 + ch * 16);
  }
  if (w >= 6) voffA[2] = 0x40000000u;
  const unsigned long long FULL = ~0ull, PART = 0x00FFFFFFFFFFFFFFull;
  const unsigned long long mask1 = (w == 3) ? PART : FULL, mask2 = (w == 5) ? PART : FULL;
  const unsigned adst0 = smb + L_A + w * 1024;
  u32x4 srd0, srd1;
  unsigned voff0, voff1;
  constexpr unsigned SSTEP = GU ? 32 * 3072 : 32 * 3840;
  if (GU) {
    srd0 = make_srd(wg, D_ * DF_ * 2);
    srd1 = make_srd(wu, D_ * DF_ * 2);
    int krow = 4 * w + (lane >> 4);
    int chp = lane & 15;
    int f = (krow & 3) | (((krow >> 3) & 1) << 2);
    int ch = (((chp >> 1) ^ f) << 1) | (chp & 1);
    voff0 = voff1 = krow * 3072 + ct * 256 + ch * 16;
  } else {
    srd0 = srd1 = make_srd(wqkv, D_ * 3 * D_ * 2);
    unsigned vo[2];
#pragma unroll
    for (int c = 0; c < 2; ++c) {
      int cidx = (w + 8 * c) * 64 + lane;
      int rho = cidx / 30, cc = cidx % 30;
      int seg = cc / 10, ch = cc % 10;
      int k = (rho & 16) | ((rho & 4) << 1) | ((rho & 8) >> 1) | (rho & 3);
      vo[c] = (cidx < 960) ? (unsigned)(k * 3840 + seg * 1280 + ct * 160 + ch * 16) : 0x40000000u;
    }
    voff0 = vo[0];
    voff1 = vo[1];
  }
  const unsigned bdst0 = smb + L_B + w * 1024;
#define KROT (((rg%3)*3+(ct&1)*5)%10)
  const unsigned krot = KROT;
#define RA(so) ((so) + krot * 128u >= 1280u ? (so) + krot * 128u - 1280u : (so) + krot * 128u)
#define RB(so) ((so) + krot * 2u * SSTEP >= 20u * SSTEP ? (so) + krot * 2u * SSTEP - 20u * SSTEP : (so) + krot * 2u * SSTEP)
#define DMA_B0(soff, dst) dma_b(srd0, voff0, RB(soff), (dst))
#define DMA_B1(soff, dst) dma_b(srd1, voff1, RB(soff), (dst))
#define DMA_A0(soff, dst) dma_b(srdx, voffA[0], RA(soff), (dst))
#define DMA_A1(soff, dst) dma_b_mask(srdx, voffA[1], RA(soff), (dst), mask1)
#define DMA_A2(soff, dst) dma_b_mask(srdx, voffA[2], RA(soff), (dst), mask2)
  const unsigned a2base = (w >= 6) ? smb + L_DUMMY : adst0 + 16384;
  const unsigned a2mul = (w >= 6) ? 0u : 1u;
  unsigned wraw[16];
  if (!GU) {
    const char* wp = wphi + (ct * 4 + wn) * 800;
    const char* p0 = wp + (5 * (fr >> 2) + (fr & 3)) * 2;
    const char* p1 = wp + (5 * (fr >> 2) + 4) * 2;
    const char* q0 = p0 + g * 160;
    const char* q1 = p1 + g * 160;
#pragma unroll
    for (int j = 0; j < 4; ++j) {
      asm volatile("global_load_ushort %0, %1, off offset:%2" : "=&v"(wraw[j]) : "v"(q0), "n"(j * 40) : "memory");
      asm volatile("global_load_ushort %0, %1, off offset:%2" : "=&v"(wraw[4 + j]) : "v"(p0), "n"(640 + j * 40) : "memory");
      asm volatile("global_load_ushort %0, %1, off offset:%2" : "=&v"(wraw[8 + j]) : "v"(q1), "n"(j * 40) : "memory");
      asm volatile("global_load_ushort %0, %1, off offset:%2" : "=&v"(wraw[12 + j]) : "v"(p1), "n"(640 + j * 40) : "memory");
    }
  }
  DMA_B0(0, bdst0); DMA_B1(0, bdst0 + 8192);
  DMA_A0(0, adst0); DMA_A1(0, adst0 + 8192);
  DMA_A2(0, a2base);
  DMA_B0(SSTEP, bdst0 + B_SLOT); DMA_B1(SSTEP, bdst0 + B_SLOT + 8192);
  DMA_B0(2 * SSTEP, bdst0 + 2 * B_SLOT); DMA_B1(2 * SSTEP, bdst0 + 2 * B_SLOT + 8192);
  DMA_A0(128, adst0 + A_SLOT); DMA_A1(128, adst0 + A_SLOT + 8192);
  DMA_A2(128, a2base + a2mul * A_SLOT);
  DMA_B0(3 * SSTEP, bdst0 + 3 * B_SLOT); DMA_B1(3 * SSTEP, bdst0 + 3 * B_SLOT + 8192);
  DMA_B0(4 * SSTEP, bdst0 + 4 * B_SLOT); DMA_B1(4 * SSTEP, bdst0 + 4 * B_SLOT + 8192);
  DMA_A0(256, adst0 + 2 * A_SLOT); DMA_A1(256, adst0 + 2 * A_SLOT + 8192);
  if (w == 0 && lane < 48) {
    int rr = lane >> 3;
    *(LAS u32x4*)(sm + L_A + (rr >> 1) * A_SLOT + ((rr & 1) ? 175 : 95) * 128 + (lane & 7) * 16) =
        u32x4{0x3F803F80u, 0x3F803F80u, 0x3F803F80u, 0x3F803F80u};
  }
  const int bid = __builtin_amdgcn_workgroup_id_x();
  if (w == 1) {
    unsigned long long nn = __builtin_amdgcn_s_memrealtime();
    u32x2 nv = u32x2{(unsigned)nn, (unsigned)(nn >> 32)};
    if (lane == 0) {
      st64_sc1(ws + WS_REQ + bid * 64, nv);
      *(LAS u32x2*)(sm + L_NONCE) = nv;
    }
  }

  const int aoff0 = L_A + mh * 6 * 2048 + fr * 128 + ((g ^ ((fr >> 1) & 7)) * 16);
  const int aoff1 = aoff0 ^ 64;
  int boff[4];
  constexpr int TSTEP = GU ? 1024 : 3840;
  {
    const int q = fr >> 2, p = lane & 3;
    if (GU) {
      int f = q | ((g & 1) << 2);
#pragma unroll
      for (int j = 0; j < 4; ++j)
        boff[j] = L_B + (j >> 1) * 8192 + (8 * g + q) * 256 + (((2 * wn + (j & 1)) ^ f) * 32) + p * 8;
    } else {
      int rho = 16 * (g >> 1) + 4 * (g & 1) + q;
#pragma unroll
      for (int j = 0; j < 3; ++j) boff[j] = L_B + rho * 480 + (20 * j + 5 * wn + p) * 8;
      boff[3] = L_B + rho * 480 + (20 * (p == 3 ? 0 : p) + 5 * wn + 4) * 8;
    }
  }

  f32x4 acc[MT][4];
#pragma unroll
  for (int m = 0; m < MT; ++m)
#pragma unroll
    for (int j = 0; j < 4; ++j) acc[m][j] = f32x4{0.f, 0.f, 0.f, 0.f};
  bf16x8 fa0[MT], fa1[MT];
  s16x4 fb0[8], fb1[8];

  WAITN_BAR(13);
  unsigned wpk[8];
  if (!GU) {
    asm volatile("" : "+v"(wraw[0]), "+v"(wraw[1]), "+v"(wraw[2]), "+v"(wraw[3]), "+v"(wraw[4]), "+v"(wraw[5]), "+v"(wraw[6]), "+v"(wraw[7]),
                 "+v"(wraw[8]), "+v"(wraw[9]), "+v"(wraw[10]), "+v"(wraw[11]), "+v"(wraw[12]), "+v"(wraw[13]), "+v"(wraw[14]), "+v"(wraw[15]));
#pragma unroll
    for (int i = 0; i < 8; ++i) {
      wpk[i] = wraw[2 * i] | (wraw[2 * i + 1] << 16);
      if (i >= 4 && (fr & 3) != 0) wpk[i] = 0u;
    }
    asm volatile("" : "+v"(wpk[0]), "+v"(wpk[1]), "+v"(wpk[2]), "+v"(wpk[3]), "+v"(wpk[4]), "+v"(wpk[5]), "+v"(wpk[6]), "+v"(wpk[7]));
  }
#pragma unroll
  for (int m = 0; m < MT; ++m) fa0[m] = ld128(sm + aoff0 + m * 2048);
#pragma unroll
  for (int j = 0; j < 8; ++j) fb0[j] = tr1(sm + boff[j >> 1] + (j & 1) * TSTEP);
  MEMFENCE();

  constexpr int NM = MT * 4;
  constexpr int DS = (MT == 6) ? 6 : 5;
#define MMA(i, FA, FB) acc[(i) >> 2][(i) & 3] = mfma32(cat(FB[2 * ((i) & 3)], FB[2 * ((i) & 3) + 1]), FA[(i) >> 2], acc[(i) >> 2][(i) & 3])
#define BADDR(sl, i) (sm + (sl) * B_SLOT + boff[(i) >> 1] + ((i) & 1) * TSTEP)
  u32x4 ackv, stv[MT];
  const char* ackp = ws + WS_ACK + bid * 64;
#define STP(m) (ws + WS_STAT + (size_t)(r0 + strow(mh * 96 + (m) * 16 + fr, nrow)) * 16)
#pragma unroll
  for (int t = 0; t < 10; ++t) {
    SCHED0();
    WAITN_BAR(t < 8 ? 11 : (t == 8 ? 7 : 1 + MT));
    SCHED0();
#pragma unroll
    for (int i = 0; i < NM; ++i) {
      MMA(i, fa0, fb0);
      SCHED0();
      if (i < 8) fb1[i] = tr1(BADDR((2 * t + 1) % 5, i));
      else if (i < 8 + MT) fa1[i - 8] = ld128(sm + (t % 3) * A_SLOT + aoff1 + (i - 8) * 2048);
      if (t < 8) {
        if (i == 2) { if (MT == 6) DMA_A2((t + 2) * 128, adst0 + 16384 + ((t + 2) % 3) * A_SLOT); else DMA_A2((t + 2) * 128, a2base + a2mul * (((t + 2) % 3) * A_SLOT)); }
        if (i == 2 + DS) DMA_B0((2 * t + 5) * SSTEP, bdst0 + ((2 * t) % 5) * B_SLOT);
        if (i == 2 + 2 * DS) DMA_B1((2 * t + 5) * SSTEP, bdst0 + ((2 * t) % 5) * B_SLOT + 8192);
      }
      SCHED0();
    }
    if (t < 9) WAITN_BAR(t < 8 ? 9 : 2); else LGKM_BAR();
    SCHED0();
#pragma unroll
    for (int i = 0; i < NM; ++i) {
      MMA(i, fa1, fb1);
      SCHED0();
      if (t < 9) {
        if (i < 8) fb0[i] = tr1(BADDR((2 * t + 2) % 5, i));
        else if (i < 8 + MT) fa0[i - 8] = ld128(sm + ((t + 1) % 3) * A_SLOT + aoff0 + (i - 8) * 2048);
      }
      if (t < 7) {
        if (i == 1) DMA_B0((2 * t + 6) * SSTEP, bdst0 + ((2 * t + 1) % 5) * B_SLOT);
        if (i == 1 + DS) DMA_B1((2 * t + 6) * SSTEP, bdst0 + ((2 * t + 1) % 5) * B_SLOT + 8192);
        if (i == 1 + 2 * DS) DMA_A0((t + 3) * 128, adst0 + (t % 3) * A_SLOT);
        if (i == 1 + 3 * DS) DMA_A1((t + 3) * 128, adst0 + (t % 3) * A_SLOT + 8192);
      }
      if (i == 4 && t == 8) {
        asm volatile("global_load_dwordx4 %0, %1, off sc1" : "=&v"(ackv) : "v"(ackp) : "memory");
#pragma unroll
        for (int m = 0; m < MT; ++m) asm volatile("global_load_dwordx4 %0, %1, off sc1" : "=&v"(stv[m]) : "v"(STP(m)) : "memory");
      }
      SCHED0();
    }
  }
  SCHED0();

  if (fr == 15) {
#pragma unroll
    for (int j = 0; j < 4; ++j) *(LAS f32x4*)(sm + L_CS + w * 256 + j * 64 + g * 16) = acc[MT - 1][j];
  }
  if (MT == 6) asm volatile("s_waitcnt vmcnt(0) lgkmcnt(0)" : "+v"(ackv), "+v"(stv[0]), "+v"(stv[1]), "+v"(stv[2]), "+v"(stv[3]), "+v"(stv[4]), "+v"(stv[MT - 1]) :: "memory");
  else asm volatile("s_waitcnt vmcnt(0) lgkmcnt(0)" : "+v"(ackv), "+v"(stv[0]), "+v"(stv[1]), "+v"(stv[2]), "+v"(stv[3]), "+v"(stv[4]) :: "memory");
  f32x4 cs[4];
#pragma unroll
  for (int j = 0; j < 4; ++j) cs[j] = *(const LAS f32x4*)(sm + L_CS + w * 256 + j * 64 + g * 16);
  {
    const u32x2 nv = *(const LAS u32x2*)(sm + L_NONCE);
    for (unsigned spins = 0; spins < SPIN_MAX; ++spins) {
      bool ok = (ackv[0] == nv[0]) && (ackv[1] == nv[1]);
#pragma unroll
      for (int m = 0; m < MT; ++m) ok = ok && (stv[m][2] == ackv[2]) && (stv[m][3] == ackv[3]);
      if (all_ok(ok)) break;
      __builtin_amdgcn_s_sleep(2);
#pragma unroll
      for (int m = 0; m < MT; ++m) asm volatile("global_load_dwordx4 %0, %1, off sc1" : "=&v"(stv[m]) : "v"(STP(m)) : "memory");
      ackv = ld128_sc1(ackp);
#pragma unroll
      for (int m = 0; m < MT; ++m) asm volatile("" : "+v"(stv[m]) :: "memory");
    }
  }
  const u32x2 ep = u32x2{ackv[2], ackv[3]};
  if (w == 0 && lane == 0) st64_sc1(ws + WS_DONE + bid * 64, ep);
  if (GU) {
    const unsigned vB = p2_vB(w, lane);
    p2_issueB_down(srd_wd, vB, c0p2, w, smb, 0, rot);
    p2_issueB_down(srd_wd, vB, c0p2, w, smb, 1, rot);
    p2_issueB_down(srd_wd, vB, c0p2, w, smb, 2, rot);
  }
  float rstd[MT];
#pragma unroll
  for (int m = 0; m < MT; ++m) {
    const f32x2 nm = sp2(opq(-u2f(stv[m][0])));
    rstd[m] = opq(u2f(stv[m][1]));
#pragma unroll
    for (int j = 0; j < 4; ++j) {
      f32x2 l = fma2(nm, lo2(cs[j]), lo2(acc[m][j])), h = fma2(nm, hi2(cs[j]), hi2(acc[m][j]));
      acc[m][j] = f32x4{l[0], l[1], h[0], h[1]};
    }
  }

  if (GU) {
    const float K0 = -2.0f * 0.7978845608028654f * 1.4426950408889634f;
    const float K1c = K0 * 0.044715f;
    ldsp stg = sm + w * 7680;
    char* hg = ws + WS_H + ct * 256 + wn * 64 + (lane & 3) * 16;
#pragma unroll
    for (int m = 0; m <= MT; ++m) {
      if (m < MT) {
        const float r = rstd[m], r2 = r * r;
        const f32x2 c0 = sp2(opq(K0 * r)), c1 = sp2(opq(K1c * r2 * r)), ir2 = sp2(opq(__builtin_amdgcn_rcpf(r2)));
        u32x2 hp[2];
#pragma unroll
        for (int j = 0; j < 2; ++j) {
#pragma unroll
          for (int q = 0; q < 2; ++q) {
            const f32x2 a = q ? hi2(acc[m][j]) : lo2(acc[m][j]), b = q ? hi2(acc[m][j + 2]) : lo2(acc[m][j + 2]);
            const f32x2 z = a * fma2(a * a, c1, c0);
            const f32x2 e = f32x2{__builtin_amdgcn_exp2f(z[0]), __builtin_amdgcn_exp2f(z[1])};
            const f32x2 d = fma2(e, ir2, ir2);
            const f32x2 hv = (a * b) * f32x2{__builtin_amdgcn_rcpf(d[0]), __builtin_amdgcn_rcpf(d[1])};
            hp[j][q] = pk2(hv[0], hv[1]);
          }
        }
        *(LAS u32x2*)(stg + (m * 16 + fr) * 80 + g * 8) = hp[0];
        *(LAS u32x2*)(stg + (m * 16 + fr) * 80 + 32 + g * 8) = hp[1];
      }
      if (m >= 1) {
        const int rl = (m - 1) * 16 + (lane >> 2);
        u32x4 v = *(const LAS u32x4*)(stg + rl * 80 + (lane & 3) * 16);
        const int sl = mh * 96 + rl;
        const int ri = sl < 95 ? sl : sl - 1;
        if (sl != 95 && ri < nrow) st128_sc1(hg + (size_t)(r0 + ri) * 3072, v);
      }
    }
    WAITN_BAR(0);
    if (w == 0 && lane == 0) st64_sc1(ws + WS_HF + rg * 128 + ct * 8, ep);
  } else {
    const int head = ct * 4 + wn;
    bf16x8 wq[2], wk[2];
#pragma unroll
    for (int ft = 0; ft < 2; ++ft) {
      wq[ft] = asb(u32x4{wpk[4 * ft], wpk[4 * ft + 1], g == 0 ? wpk[4 * ft + 2] : 0u, g == 0 ? wpk[4 * ft + 3] : 0u});
      wk[ft] = asb(u32x4{wpk[4 * ft], wpk[4 * ft + 1], g == 1 ? wpk[4 * ft + 2] : 0u, g == 1 ? wpk[4 * ft + 3] : 0u});
    }
    const unsigned tag = (ep[0] ^ (ep[1] * 0x9E3779B1u)) & 0xBFFFBFFFu;
    const float L2E = 1.4426950408889634f;
    const f32x4 z4 = f32x4{0.f, 0.f, 0.f, 0.f};
    bool live[6];
    u32x2 rem[6];
#pragma unroll
    for (int m = 0; m < 6; ++m) {
      int s = mh * 96 + m * 16 + fr;
      int ri = s < 95 ? s : s - 1;
      live[m] = (m < MT) && (s != 95) && (ri < nrow);
      if (m < MT) rem[m] = u32x2{pk2(acc[m][3][0], acc[m][3][1]), pk2(acc[m][3][2], acc[m][3][3])};
    }
    ldsp pk_img = sm + w * 17408;
    ldsp v_img = pk_img + 96 * 104;
    {
      unsigned kf[6][5];
      u32x2 vv[6], ve[6];
#pragma unroll
      for (int mp = 0; mp < 3; ++mp) {
        f32x2 y01[2], y23[2], y4;
#pragma unroll
        for (int hh = 0; hh < 2; ++hh) {
          const int m = 2 * mp + hh;
          if (m < MT) {
            const f32x2 r = sp2(rstd[m]);
            const float c = opq(rstd[m] * L2E);
            u32x4 kb = u32x4{pk2(acc[m][1][0], acc[m][1][1]), pk2(acc[m][1][2], acc[m][1][3]), rem[m][0], rem[m][1]};
            f32x4 k0 = mfma32(wk[0], asb(kb), z4);
            f32x4 k1 = mfma32(wk[1], asb(kb), z4);
            y01[hh] = lo2(k0) * sp2(c); y23[hh] = hi2(k0) * sp2(c); y4[hh] = k1[0] * c;
            vv[m] = u32x2{pk2v(lo2(acc[m][2]) * r), pk2v(hi2(acc[m][2]) * r)};
            u32x2 vr = u32x2{pk2v(lo2(acc[m][3]) * r), pk2v(hi2(acc[m][3]) * r)};
            ve[m] = (g == 2) ? vr : u32x2{0x3F803F80u, 0x3F803F80u};
          } else {
            y01[hh] = sp2(0.f); y23[hh] = sp2(0.f); y4[hh] = 0.f;
            vv[m] = u32x2{0u, 0u}; ve[m] = vv[m];
          }
        }
        featmap2(y01[0], y23[0], y01[1], y23[1], y4, live[2 * mp], live[2 * mp + 1], g, kf[2 * mp], kf[2 * mp + 1]);
      }
      SCHED0();
#pragma unroll
      for (int m = 0; m < 6; ++m) {
        int tok = m * 16 + fr;
        *(LAS u32x2*)(pk_img + tok * 104 + g * 8) = u32x2{kf[m][0], kf[m][1]};
        *(LAS u32x2*)(pk_img + tok * 104 + 32 + g * 8) = u32x2{kf[m][2], kf[m][3]};
        *(LAS u32x2*)(pk_img + tok * 104 + 64 + g * 8) = u32x2{kf[m][4], 0u};
        *(LAS u32x2*)(v_img + tok * 72 + g * 8) = vv[m];
        *(LAS u32x2*)(v_img + tok * 72 + 32 + (g ^ 2) * 8) = ve[m];
      }
      LGKM0();
      SCHED0();
    }
    f32x4 kv[3][2];
#pragma unroll
    for (int ft = 0; ft < 3; ++ft)
#pragma unroll
      for (int dt = 0; dt < 2; ++dt) kv[ft][dt] = z4;
    {
      const int q = fr >> 2, p = lane & 3;
#pragma unroll
      for (int s = 0; s < 3; ++s) {
        int row = 32 * s + 8 * g + q;
        bf16x8 a[3], b[2];
#pragma unroll
        for (int ft = 0; ft < 3; ++ft) a[ft] = tr2(pk_img + row * 104 + ft * 32 + p * 8, pk_img + (row + 4) * 104 + ft * 32 + p * 8);
#pragma unroll
        for (int dt = 0; dt < 2; ++dt) b[dt] = tr2(v_img + row * 72 + dt * 32 + p * 8, v_img + (row + 4) * 72 + dt * 32 + p * 8);
#pragma unroll
        for (int ft = 0; ft < 3; ++ft)
#pragma unroll
          for (int dt = 0; dt < 2; ++dt) kv[ft][dt] = mfma32(a[ft], b[dt], kv[ft][dt]);
      }
    }
    {
      f32x4 o5[5] = {kv[0][0], kv[1][0], kv[0][1], kv[1][1], f32x4{kv[2][0][0], kv[2][0][1], kv[2][1][0], kv[2][1][1]}};
      ldsp ex = sm + (4 + wn) * 17408;
      if (mh == 1) {
        LGKM0();
#pragma unroll
        for (int i = 0; i < 5; ++i) *(LAS f32x4*)(ex + i * 1024 + lane * 16) = o5[i];
      }
      LGKM_BAR();
      if (mh == 0) {
        unsigned pp[10];
#pragma unroll
        for (int i = 0; i < 5; ++i) {
          f32x4 e = *(const LAS f32x4*)(ex + i * 1024 + lane * 16);
          pp[2 * i] = pk2(o5[i][0] + e[0], o5[i][1] + e[1]);
          pp[2 * i + 1] = pk2(o5[i][2] + e[2], o5[i][3] + e[3]);
        }
        char* kp = ws + WS_KV + (size_t)(rg * NH + head) * 4096 + lane * 16;
        st128_sc1(kp, u32x4{pp[0], pp[1], pp[2], tag});
        st128_sc1(kp + 1024, u32x4{pp[3], pp[4], pp[5], tag});
        st128_sc1(kp + 2048, u32x4{pp[6], pp[7], pp[8], tag});
        st128_sc1(kp + 3072, u32x4{pp[9], 0u, 0u, tag});
      }
    }
    unsigned qf[6][5];
#pragma unroll
    for (int mp = 0; mp < 3; ++mp) {
      f32x2 y01[2], y23[2], y4;
#pragma unroll
      for (int hh = 0; hh < 2; ++hh) {
        const int m = 2 * mp + hh;
        if (m < MT) {
          const float c = opq(rstd[m] * L2E);
          u32x4 qb = u32x4{pk2(acc[m][0][0], acc[m][0][1]), pk2(acc[m][0][2], acc[m][0][3]), rem[m][0], rem[m][1]};
          f32x4 q0 = mfma32(wq[0], asb(qb), z4);
          f32x4 q1 = mfma32(wq[1], asb(qb), z4);
          y01[hh] = lo2(q0) * sp2(c); y23[hh] = hi2(q0) * sp2(c); y4[hh] = q1[0] * c;
        } else {
          y01[hh] = sp2(0.f); y23[hh] = sp2(0.f); y4[hh] = 0.f;
        }
      }
      featmap2(y01[0], y23[0], y01[1], y23[1], y4, true, true, g, qf[2 * mp], qf[2 * mp + 1]);
    }
    float ks[20];
    {
      {
        const char* p1 = ws + WS_KV + ((size_t)(mh * 6 + (lane < 6 ? lane : 0)) * NH + head) * 4096 + 3072 + 1008;
        for (unsigned spins = 0; spins < SPIN_MAX; ++spins) {
          u32x4 v = ld128_sc1(p1);
          if (all_ok(v[3] == tag)) break;
          __builtin_amdgcn_s_sleep(POLLS);
        }
      }
      u32x4 pv[6][4];
      const char* kp = ws + WS_KV + (size_t)((mh * 6) * NH + head) * 4096 + lane * 16;
      for (unsigned spins = 0; spins < SPIN_MAX; ++spins) {
#pragma unroll
        for (int c = 0; c < 6; ++c) {
          const char* p = kp + (size_t)c * NH * 4096;
          asm volatile("global_load_dwordx4 %0, %4, off sc1\n\tglobal_load_dwordx4 %1, %4, off offset:1024 sc1\n\t"
                       "global_load_dwordx4 %2, %4, off offset:2048 sc1\n\tglobal_load_dwordx4 %3, %4, off offset:3072 sc1"
                       : "=&v"(pv[c][0]), "=&v"(pv[c][1]), "=&v"(pv[c][2]), "=&v"(pv[c][3]) : "v"(p) : "memory");
        }
        asm volatile("s_waitcnt vmcnt(0)" : "+v"(pv[0][0]), "+v"(pv[0][1]), "+v"(pv[0][2]), "+v"(pv[0][3]), "+v"(pv[1][0]), "+v"(pv[1][1]), "+v"(pv[1][2]), "+v"(pv[1][3]),
                     "+v"(pv[2][0]), "+v"(pv[2][1]), "+v"(pv[2][2]), "+v"(pv[2][3]), "+v"(pv[3][0]), "+v"(pv[3][1]), "+v"(pv[3][2]), "+v"(pv[3][3]),
                     "+v"(pv[4][0]), "+v"(pv[4][1]), "+v"(pv[4][2]), "+v"(pv[4][3]), "+v"(pv[5][0]), "+v"(pv[5][1]), "+v"(pv[5][2]), "+v"(pv[5][3]) :: "memory");
        bool ok = true;
#pragma unroll
        for (int c = 0; c < 6; ++c)
#pragma unroll
          for (int i = 0; i < 4; ++i) ok = ok && (pv[c][i][3] == tag);
        if (all_ok(ok)) break;
        __builtin_amdgcn_s_sleep(2);
      }
      f32x4 kt[5];
      {
        const bool dg = (g == (fr >> 2));
        const int r = fr & 3;
        bf16x8 sel[4];
#pragma unroll
        for (int q = 0; q < 4; ++q) {
          const int j = q == 0 ? r : (q == 1 ? (r < 2 ? 4 + r : 8) : (q == 2 ? (r >= 2 ? r - 2 : 8) : 2 + r));
          const unsigned one = dg ? ((j & 1) ? 0x3F800000u : 0x00003F80u) : 0u;
          sel[q] = asb(u32x4{(j >> 1) == 0 ? one : 0u, (j >> 1) == 1 ? one : 0u, (j >> 1) == 2 ? one : 0u, 0u});
        }
#pragma unroll
        for (int t = 0; t < 5; ++t) kt[t] = z4;
#pragma unroll
        for (int c = 0; c < 6; ++c) {
          kt[0] = mfma32(sel[0], asb(pv[c][0]), kt[0]);
          kt[1] = mfma32(sel[1], asb(pv[c][0]), kt[1]);
          kt[2] = mfma32(sel[3], asb(pv[c][1]), kt[2]);
          kt[3] = mfma32(sel[0], asb(pv[c][2]), kt[3]);
          kt[4] = mfma32(sel[1], asb(pv[c][2]), kt[4]);
          kt[1] = mfma32(sel[2], asb(pv[c][1]), kt[1]);
          kt[4] = mfma32(sel[2], asb(pv[c][3]), kt[4]);
        }
      }
      ldsp mine = sm + w * 17408 + 12288, other = sm + (w ^ 4) * 17408 + 12288;
      SCHED0();
#pragma unroll
      for (int i = 0; i < 5; ++i) *(LAS f32x4*)(mine + i * 1024 + lane * 16) = kt[i];
      LGKM_BAR();
#pragma unroll
      for (int i = 0; i < 5; ++i) {
        const f32x4 e = kt[i] + *(const LAS f32x4*)(other + i * 1024 + lane * 16);
#pragma unroll
        for (int q = 0; q < 4; ++q) ks[4 * i + q] = e[q];
      }
    }
    const bf16x8 ka0 = asb(u32x4{pk2(ks[0], ks[1]), pk2(ks[2], ks[3]), pk2(ks[4], ks[5]), pk2(ks[6], ks[7])});
    const bf16x8 ka1 = asb(u32x4{pk2(ks[8], ks[9]), pk2(ks[10], ks[11]), pk2(ks[12], ks[13]), pk2(ks[14], ks[15])});
    const bf16x8 kb0 = asb(u32x4{pk2(ks[16], ks[17]), 0u, 0u, 0u});
    const bf16x8 kb1 = asb(u32x4{pk2(ks[18], ks[19]), 0u, 0u, 0u});
    {
      u32x2 om_[MT], or_[MT];
#pragma unroll
      for (int m = 0; m < MT; ++m) {
        bf16x8 b0 = asb(u32x4{qf[m][0], qf[m][1], qf[m][2], qf[m][3]});
        bf16x8 b1 = asb(u32x4{qf[m][4], 0u, 0u, 0u});
        f32x4 n0 = mfma32(kb0, b1, mfma32(ka0, b0, z4));
        f32x4 n1 = mfma32(kb1, b1, mfma32(ka1, b0, z4));
        float da = n1[0], db = n1[0];
        swap16(da, db);
        const f32x2 inv = sp2(__builtin_amdgcn_rcpf(db + 1e-6f));
        om_[m] = u32x2{pk2v(lo2(n0) * inv), pk2v(hi2(n0) * inv)};
        or_[m] = u32x2{pk2v(lo2(n1) * inv), pk2v(hi2(n1) * inv)};
      }
      SCHED0();
      ldsp stg = sm + w * 17408 + 8192;
#pragma unroll
      for (int m = 0; m < MT; ++m) {
        *(LAS u32x2*)(stg + (m * 16 + fr) * 32 + g * 8) = om_[m];
        if (g == 0) *(LAS u32x2*)(stg + 3072 + (m * 16 + fr) * 8) = or_[m];
      }
      LGKM0();
      const int tok0 = r0 + mh * 95;
      const int ntok = mh == 0 ? 95 : nrow - 95;
      char* om = ws + WS_OM + ((size_t)head * S_ + tok0) * 32;
      char* orr = ws + WS_OR + ((size_t)head * S_ + tok0) * 8;
      u32x4 tv[3];
      u32x2 tr[2];
#pragma unroll
      for (int i = 0; i < 3; ++i) tv[i] = *(const LAS u32x4*)(stg + i * 1024 + lane * 16);
#pragma unroll
      for (int i = 0; i < 2; ++i) tr[i] = *(const LAS u32x2*)(stg + 3072 + ((i * 64 + lane) < 96 ? (i * 64 + lane) : 0) * 8);
#pragma unroll
      for (int i = 0; i < 3; ++i)
        if (i * 32 + (lane >> 1) < ntok) st128_sc1(om + i * 1024 + lane * 16, tv[i]);
#pragma unroll
      for (int i = 0; i < 2; ++i)
        if (i * 64 + lane < ntok) st64_sc1(orr + (i * 64 + lane) * 8, tr[i]);
    }
    WAITN_BAR(0);
    if (w == 0 && lane == 0) st64_sc1(ws + WS_OF + rg * 128 + ct * 8, ep);
  }
  return ep;
}

DI u32x2 service(ldsp sm, const char* x, char* ws, int sid, int w, int lane) {
  const int bid = __builtin_amdgcn_workgroup_id_x();
  const int g = lane >> 4, fr = lane & 15;
  if (w == 0) {
    unsigned long long nn = __builtin_amdgcn_s_memrealtime();
    u32x2 nv = u32x2{(unsigned)nn, (unsigned)(nn >> 32)};
    if (lane == 0) {
      if (sid != 15) st64_sc1(ws + WS_REQ + bid * 64, nv);
      *(LAS u32x2*)(sm + L_NONCE) = nv;
    }
  }
  LGKM_BAR();
  const u32x2 nv = *(const LAS u32x2*)(sm + L_NONCE);
  u32x2 ep = u32x2{nv[0] ^ 0x5bd1e995u, nv[1] ^ 0x80000000u};
  if (sid == 15 && w < 4) {
    const int cb = w * 64 + lane;
    const bool me = (cb == bid);
    u32x2 lastq = u32x2{0u, 0u};
    for (unsigned spins = 0; spins < SPIN_MAX; ++spins) {
      u32x2 rq, dn;
      asm volatile("global_load_dwordx2 %0, %2, off sc1\n\tglobal_load_dwordx2 %1, %3, off sc1\n\ts_waitcnt vmcnt(0)"
                   : "=&v"(rq), "=&v"(dn) : "v"(ws + WS_REQ + cb * 64), "v"(ws + WS_DONE + cb * 64) : "memory");
      if (all_ok(me || (dn[0] == ep[0] && dn[1] == ep[1]))) break;
      if (spins == 0 || rq[0] != lastq[0] || rq[1] != lastq[1]) st128_sc1(ws + WS_ACK + cb * 64, u32x4{rq[0], rq[1], ep[0], ep[1]});
      lastq = rq;
      if (spins > 12) __builtin_amdgcn_s_sleep(16);
    }
  } else {
    const int npass = sid == 15 ? 2 : 1;
    bool have = (sid == 15);
#pragma unroll 1
    for (int ps = 0; ps < npass; ++ps) {
      const int rbase = sid * 128 + (sid == 15 ? (w - 4) * 32 + ps * 16 : w * 16) + g;
      u32x4 v[4][5];
#pragma unroll
      for (int i = 0; i < 4; ++i)
#pragma unroll
        for (int c = 0; c < 5; ++c) v[i][c] = *(const u32x4*)(x + (size_t)(rbase + 4 * i) * 1280 + fr * 16 + c * 256);
      float mean[4], rstd[4];
#pragma unroll
      for (int i = 0; i < 4; ++i) {
        f32x2 a = sp2(0.f);
#pragma unroll
        for (int c = 0; c < 5; ++c)
#pragma unroll
          for (int e = 0; e < 4; ++e) a += f32x2{u2f(v[i][c][e] << 16), u2f(v[i][c][e] & 0xffff0000u)};
        float s = a[0] + a[1];
        s += dpp<0xB1>(s); s += dpp<0x4E>(s); s += dpp<0x141>(s); s += dpp<0x140>(s);
        mean[i] = s * (1.0f / 640.0f);
        const f32x2 m2 = sp2(opq(mean[i]));
        f32x2 b = sp2(0.f);
#pragma unroll
        for (int c = 0; c < 5; ++c)
#pragma unroll
          for (int e = 0; e < 4; ++e) {
            const f32x2 d = f32x2{u2f(v[i][c][e] << 16), u2f(v[i][c][e] & 0xffff0000u)} - m2;
            b = fma2(d, d, b);
          }
        float t = b[0] + b[1];
        t += dpp<0xB1>(t); t += dpp<0x4E>(t); t += dpp<0x141>(t); t += dpp<0x140>(t);
        rstd[i] = __builtin_amdgcn_rsqf(t * (1.0f / 640.0f) + 1e-5f);
      }
      if (!have) {
        u32x4 ack;
        for (unsigned spins = 0; spins < SPIN_MAX; ++spins) {
          ack = ld128_sc1(ws + WS_ACK + bid * 64);
          if (all_ok(ack[0] == nv[0] && ack[1] == nv[1])) break;
          __builtin_amdgcn_s_sleep(2);
        }
        ep = u32x2{ack[2], ack[3]};
        have = true;
        if (w == 0 && lane == 0) st64_sc1(ws + WS_DONE + bid * 64, ep);
      }
#pragma unroll
      for (int i = 0; i < 4; ++i)
        if (fr == 0) st128_sc1(ws + WS_STAT + (size_t)(rbase + 4 * i) * 16, u32x4{f2u(mean[i]), f2u(rstd[i]), ep[0], ep[1]});
    }
  }
  return ep;
}

__global__ __launch_bounds__(512, 2) void fused(const char* __restrict__ x, const char* __restrict__ wqkv,
                                                const char* __restrict__ wphi, const char* __restrict__ wo,
                                                const char* __restrict__ wg, const char* __restrict__ wu,
                                                const char* __restrict__ wd, char* __restrict__ ws, char* __restrict__ out) {
  extern __shared__ __attribute__((aligned(16))) char smem[];
  ldsp sm = (ldsp)smem;
  const int tid = __builtin_amdgcn_workitem_id_x();
  const int bp = __builtin_amdgcn_workgroup_id_x(), xcd = bp & 7, ip = bp >> 3;
  const int idx = ip < 2 ? 30 + ip : ip - 2;
  const int b = idx * 8 + xcd;
  const int w = __builtin_amdgcn_readfirstlane(tid >> 6), lane = tid & 63;
  const u32x4 srd_wd = make_srd(wd, DF_ * D_ * 2);
  const int rgl = idx / 5, c5 = idx % 5;
  const int rg = (xcd >> 2) * 6 + rgl, cq = xcd & 3;
  if (idx < 30 && c5 >= 3) {
    const int ct = cq * 2 + (c5 - 3);
    if (w < 4) phase1<6, false>(sm, x, wqkv, wphi, wg, wu, srd_wd, ws, rg, ct, 0, 0, w, lane);
    else phase1<5, false>(sm, x, wqkv, wphi, wg, wu, srd_wd, ws, rg, ct, 0, 0, w, lane);
    return;
  }
  const int p2i = idx < 30 ? rgl * 3 + c5 : 18 + (idx - 30);
  const int rt = (xcd >> 1) * 4 + p2i / 5, ct2 = (xcd & 1) * 5 + p2i % 5;
#define ROTF ((((p2i % 5) * 12) / 5 + (p2i / 5) * 3) % 12)
  const int rot = ROTF;
  u32x2 ep;
  if (idx < 30) {
    const int ct = cq * 3 + c5;
    if (w < 4) ep = phase1<6, true>(sm, x, wqkv, wphi, wg, wu, srd_wd, ws, rg, ct, ct2 * 64, rot, w, lane);
    else ep = phase1<5, true>(sm, x, wqkv, wphi, wg, wu, srd_wd, ws, rg, ct, ct2 * 64, rot, w, lane);
  } else {
    ep = service(sm, x, ws, b - 240, w, lane);
    {
      const u32x4 srd_wo = make_srd(wo, D_ * D_ * 2);
      const unsigned vp = (lane >> 3) * 1280 + (lane & 7) * 16 + (xcd & 1) * 640;
      const unsigned dst = (unsigned)(size_t)sm + w * 1024;
      const int w16 = (idx - 30) * 8 + w;
      if (!(b == 255 && w < 4)) {
#pragma unroll 1
        for (int p = w16; p < 192; p += 16) {
#pragma unroll
          for (int c = 0; c < 5; ++c) dma_b(srd_wd, vp, p * 8 * 1280 + c * 128, dst);
          asm volatile("s_waitcnt vmcnt(30)" ::: "memory");
        }
#pragma unroll 1
        for (int p = w16; p < 80; p += 16) {
#pragma unroll
          for (int c = 0; c < 5; ++c) dma_b(srd_wo, vp, p * 8 * 1280 + c * 128, dst);
          asm volatile("s_waitcnt vmcnt(30)" ::: "memory");
        }
      }
      WAITN_BAR(0);
    }
    const unsigned vB = p2_vB(w, lane);
    const unsigned smb = (unsigned)(size_t)sm;
    p2_issueB_down(srd_wd, vB, ct2 * 64, w, smb, 0, rot);
    p2_issueB_down(srd_wd, vB, ct2 * 64, w, smb, 1, rot);
    p2_issueB_down(srd_wd, vB, ct2 * 64, w, smb, 2, rot);
  }
  phase2(sm, x, wo, srd_wd, ws, out, rt, ct2, rot, w, lane, ep);
}


extern "C" void kernel_launch(void* const* d_in, const int* in_sizes, int n_in, void* d_out, int out_size, void* d_ws,
                              size_t ws_size, hipStream_t stream) {
  (void)hipFuncSetAttribute((const void*)fused, hipFuncAttributeMaxDynamicSharedMemorySize, K_LDS);
  fused<<<256, 512, K_LDS, stream>>>((const char*)d_in[0], (const char*)d_in[1], (const char*)d_in[2], (const char*)d_in[3],
                                     (const char*)d_in[5], (const char*)d_in[6], (const char*)d_in[7], (char*)d_ws, (char*)d_out);
}
```
